```python
import torch

def _softmax(x, axis=-1):
    return torch.softmax(x, dim=axis)
def _gelu(x):
    return torch.nn.functional.gelu(x, approximate="tanh")
def _silu(x):
    return torch.nn.functional.silu(x)
def _sigmoid(x):
    return torch.sigmoid(x)
def _layernorm(x, axis=-1, eps=1e-5):
    m = x.mean(dim=axis, keepdim=True)
    v = x.var(dim=axis, keepdim=True, unbiased=False)
    return (x - m) / torch.sqrt(v + eps)
def _scatter_add(tgt, idx, src, axis=0):
    return tgt.clone().index_add_(axis, idx, src)
def _segment_sum(src, seg, n):
    out = torch.zeros((n,) + src.shape[1:], dtype=src.dtype)
    return out.index_add_(0, seg, src)
def _pad(x, pw):
    flat = tuple(v for pair in reversed(pw) for v in pair)
    return torch.nn.functional.pad(x, flat)

kernel_name = 'sequential_preLN__spectral_fnet__glu_gated_glu__prompt'

def setup_inputs():

    _w = (lambda a: (lambda t: t.to(torch.bfloat16) if t.is_floating_point() else t)(torch.as_tensor(a)))
    def _A_setup():
        B, S, D = 1, 2048, 640
        ns = torch.arange(S)[:, None] * torch.arange(S)[None, :]
        nd = torch.arange(D)[:, None] * torch.arange(D)[None, :]
        Ws_re = torch.cos(-2.0 * torch.pi * ns / S).to(float)
        Ws_im = torch.sin(-2.0 * torch.pi * ns / S).to(float)
        Wd_re = torch.cos(-2.0 * torch.pi * nd / D).to(float)
        Wd_im = torch.sin(-2.0 * torch.pi * nd / D).to(float)
        return {
            "x": _w((lambda *s: torch.randn(*s, dtype=torch.bfloat16) * 0.05)(B, S, D)),
            "dft_seq_re": _w(Ws_re),
            "dft_seq_im": _w(Ws_im),
            "dft_hid_re": _w(Wd_re),
            "dft_hid_im": _w(Wd_im),
        }
    _A_in = _A_setup()

    def _B_setup():
        B, S, D, F = 1, 2048, 640, 1728
        return {
            "x": _w((lambda *s: torch.randn(*s, dtype=torch.bfloat16) * 0.05)(B, S, D)),
            "w_gate": _w((lambda *s: torch.randn(*s, dtype=torch.bfloat16) * 1.6)(D, F)),
            "w_up": _w((lambda *s: torch.randn(*s, dtype=torch.bfloat16) * 0.8)(D, F)),
            "w_down": _w((lambda *s: torch.randn(*s, dtype=torch.bfloat16) * 0.8)(F, D)),
        }
    _B_in = _B_setup()

    return {
        "A_x": _A_in["x"],
        "A_dft_seq_re": _A_in["dft_seq_re"],
        "A_dft_seq_im": _A_in["dft_seq_im"],
        "A_dft_hid_re": _A_in["dft_hid_re"],
        "A_dft_hid_im": _A_in["dft_hid_im"],
        "B_x": _B_in["x"],
        "B_w_gate": _B_in["w_gate"],
        "B_w_up": _B_in["w_up"],
        "B_w_down": _B_in["w_down"],
    }

def reference(A_x, A_dft_seq_re, A_dft_seq_im, A_dft_hid_re, A_dft_hid_im, B_x, B_w_gate, B_w_up, B_w_down):
    def _A_ref(x, dft_seq_re, dft_seq_im, dft_hid_re, dft_hid_im):
        a = torch.einsum("bsd,ed->bse", x, dft_hid_re)
        b = torch.einsum("bsd,ed->bse", x, dft_hid_im)
        y = torch.einsum("ts,bse->bte", dft_seq_re, a) - torch.einsum("ts,bse->bte", dft_seq_im, b)
        return y

    def _B_ref(x, w_gate, w_up, w_down):
        g = torch.einsum("bsd,df->bsf", x, w_gate)
        u = torch.einsum("bsd,df->bsf", x, w_up)
        h = _sigmoid(g) * u
        return torch.einsum("bsf,fd->bsd", h, w_down)

    x = A_x
    h = x + _A_ref(_layernorm(x, axis=-1), A_dft_seq_re, A_dft_seq_im, A_dft_hid_re, A_dft_hid_im)
    h2 = h + _B_ref(_layernorm(h, axis=-1), B_w_gate, B_w_up, B_w_down)
    return h2
```

```cpp
#include <hip/hip_runtime.h>
#include <hip/hip_bf16.h>

typedef unsigned short u16;
typedef short bf16x8 __attribute__((ext_vector_type(8)));
typedef short bf16x4 __attribute__((ext_vector_type(4)));
typedef float f32x4 __attribute__((ext_vector_type(4)));
typedef float f32x2 __attribute__((ext_vector_type(2)));
typedef unsigned u32x2 __attribute__((ext_vector_type(2)));
typedef unsigned u32x4 __attribute__((ext_vector_type(4)));
#define LAS __attribute__((address_space(3)))
#define GAS __attribute__((address_space(1)))

constexpr int S_ = 2048, D_ = 640, F_ = 1728;

__device__ __forceinline__ float bf2f(u16 v) { return __uint_as_float(((unsigned)v) << 16); }
__device__ __forceinline__ unsigned pk2(float lo, float hi) {
  unsigned r; asm volatile("v_cvt_pk_bf16_f32 %0, %1, %2" : "=v"(r) : "v"(lo), "v"(hi)); return r;
}
__device__ __forceinline__ float rbf(float v) { return __uint_as_float(pk2(v, v) << 16); }

template <int N> __device__ __forceinline__ void wait_vm() { asm volatile("s_waitcnt vmcnt(%0)" ::"n"(N) : "memory"); }
__device__ __forceinline__ void wait_lgkm0() { asm volatile("s_waitcnt lgkmcnt(0)" ::: "memory"); }
__device__ __forceinline__ void bar() { asm volatile("" ::: "memory"); __builtin_amdgcn_s_barrier(); asm volatile("" ::: "memory"); }

__device__ __forceinline__ void glds16(const char* g, LAS char* l) {
  __builtin_amdgcn_global_load_lds((const GAS void*)g, (LAS void*)l, 16, 0, 0);
}

template <int BM_, int BN_, int WM_, int WN_, int WK_, int NS_, int NK_, int AMODE_, int BMODE_>
struct Cfg {
  static constexpr int BM = BM_, BN = BN_, WM = WM_, WN = WN_, WK = WK_, NS = NS_, NK = NK_, AMODE = AMODE_, BMODE = BMODE_;
  static constexpr int NW = WM * WN * WK, NT = NW * 64;
  static constexpr int MF = BM / WM / 16, NF = BN / WN / 16, KSW = 2 / WK;
  static constexpr int ABYTES = BM * 128, BBYTES = BN * 128, STAGE = ABYTES + BBYTES;
  static constexpr int APW = ABYTES / NW, BPW = BBYTES / NW;
  static constexpr int GA = (APW + 1023) / 1024, GB = (BPW + 1023) / 1024, G = GA + GB;
  static constexpr int LDS = NS * STAGE;
  static_assert(APW % 512 == 0 && BPW % 512 == 0, "pieces");
  static_assert((NS - 1) * G <= 63, "vmcnt");
  static_assert(NS <= NK, "stages");
};

template <class C> struct Frags { bf16x8 a[C::KSW][C::MF]; bf16x8 b[C::KSW][C::NF]; };

template <class C, class Src>
struct Loop {
  LAS char* lds; const Src& src;
  int wid, lane, wm, wn, wk;
  unsigned voffA[C::GA], voffB[C::GB];
  unsigned rdA[2], rdB[2];

  __device__ __forceinline__ Loop(LAS char* lds_, const Src& s) : lds(lds_), src(s) {
    const int tid = threadIdx.x;
    wid = __builtin_amdgcn_readfirstlane(tid >> 6); lane = tid & 63;
    wk = wid % C::WK; wn = (wid / C::WK) % C::WN; wm = wid / (C::WK * C::WN);
    const int fr = lane & 15, fq = lane >> 4;
#pragma unroll
    for (int i = 0; i < C::GA; ++i) {
      const int p = (wid * C::APW + i * 1024) / 16 + lane;
      if constexpr (C::AMODE == 0) voffA[i] = p * 16;
      else { const int row = p >> 3, c = (p & 7) ^ ((row >> 1) & 7); voffA[i] = row * src.ldA + c * 16; }
    }
#pragma unroll
    for (int i = 0; i < C::GB; ++i) {
      int p = (wid * C::BPW + i * 1024) / 16 + lane;
      if (p >= C::BBYTES / 16) p = C::BBYTES / 16 - 1;
      if constexpr (C::BMODE == 0) voffB[i] = p * 16;
      else if constexpr (C::BMODE == 1) { const int row = p >> 3, c = (p & 7) ^ ((row >> 1) & 7); voffB[i] = row * src.ldB + c * 16; }
      else if constexpr (C::BMODE == 2) { const int kp = p / 10, c = p % 10, k = kp ^ (((kp >> 3) & 1) << 2); voffB[i] = k * src.ldB + c * 16; }
      else { const int q = p & 511, k = q >> 3, c = (q & 7) ^ ((((k >> 3) & 1) << 2) | (((k >> 1) & 1) << 1)); voffB[i] = k * src.ldB + c * 16; }
    }
    const int arow = wm * (C::BM / C::WM) + fr;
#pragma unroll
    for (int s = 0; s < 2; ++s) {
      const int ks = (C::WK == 2) ? wk : s;
      if constexpr (C::AMODE == 0) rdA[s] = ((ks * 4 + fq) * C::BM + arow) * 16;
      else rdA[s] = arow * 128 + (((ks * 4 + fq) ^ ((fr >> 1) & 7)) * 16);
    }
    if constexpr (C::BMODE <= 1) {
      const int brow = wn * (C::BN / C::WN) + fr;
#pragma unroll
      for (int s = 0; s < 2; ++s) {
        const int ks = (C::WK == 2) ? wk : s;
        if constexpr (C::BMODE == 0) rdB[s] = C::ABYTES + ((ks * 4 + fq) * C::BN + brow) * 16;
        else rdB[s] = C::ABYTES + brow * 128 + (((ks * 4 + fq) ^ ((fr >> 1) & 7)) * 16);
      }
    } else if constexpr (C::BMODE == 2) {
      const int g = lane >> 4, q = (lane >> 2) & 3, p = lane & 3;
      const int ks0 = (C::WK == 2) ? wk : 0;
#pragma unroll
      for (int t = 0; t < 2; ++t) rdB[t] = C::ABYTES + (32 * ks0 + 8 * g + 4 * (t ^ (g & 1)) + q) * 160 + p * 8 + wn * (C::BN / C::WN) * 2;
    } else {
      const int g = lane >> 4, q = (lane >> 2) & 3, p = lane & 3;
      const int ks0 = (C::WK == 2) ? wk : 0;
#pragma unroll
      for (int j = 0; j < 2; ++j) {
        const int jj = 2 * wn + j;
        rdB[j] = C::ABYTES + (32 * ks0 + 8 * g + q) * 128 + ((((jj ^ (((g & 1) << 1) | (q >> 1))) << 1) | (p >> 1)) * 16) + 8 * (p & 1);
      }
    }
  }

  __device__ __forceinline__ void issue(int k, unsigned buf) {
    const char* ga = src.baseA(k);
    LAS char* la = lds + buf + wid * C::APW;
#pragma unroll
    for (int i = 0; i < C::GA; ++i) {
      if ((i + 1) * 1024 <= C::APW) glds16(ga + voffA[i], la + i * 1024);
      else if (lane < 32) glds16(ga + voffA[i], la + i * 1024);
    }
    const char* gb = src.baseB(k, wid);
    LAS char* lb = lds + buf + C::ABYTES + wid * C::BPW;
#pragma unroll
    for (int i = 0; i < C::GB; ++i) {
      if ((i + 1) * 1024 <= C::BPW) glds16(gb + voffB[i], lb + i * 1024);
      else if (lane < 32) glds16(gb + voffB[i], lb + i * 1024);
    }
  }

  __device__ __forceinline__ void read(Frags<C>& f, unsigned buf) {
    LAS char* b = lds + buf;
#pragma unroll
    for (int s = 0; s < C::KSW; ++s) {
      if constexpr (C::BMODE <= 1) {
        constexpr int step = (C::BMODE == 0) ? 256 : 2048;
#pragma unroll
        for (int n = 0; n < C::NF; ++n) f.b[s][n] = *(const LAS bf16x8*)(b + rdB[s] + n * step);
      } else if constexpr (C::BMODE == 2) {
#pragma unroll
        for (int n = 0; n < C::NF; ++n) {
          bf16x4 lo = __builtin_amdgcn_ds_read_tr16_b64_v4i16((LAS bf16x4*)(b + rdB[0] + s * 5120 + n * 32));
          bf16x4 hi = __builtin_amdgcn_ds_read_tr16_b64_v4i16((LAS bf16x4*)(b + rdB[1] + s * 5120 + n * 32));
          f.b[s][n] = __builtin_shufflevector(lo, hi, 0, 1, 2, 3, 4, 5, 6, 7);
        }
      } else {
#pragma unroll
        for (int n = 0; n < C::NF; ++n) {
          const int j = n & 1, mat = n >> 1;
          bf16x4 lo = __builtin_amdgcn_ds_read_tr16_b64_v4i16((LAS bf16x4*)(b + rdB[j] + mat * 8192 + s * 4096));
          bf16x4 hi = __builtin_amdgcn_ds_read_tr16_b64_v4i16((LAS bf16x4*)(b + rdB[j] + mat * 8192 + s * 4096 + 512));
          f.b[s][n] = __builtin_shufflevector(lo, hi, 0, 1, 2, 3, 4, 5, 6, 7);
        }
      }
      constexpr int astep = (C::AMODE == 0) ? 256 : 2048;
#pragma unroll
      for (int m = 0; m < C::MF; ++m) f.a[s][m] = *(const LAS bf16x8*)(b + rdA[s] + m * astep);
    }
  }

  __device__ __forceinline__ void mma(f32x4 (&acc)[C::MF][C::NF], const Frags<C>& f) {
#pragma unroll
    for (int s = 0; s < C::KSW; ++s)
#pragma unroll
      for (int m = 0; m < C::MF; ++m)
#pragma unroll
        for (int n = 0; n < C::NF; ++n)
          acc[m][n] = __builtin_amdgcn_mfma_f32_16x16x32_bf16(f.b[s][n], f.a[s][m], acc[m][n], 0, 0, 0);
  }

  template <int K> __device__ __forceinline__ void tail(f32x4 (&acc)[C::MF][C::NF], Frags<C>& cur, Frags<C>& nxt) {
    if constexpr (K < C::NK) {
      if constexpr (K + 1 < C::NK) {
        constexpr int outst = (C::NS - 2 < C::NK - 2 - K) ? C::NS - 2 : C::NK - 2 - K;
        wait_vm<outst * C::G>();
        wait_lgkm0();
        bar();
        if constexpr (K + C::NS < C::NK) issue(K + C::NS, (K % C::NS) * C::STAGE);
        read(nxt, ((K + 1) % C::NS) * C::STAGE);
      }
      mma(acc, cur);
      tail<K + 1>(acc, nxt, cur);
    }
  }

  __device__ __forceinline__ void run(f32x4 (&acc)[C::MF][C::NF]) {
    Frags<C> f0, f1;
#pragma unroll
    for (int s = 0; s < C::NS; ++s) issue(s, s * C::STAGE);
    wait_vm<(C::NS - 1) * C::G>();
    bar();
    read(f0, 0);
    constexpr int NMAIN = ((C::NK - C::NS) / 2) * 2;
    unsigned cb = 0;
    for (int k = 0; k < NMAIN; k += 2) {
      unsigned nb = cb + C::STAGE; if (nb == C::LDS) nb = 0;
      wait_vm<(C::NS - 2) * C::G>(); wait_lgkm0(); bar();
      issue(k + C::NS, cb);
      read(f1, nb);
      mma(acc, f0);
      unsigned nb2 = nb + C::STAGE; if (nb2 == C::LDS) nb2 = 0;
      wait_vm<(C::NS - 2) * C::G>(); wait_lgkm0(); bar();
      issue(k + 1 + C::NS, nb);
      read(f0, nb2);
      mma(acc, f1);
      cb = nb2;
    }
    tail<NMAIN>(acc, f0, f1);
  }

  __device__ __forceinline__ void reduce_k(f32x4 (&acc)[C::MF][C::NF]) {
    static_assert(C::WK == 2 && C::MF == 2, "");
    wait_lgkm0(); bar();
    LAS f32x4* x = (LAS f32x4*)lds;
#pragma unroll
    for (int n = 0; n < C::NF; ++n) x[(wid * C::NF + n) * 64 + lane] = wk ? acc[0][n] : acc[1][n];
    wait_lgkm0(); bar();
#pragma unroll
    for (int n = 0; n < C::NF; ++n) {
      const f32x4 o = x[((wid ^ 1) * C::NF + n) * 64 + lane];
      acc[0][n] = (wk ? acc[1][n] : acc[0][n]) + o;
    }
  }
};

__global__ __launch_bounds__(512) void k0_ln(const u16* __restrict__ x, u16* __restrict__ xnI,
                                             const u16* __restrict__ wg, const u16* __restrict__ wu, float* __restrict__ cs) {
  __shared__ __attribute__((aligned(16))) char smem[8 * 1296 + 64];
  const int tid = threadIdx.x, wid = tid >> 6, lane = tid & 63;
  if (blockIdx.x < 256) {
    const int koct = blockIdx.x, row = koct * 8 + wid;
    const u16* xr = x + row * D_;
    bf16x8 v0 = *(const bf16x8*)(xr + lane * 8);
    bf16x8 v1 = {0, 0, 0, 0, 0, 0, 0, 0};
    if (lane < 16) v1 = *(const bf16x8*)(xr + 512 + lane * 8);
    float f[16];
#pragma unroll
    for (int i = 0; i < 8; ++i) { f[i] = bf2f((u16)v0[i]); f[8 + i] = bf2f((u16)v1[i]); }
    float s = 0.f;
#pragma unroll
    for (int i = 0; i < 16; ++i) s += f[i];
#pragma unroll
    for (int o = 32; o >= 1; o >>= 1) s += __shfl_xor(s, o);
    const float mean = s * (1.0f / D_);
    float q = 0.f;
#pragma unroll
    for (int i = 0; i < 8; ++i) { const float d = f[i] - mean; q += d * d; }
    if (lane < 16) {
#pragma unroll
      for (int i = 8; i < 16; ++i) { const float d = f[i] - mean; q += d * d; }
    }
#pragma unroll
    for (int o = 32; o >= 1; o >>= 1) q += __shfl_xor(q, o);
    const float mb = rbf(mean), vb = rbf(q * (1.0f / D_));
    const float den = rbf(sqrtf(rbf(vb + 1e-5f)));
    const float rden = 1.0f / den;
    u32x4 w0, w1;
#pragma unroll
    for (int i = 0; i < 4; ++i) {
      w0[i] = pk2(rbf(f[2 * i] - mb) * rden, rbf(f[2 * i + 1] - mb) * rden);
      w1[i] = pk2(rbf(f[8 + 2 * i] - mb) * rden, rbf(f[9 + 2 * i] - mb) * rden);
    }
    char* sr = smem + wid * 1296;
    *(u32x4*)(sr + lane * 16) = w0;
    if (lane < 16) *(u32x4*)(sr + 1024 + lane * 16) = w1;
    __syncthreads();
    for (int d = tid; d < D_; d += 512) {
      bf16x8 o;
#pragma unroll
      for (int r = 0; r < 8; ++r) o[r] = *(const short*)(smem + r * 1296 + d * 2);
      const int j = d / 80, n = d % 80;
      *(bf16x8*)(xnI + (((size_t)j * 256 + koct) * 80 + n) * 8) = o;
    }
  } else {
    const int b = blockIdx.x - 256, mat = b / 27, c0 = (b % 27) * 64;
    const u16* w = (mat ? wu : wg) + c0 + (tid & 7) * 8;
    const int rg = tid >> 3;
    float a[8] = {0, 0, 0, 0, 0, 0, 0, 0};
    bf16x8 v[10];
#pragma unroll
    for (int i = 0; i < 10; ++i) v[i] = *(const bf16x8*)(w + (size_t)(rg + 64 * i) * F_);
#pragma unroll
    for (int i = 0; i < 10; ++i)
#pragma unroll
      for (int e = 0; e < 8; ++e) a[e] += bf2f((u16)v[i][e]);
#pragma unroll
    for (int e = 0; e < 8; ++e) {
      a[e] += __shfl_xor(a[e], 8); a[e] += __shfl_xor(a[e], 16); a[e] += __shfl_xor(a[e], 32);
    }
    float* sf = (float*)smem;
    if (lane < 8) {
#pragma unroll
      for (int e = 0; e < 8; ++e) sf[wid * 64 + lane * 8 + e] = a[e];
    }
    __syncthreads();
    if (tid < 64) {
      float t = 0.f;
#pragma unroll
      for (int w8 = 0; w8 < 8; ++w8) t += sf[w8 * 64 + tid];
      cs[mat * F_ + c0 + tid] = t;
    }
  }
}

using C1 = Cfg<128, 80, 4, 1, 1, 6, 32, 1, 0>;
struct Src1 {
  const char* a; const char* b; static constexpr int ldA = S_ * 2, ldB = 0;
  __device__ __forceinline__ const char* baseA(int k) const { return a + k * 128; }
  __device__ __forceinline__ const char* baseB(int k, int) const { return b + k * (80 * 128); }
};
__global__ __launch_bounds__(C1::NT) void k1_seq(const u16* __restrict__ sre, const u16* __restrict__ sim,
                                                 const u16* __restrict__ xnI, u16* __restrict__ pqI) {
  __shared__ __attribute__((aligned(1024))) char smem[C1::LDS];
  const int bid = blockIdx.x, xcd = bid & 7, idx = bid >> 3;
  const int j = idx & 7, mq = idx >> 3;
  const int isQ = mq >> 1, t0 = xcd * 256 + (mq & 1) * 128;
  Src1 src{(const char*)((isQ ? sim : sre) + (size_t)t0 * S_), (const char*)(xnI + (size_t)j * 256 * 80 * 8)};
  Loop<C1, Src1> L((LAS char*)smem, src);
  f32x4 acc[C1::MF][C1::NF];
#pragma unroll
  for (int m = 0; m < C1::MF; ++m)
#pragma unroll
    for (int n = 0; n < C1::NF; ++n) acc[m][n] = (f32x4){0.f, 0.f, 0.f, 0.f};
  L.run(acc);
  const int fr = L.lane & 15, fq = L.lane >> 4;
  const float sg = isQ ? -1.f : 1.f;
#pragma unroll
  for (int m = 0; m < C1::MF; ++m) {
    const int t = t0 + L.wm * 32 + m * 16 + fr, mt = t >> 6, r = t & 63;
#pragma unroll
    for (int n = 0; n < C1::NF; ++n) {
      const int koct = isQ * 80 + j * 10 + n * 2 + (fq >> 1);
      u32x2 w; w[0] = pk2(sg * acc[m][n][0], sg * acc[m][n][1]); w[1] = pk2(sg * acc[m][n][2], sg * acc[m][n][3]);
      *(u32x2*)(pqI + (((size_t)mt * 160 + koct) * 64 + r) * 8 + (fq & 1) * 4) = w;
    }
  }
}

using C2 = Cfg<64, 80, 2, 1, 2, 8, 20, 0, 1>;
struct Src2 {
  const char* a; const char* bre; const char* bim; static constexpr int ldA = 0, ldB = D_ * 2;
  __device__ __forceinline__ const char* baseA(int k) const { return a + k * (64 * 128); }
  __device__ __forceinline__ const char* baseB(int k, int) const { return k < 10 ? bre + k * 128 : bim + (k - 10) * 128; }
};
__global__ __launch_bounds__(C2::NT) void k2_hid(const u16* __restrict__ pqI, const u16* __restrict__ hre, const u16* __restrict__ him,
                                                 const u16* __restrict__ x, u16* __restrict__ hI, f32x2* __restrict__ stats) {
  __shared__ __attribute__((aligned(1024))) char smem[C2::LDS];
  const int bid = blockIdx.x, xcd = bid & 7, idx = bid >> 3;
  const int j = idx & 7, mt = xcd * 4 + (idx >> 3);
  Src2 src{(const char*)(pqI + (size_t)mt * 160 * 64 * 8), (const char*)(hre + (size_t)j * 80 * D_), (const char*)(him + (size_t)j * 80 * D_)};
  Loop<C2, Src2> L((LAS char*)smem, src);
  f32x4 acc[C2::MF][C2::NF];
#pragma unroll
  for (int m = 0; m < C2::MF; ++m)
#pragma unroll
    for (int n = 0; n < C2::NF; ++n) acc[m][n] = (f32x4){0.f, 0.f, 0.f, 0.f};
  const int fr = L.lane & 15, fq = L.lane >> 4;
  const int t = mt * 64 + L.wm * 32 + L.wk * 16 + fr;
  L.run(acc);
  bf16x4 xv[C2::NF];
#pragma unroll
  for (int n = 0; n < C2::NF; ++n) xv[n] = *(const bf16x4*)(x + (size_t)t * D_ + j * 80 + n * 16 + fq * 4);
  L.reduce_k(acc);
  float hv[C2::NF][4];
  float s = 0.f;
#pragma unroll
  for (int n = 0; n < C2::NF; ++n) {
    u32x2 w;
    w[0] = pk2(acc[0][n][0] + bf2f((u16)xv[n][0]), acc[0][n][1] + bf2f((u16)xv[n][1]));
    w[1] = pk2(acc[0][n][2] + bf2f((u16)xv[n][2]), acc[0][n][3] + bf2f((u16)xv[n][3]));
    hv[n][0] = __uint_as_float(w[0] << 16); hv[n][1] = __uint_as_float(w[0] & 0xffff0000u);
    hv[n][2] = __uint_as_float(w[1] << 16); hv[n][3] = __uint_as_float(w[1] & 0xffff0000u);
    s += (hv[n][0] + hv[n][1]) + (hv[n][2] + hv[n][3]);
    const int koct = j * 10 + n * 2 + (fq >> 1);
    *(u32x2*)(hI + (((size_t)(t >> 8) * 80 + koct) * 256 + (t & 255)) * 8 + (fq & 1) * 4) = w;
  }
  s += __shfl_xor(s, 16); s += __shfl_xor(s, 32);
  const float mean = s * (1.0f / 80.0f);
  float q = 0.f;
#pragma unroll
  for (int n = 0; n < C2::NF; ++n)
#pragma unroll
    for (int r = 0; r < 4; ++r) { const float d = hv[n][r] - mean; q += d * d; }
  q += __shfl_xor(q, 16); q += __shfl_xor(q, 32);
  if (fq == 0) stats[t * 8 + j] = (f32x2){mean, q};
}

using C3 = Cfg<256, 128, 4, 2, 1, 3, 10, 0, 3>;
struct Src3 {
  const char* a; const char* bg; const char* bu; static constexpr int ldA = 0, ldB = F_ * 2;
  __device__ __forceinline__ const char* baseA(int k) const { return a + k * (256 * 128); }
  __device__ __forceinline__ const char* baseB(int k, int wid) const { return (wid < 4 ? bg : bu) + (size_t)k * (64 * F_ * 2); }
};
__global__ __launch_bounds__(C3::NT) void k3_glu(const u16* __restrict__ hI, const u16* __restrict__ wg, const u16* __restrict__ wu,
                                                 const f32x2* __restrict__ stats, const float* __restrict__ cs, u16* __restrict__ hhI) {
  __shared__ __attribute__((aligned(1024))) char smem[C3::LDS];
  const int bid = blockIdx.x, mt = bid & 7, j = bid >> 3;
  Src3 src{(const char*)(hI + (size_t)mt * 80 * 256 * 8), (const char*)(wg + j * 64), (const char*)(wu + j * 64)};
  Loop<C3, Src3> L((LAS char*)smem, src);
  f32x4 acc[C3::MF][C3::NF];
#pragma unroll
  for (int m = 0; m < C3::MF; ++m)
#pragma unroll
    for (int n = 0; n < C3::NF; ++n) acc[m][n] = (f32x4){0.f, 0.f, 0.f, 0.f};
  const int fr = L.lane & 15, fq = L.lane >> 4;
  L.run(acc);
  float mb[4], rden[4];
#pragma unroll
  for (int m = 0; m < 4; ++m) {
    const int t = mt * 256 + L.wm * 64 + m * 16 + fr;
    const f32x4 p = *(const f32x4*)(stats + t * 8 + fq * 2);
    float s = p[0] + p[2];
    s += __shfl_xor(s, 16); s += __shfl_xor(s, 32);
    const float mean = s * 0.125f;
    const float d0 = p[0] - mean, d1 = p[2] - mean;
    float q = p[1] + p[3] + 80.0f * (d0 * d0 + d1 * d1);
    q += __shfl_xor(q, 16); q += __shfl_xor(q, 32);
    const float vb = rbf(q * (1.0f / D_));
    mb[m] = rbf(mean);
    rden[m] = 1.0f / rbf(sqrtf(rbf(vb + 1e-5f)));
  }
#pragma unroll
  for (int n = 0; n < 2; ++n) {
    const int f0 = j * 64 + L.wn * 32 + n * 16 + fq * 4;
    const f32x4 cg = *(const f32x4*)(cs + f0), cu = *(const f32x4*)(cs + F_ + f0);
#pragma unroll
    for (int m = 0; m < 4; ++m) {
      const int t = mt * 256 + L.wm * 64 + m * 16 + fr;
      float o[4];
#pragma unroll
      for (int r = 0; r < 4; ++r) {
        const float g = (acc[m][n][r] - mb[m] * cg[r]) * rden[m];
        const float u = (acc[m][n + 2][r] - mb[m] * cu[r]) * rden[m];
        o[r] = u * __builtin_amdgcn_rcpf(1.0f + __expf(-g));
      }
      u32x2 w; w[0] = pk2(o[0], o[1]); w[1] = pk2(o[2], o[3]);
      *(u32x2*)(hhI + (((size_t)(t >> 6) * 216 + (f0 >> 3)) * 64 + (t & 63)) * 8 + (fq & 1) * 4) = w;
    }
  }
}

using C4 = Cfg<64, 80, 2, 1, 2, 8, 27, 0, 2>;
struct Src4 {
  const char* a; const char* b; static constexpr int ldA = 0, ldB = D_ * 2;
  __device__ __forceinline__ const char* baseA(int k) const { return a + k * (64 * 128); }
  __device__ __forceinline__ const char* baseB(int k, int) const { return b + (size_t)k * (64 * D_ * 2); }
};
__global__ __launch_bounds__(C4::NT) void k4_down(const u16* __restrict__ hhI, const u16* __restrict__ wd,
                                                  const u16* __restrict__ hI, u16* __restrict__ out) {
  __shared__ __attribute__((aligned(1024))) char smem[C4::LDS];
  const int bid = blockIdx.x, xcd = bid & 7, idx = bid >> 3;
  const int j = idx & 7, mt = xcd * 4 + (idx >> 3);
  Src4 src{(const char*)(hhI + (size_t)mt * 216 * 64 * 8), (const char*)(wd + j * 80)};
  Loop<C4, Src4> L((LAS char*)smem, src);
  f32x4 acc[C4::MF][C4::NF];
#pragma unroll
  for (int m = 0; m < C4::MF; ++m)
#pragma unroll
    for (int n = 0; n < C4::NF; ++n) acc[m][n] = (f32x4){0.f, 0.f, 0.f, 0.f};
  const int fr = L.lane & 15, fq = L.lane >> 4;
  const int t = mt * 64 + L.wm * 32 + L.wk * 16 + fr;
  L.run(acc);
  bf16x4 hv[C4::NF];
#pragma unroll
  for (int n = 0; n < C4::NF; ++n) {
    const int koct = j * 10 + n * 2 + (fq >> 1);
    hv[n] = *(const bf16x4*)(hI + (((size_t)(t >> 8) * 80 + koct) * 256 + (t & 255)) * 8 + (fq & 1) * 4);
  }
  L.reduce_k(acc);
#pragma unroll
  for (int n = 0; n < C4::NF; ++n) {
    u32x2 w;
    w[0] = pk2(acc[0][n][0] + bf2f((u16)hv[n][0]), acc[0][n][1] + bf2f((u16)hv[n][1]));
    w[1] = pk2(acc[0][n][2] + bf2f((u16)hv[n][2]), acc[0][n][3] + bf2f((u16)hv[n][3]));
    *(u32x2*)(out + (size_t)t * D_ + j * 80 + n * 16 + fq * 4) = w;
  }
}

extern "C" void kernel_launch(void* const* d_in, const int* in_sizes, int n_in, void* d_out, int out_size,
                              void* d_ws, size_t ws_size, hipStream_t stream) {
  const u16* x = (const u16*)d_in[0];
  const u16* sre = (const u16*)d_in[1];
  const u16* sim = (const u16*)d_in[2];
  const u16* hre = (const u16*)d_in[3];
  const u16* him = (const u16*)d_in[4];
  const u16* wg = (const u16*)d_in[6];
  const u16* wu = (const u16*)d_in[7];
  const u16* wd = (const u16*)d_in[8];
  char* ws = (char*)d_ws;
  u16* xnI = (u16*)(ws);
  u16* pqI = (u16*)(ws + (4u << 20));
  u16* hI = (u16*)(ws + (12u << 20));
  u16* hhI = (u16*)(ws + (16u << 20));
  f32x2* stats = (f32x2*)(ws + (24u << 20));
  float* cs = (float*)(ws + (25u << 20));
  k0_ln<<<256 + 54, 512, 0, stream>>>(x, xnI, wg, wu, cs);
  k1_seq<<<256, C1::NT, 0, stream>>>(sre, sim, xnI, pqI);
  k2_hid<<<256, C2::NT, 0, stream>>>(pqI, hre, him, x, hI, stats);
  k3_glu<<<216, C3::NT, 0, stream>>>(hI, wg, wu, stats, cs, hhI);
  k4_down<<<256, C4::NT, 0, stream>>>(hhI, wd, hI, (u16*)d_out);
}
```

```cpp
#include <hip/hip_runtime.h>
#include <hip/hip_bf16.h>
#define S_MODE 0
#define S_SPLIT 16
#define K1_AUXA 2
#define K1_AUXB 0

typedef unsigned short u16;
typedef short bf16x8 __attribute__((ext_vector_type(8)));
typedef short bf16x4 __attribute__((ext_vector_type(4)));
typedef __bf16 bf16x2n __attribute__((ext_vector_type(2)));
typedef float f32x4 __attribute__((ext_vector_type(4)));
typedef float f32x2 __attribute__((ext_vector_type(2)));
typedef unsigned u32x2 __attribute__((ext_vector_type(2)));
typedef unsigned u32x4 __attribute__((ext_vector_type(4)));
#define LAS __attribute__((address_space(3)))
#define GAS __attribute__((address_space(1)))

constexpr int S_ = 2048, D_ = 640, F_ = 1728;

__device__ __forceinline__ float bf2f(u16 v) { return __uint_as_float(((unsigned)v) << 16); }
__device__ __forceinline__ unsigned pk2(float lo, float hi) {
  unsigned r; asm("v_cvt_pk_bf16_f32 %0, %1, %2" : "=v"(r) : "v"(lo), "v"(hi)); return r;
}
__device__ __forceinline__ float rbf(float v) { return __uint_as_float(pk2(v, v) << 16); }

#define CSTAMP(kid, i)
#define STAMP(kid, i)
template <int N> __device__ __forceinline__ void wait_vm() { asm volatile("s_waitcnt vmcnt(%0)" ::"n"(N) : "memory"); }
__device__ __forceinline__ void wait_lgkm0() { asm volatile("" ::: "memory"); __builtin_amdgcn_s_waitcnt(0xC07F); asm volatile("" ::: "memory"); }
__device__ __forceinline__ void bar() { asm volatile("" ::: "memory"); __builtin_amdgcn_s_barrier(); asm volatile("" ::: "memory"); }

template <int AUX> __device__ __forceinline__ void glds16(const char* g, LAS char* l) {
  __builtin_amdgcn_global_load_lds((const GAS void*)g, (LAS void*)l, 16, 0, AUX);
}
template <int AUX> __device__ __forceinline__ void touch(const char* g, LAS char* scratch) {
  __builtin_amdgcn_global_load_lds((const GAS void*)g, (LAS void*)scratch, 4, 0, AUX);
}
__device__ __forceinline__ float dot_ones(const bf16x8& v, float acc) {
  const u32x4 u = __builtin_bit_cast(u32x4, v);
  const bf16x2n ones = __builtin_bit_cast(bf16x2n, 0x3F803F80u);
#pragma unroll
  for (int i = 0; i < 4; ++i) acc = __builtin_amdgcn_fdot2_f32_bf16(__builtin_bit_cast(bf16x2n, u[i]), ones, acc, false);
  return acc;
}

template <int KID_, int BM_, int BN_, int BK_, int WM_, int WN_, int WK_, int NL_, int NS_, int NK_, int AMODE_, int BMODE_, int SWAP_, int EXTRA_>
struct Cfg {
  static constexpr int KID = KID_, BM = BM_, BN = BN_, BK = BK_, WM = WM_, WN = WN_, WK = WK_, NL = NL_, NS = NS_, NK = NK_;
  static constexpr int AMODE = AMODE_, BMODE = BMODE_, SWAP = SWAP_, EXTRA = EXTRA_;
  static constexpr int NC = WM * WN * WK, NW = NC + NL, NT = NW * 64;
  static constexpr int MF = BM / WM / 16, NF = BN / WN / 16, KSW = BK / 32 / WK;
  static constexpr int ABYTES = BM * BK * 2, BBYTES = BN * BK * 2, STAGE = ABYTES + BBYTES;
  static constexpr int PA = ABYTES / 1024, PB = BBYTES / 1024;
  static constexpr int GA = PA / NL, GB = PB / NL;
  static constexpr int G = GA + GB;
  static constexpr int LDS = NS * STAGE;
  static_assert(PA % NL == 0 && PB % NL == 0, "pieces");
  static_assert((NS - 2) * G <= 63, "vmcnt");
  static_assert(NS <= NK, "stages");
  static_assert(KSW >= 1, "ksw");
};

template <class C> struct Frags { bf16x8 a[C::KSW][C::MF]; bf16x8 b[C::KSW][C::NF]; };

template <class C, class Src>
struct Loader {
  LAS char* lds; const Src& src;
  int lw, lane;
  unsigned voffA[C::GA], voffB[C::GB];
  template <int ROWS> static __device__ __forceinline__ unsigned rowmajor(int p, int ld) {
    const int sub = p / (ROWS * 8), q = p % (ROWS * 8), row = q >> 3, c = (q & 7) ^ ((row >> 1) & 7);
    return row * ld + sub * 128 + c * 16;
  }
  __device__ __forceinline__ Loader(LAS char* lds_, const Src& s, int lw_, int lane_) : lds(lds_), src(s), lw(lw_), lane(lane_) {
#pragma unroll
    for (int i = 0; i < C::GA; ++i) {
      const int p = (i * C::NL + lw) * 64 + lane;
      if constexpr (C::AMODE == 0) voffA[i] = p * 16;
      else voffA[i] = rowmajor<C::BM>(p, src.ldA);
    }
#pragma unroll
    for (int i = 0; i < C::GB; ++i) {
      const int p = (i * C::NL + lw) * 64 + lane;
      if constexpr (C::BMODE == 0) voffB[i] = p * 16;
      else if constexpr (C::BMODE == 1) voffB[i] = rowmajor<C::BN>(p, src.ldB);
      else if constexpr (C::BMODE == 2) { const int kp = p / 10, c = p % 10, k = kp ^ (((kp >> 3) & 1) << 2); voffB[i] = k * src.ldB + c * 16; }
      else { const int q = p % (C::BK * 8), k = q >> 3, c = (q & 7) ^ ((((k >> 3) & 1) << 2) | (((k >> 1) & 1) << 1)); voffB[i] = k * src.ldB + c * 16; }
    }
  }
  __device__ __forceinline__ void issue(int k, unsigned buf) {
    const char* ga = src.baseA(k);
    LAS char* la = lds + buf + lw * 1024;
#pragma unroll
    for (int i = 0; i < C::GA; ++i) glds16<Src::AUXA>(ga + voffA[i], la + i * C::NL * 1024);
    LAS char* lb = la + C::ABYTES;
#pragma unroll
    for (int i = 0; i < C::GB; ++i) glds16<Src::AUXB>(src.baseB(k, i >= C::GB / 2) + voffB[i], lb + i * C::NL * 1024);
  }
  template <int K> __device__ __forceinline__ void tail() {
    if constexpr (K + 1 < C::NK) {
      constexpr int outst = (C::NS - 2 < C::NK - 2 - K) ? C::NS - 2 : C::NK - 2 - K;
      wait_vm<outst * C::G + Src::NPF>();
      bar();
      tail<K + 1>();
    }
  }
  __device__ __forceinline__ void run() {
#pragma unroll 1
    for (int s = 0; s < C::NS; ++s) issue(s, s * C::STAGE);
    constexpr int W0 = (C::NS - 1) * C::G > 63 ? 63 : (C::NS - 1) * C::G;
    wait_vm<W0>();
    bar();
    unsigned cb = 0;
#pragma unroll 1
    for (int k = 0; k < C::NK - C::NS; ++k) {
      wait_vm<(C::NS - 2) * C::G>(); bar();
      issue(k + C::NS, cb);
      cb += C::STAGE; if (cb == C::LDS) cb = 0;
    }
    src.prefetch(lw, lane, lds + C::LDS);
    tail<C::NK - C::NS>();
    if constexpr (C::WK == 2) { bar(); bar(); }
    src.after(lw, lane);
  }
};

template <class C>
struct Consumer {
  LAS char* lds;
  int wid, lane, wm, wn, wk;
  unsigned rdA[2], rdB[4];
  f32x4 accS[C::MF], accQ[C::MF];
  float bsum[C::NF];

  __device__ __forceinline__ Consumer(LAS char* lds_, int wid_, int lane_) : lds(lds_), wid(wid_), lane(lane_) {
    wk = wid % C::WK; wn = (wid / C::WK) % C::WN; wm = wid / (C::WK * C::WN);
    const int fr = lane & 15, fq = lane >> 4;
    const int arow = wm * (C::BM / C::WM) + fr;
    const int ss0 = wk * C::KSW;
#pragma unroll
    for (int s = 0; s < 2; ++s) {
      const int ss = ss0 + s;
      if constexpr (C::AMODE == 0) rdA[s] = ((ss0 * 4 + fq) * C::BM + arow) * 16;
      else rdA[s] = (ss0 >> 1) * (C::BM * 128) + arow * 128 + ((((ss & 1) * 4 + fq) ^ ((fr >> 1) & 7)) * 16);
    }
    if constexpr (C::BMODE <= 1) {
      const int brow = wn * (C::BN / C::WN) + fr;
#pragma unroll
      for (int s = 0; s < 2; ++s) {
        const int ss = ss0 + s;
        if constexpr (C::BMODE == 0) rdB[s] = C::ABYTES + ((ss0 * 4 + fq) * C::BN + brow) * 16;
        else rdB[s] = C::ABYTES + (ss0 >> 1) * (C::BN * 128) + brow * 128 + ((((ss & 1) * 4 + fq) ^ ((fr >> 1) & 7)) * 16);
      }
    } else if constexpr (C::BMODE == 2) {
      const int g = lane >> 4, q = (lane >> 2) & 3, p = lane & 3;
#pragma unroll
      for (int t = 0; t < 2; ++t) rdB[t] = C::ABYTES + (32 * ss0 + 8 * g + 4 * (t ^ (g & 1)) + q) * 160 + p * 8 + wn * (C::BN / C::WN) * 2;
    } else {
      const int g = lane >> 4, q = (lane >> 2) & 3, p = lane & 3;
#pragma unroll
      for (int j = 0; j < (C::BMODE == 4 ? 4 : 2); ++j) {
        const int jj = (C::BMODE == 4) ? j : 2 * wn + j;
        rdB[j] = C::ABYTES + (32 * ss0 + 8 * g + q) * 128 + ((((jj ^ (((g & 1) << 1) | (q >> 1))) << 1) | (p >> 1)) * 16) + 8 * (p & 1);
      }
    }
#pragma unroll
    for (int m = 0; m < C::MF; ++m) { accS[m] = (f32x4){0.f, 0.f, 0.f, 0.f}; accQ[m] = accS[m]; }
#pragma unroll
    for (int n = 0; n < C::NF; ++n) bsum[n] = 0.f;
  }

  __device__ __forceinline__ void read(Frags<C>& f, unsigned buf) {
    static_assert(C::KSW <= 2 || (C::AMODE == 0 && C::BMODE != 1), "row-major images: at most two substeps per wave");
    LAS char* b = lds + buf;
#pragma unroll
    for (int s = 0; s < C::KSW; ++s) {
      if constexpr (C::BMODE == 0) {
#pragma unroll
        for (int n = 0; n < C::NF; ++n) f.b[s][n] = *(const LAS bf16x8*)(b + rdB[0] + s * (C::BN * 64) + n * 256);
      } else if constexpr (C::BMODE == 1) {
#pragma unroll
        for (int n = 0; n < C::NF; ++n) f.b[s][n] = *(const LAS bf16x8*)(b + rdB[s] + n * 2048);
      } else if constexpr (C::BMODE == 2) {
#pragma unroll
        for (int n = 0; n < C::NF; ++n) {
          bf16x4 lo = __builtin_amdgcn_ds_read_tr16_b64_v4i16((LAS bf16x4*)(b + rdB[0] + s * 5120 + n * 32));
          bf16x4 hi = __builtin_amdgcn_ds_read_tr16_b64_v4i16((LAS bf16x4*)(b + rdB[1] + s * 5120 + n * 32));
          f.b[s][n] = __builtin_shufflevector(lo, hi, 0, 1, 2, 3, 4, 5, 6, 7);
        }
      } else {
#pragma unroll
        for (int n = 0; n < C::NF; ++n) {
          const int j = (C::BMODE == 4) ? n : (n & 1), mat = (C::BMODE == 4) ? 0 : (n >> 1);
          bf16x4 lo = __builtin_amdgcn_ds_read_tr16_b64_v4i16((LAS bf16x4*)(b + rdB[j] + mat * (C::BK * 128) + s * 4096));
          bf16x4 hi = __builtin_amdgcn_ds_read_tr16_b64_v4i16((LAS bf16x4*)(b + rdB[j] + mat * (C::BK * 128) + s * 4096 + 512));
          f.b[s][n] = __builtin_shufflevector(lo, hi, 0, 1, 2, 3, 4, 5, 6, 7);
        }
      }
      if constexpr (C::AMODE == 0) {
#pragma unroll
        for (int m = 0; m < C::MF; ++m) f.a[s][m] = *(const LAS bf16x8*)(b + rdA[0] + s * (C::BM * 64) + m * 256);
      } else {
#pragma unroll
        for (int m = 0; m < C::MF; ++m) f.a[s][m] = *(const LAS bf16x8*)(b + rdA[s] + m * 2048);
      }
    }
  }

  __device__ __forceinline__ void mma(f32x4 (&acc)[C::MF][C::NF], const Frags<C>& f) {
#pragma unroll
    for (int s = 0; s < C::KSW; ++s) {
#pragma unroll
      for (int m = 0; m < C::MF; ++m)
#pragma unroll
        for (int n = 0; n < C::NF; ++n) {
          if constexpr (C::SWAP) acc[m][n] = __builtin_amdgcn_mfma_f32_16x16x32_bf16(f.b[s][n], f.a[s][m], acc[m][n], 0, 0, 0);
          else acc[m][n] = __builtin_amdgcn_mfma_f32_16x16x32_bf16(f.a[s][m], f.b[s][n], acc[m][n], 0, 0, 0);
        }
      if constexpr (C::EXTRA & 1) {
        const bf16x8 ones = {0x3F80, 0x3F80, 0x3F80, 0x3F80, 0x3F80, 0x3F80, 0x3F80, 0x3F80};
#pragma unroll
        for (int m = 0; m < C::MF; ++m) {
          accS[m] = __builtin_amdgcn_mfma_f32_16x16x32_bf16(f.a[s][m], ones, accS[m], 0, 0, 0);
          accQ[m] = __builtin_amdgcn_mfma_f32_16x16x32_bf16(f.a[s][m], f.a[s][m], accQ[m], 0, 0, 0);
        }
      }
      if constexpr (C::EXTRA & 2) {
#pragma unroll
        for (int n = 0; n < C::NF; ++n) bsum[n] = dot_ones(f.b[s][n], bsum[n]);
      }
    }
  }
  static constexpr int NRD = C::KSW * (C::MF + (C::BMODE >= 2 ? 2 : 1) * C::NF);
  static constexpr int NMM = C::KSW * C::MF * (C::NF + ((C::EXTRA & 1) ? 2 : 0));
  static constexpr int NVA = (C::EXTRA & 2) ? C::KSW * C::NF * 4 : 0;
  __device__ __forceinline__ void interleave() {
    constexpr int RPM = (NRD + NMM - 3) / (NMM - 2);
    constexpr int VPM = (NVA + NMM - 1) / NMM;
#pragma unroll
    for (int i = 0; i < NMM; ++i) {
      __builtin_amdgcn_sched_group_barrier(0x100, RPM, 0);
      __builtin_amdgcn_sched_group_barrier(0x008, 1, 0);
      if constexpr (VPM > 0) __builtin_amdgcn_sched_group_barrier(0x002, VPM, 0);
    }
  }
  __device__ __forceinline__ void step(f32x4 (&acc)[C::MF][C::NF], Frags<C>& cur, Frags<C>& nxt, unsigned nb) {
    wait_lgkm0(); bar();
    __builtin_amdgcn_sched_barrier(0);
    read(nxt, nb);
    mma(acc, cur);
    interleave();
    __builtin_amdgcn_sched_barrier(0);
  }

  __device__ __forceinline__ void run(f32x4 (&acc)[C::MF][C::NF]) {
    Frags<C> f0, f1;
    bar();
    if (wid == 0) { STAMP(C::KID, 1); CSTAMP(C::KID, 8); }
    read(f0, 0);
    unsigned nb = 0;
#pragma unroll 1
    for (int k = 0; k + 2 < C::NK; k += 2) {
      nb += C::STAGE; if (nb == C::LDS) nb = 0;
      step(acc, f0, f1, nb);
      nb += C::STAGE; if (nb == C::LDS) nb = 0;
      step(acc, f1, f0, nb);
    }
    if constexpr (C::NK % 2 == 0) {
      nb += C::STAGE; if (nb == C::LDS) nb = 0;
      step(acc, f0, f1, nb);
      mma(acc, f1);
    } else {
      mma(acc, f0);
    }
    if (wid == 0) { STAMP(C::KID, 2); CSTAMP(C::KID, 9); }
  }

  static constexpr bool SPLM = (C::MF % 2 == 0);
  static constexpr int RM = SPLM ? C::MF / 2 : C::MF, RN = SPLM ? C::NF : C::NF / 2;
  __device__ __forceinline__ void reduce_k(f32x4 (&acc)[C::MF][C::NF], f32x4 (&red)[RM][RN]) {
    static_assert(C::WK == 2, "");
    wait_lgkm0(); bar();
    LAS f32x4* x = (LAS f32x4*)lds;
#pragma unroll
    for (int m = 0; m < RM; ++m)
#pragma unroll
      for (int n = 0; n < RN; ++n) {
        const f32x4 lo = acc[m][n], hi = SPLM ? acc[m + RM][n] : acc[m][n + RN];
        x[(wid * RM * RN + m * RN + n) * 64 + lane] = wk ? lo : hi;
        red[m][n] = wk ? hi : lo;
      }
    wait_lgkm0(); bar();
#pragma unroll
    for (int m = 0; m < RM; ++m)
#pragma unroll
      for (int n = 0; n < RN; ++n) red[m][n] += x[((wid ^ 1) * RM * RN + m * RN + n) * 64 + lane];
  }
};

#define GEMM_BODY(C, SRC, src)                                                          \
  const int wid_ = __builtin_amdgcn_readfirstlane(threadIdx.x >> 6), lane_ = threadIdx.x & 63; \
  if (wid_ == 0) STAMP(C::KID, 0);                                                      \
  if (wid_ >= C::NC) { Loader<C, SRC> ld((LAS char*)smem, src, wid_ - C::NC, lane_); ld.run(); return; } \
  Consumer<C> L((LAS char*)smem, wid_, lane_);                                          \
  f32x4 acc[C::MF][C::NF];                                                              \
  _Pragma("unroll") for (int m = 0; m < C::MF; ++m)                                     \
  _Pragma("unroll") for (int n = 0; n < C::NF; ++n) acc[m][n] = (f32x4){0.f, 0.f, 0.f, 0.f};

__global__ __launch_bounds__(512) void k0_ln(const u16* __restrict__ x, u16* __restrict__ xnI,
                                             const u16* __restrict__ sre, const u16* __restrict__ sim,
                                             const u16* __restrict__ hre, const u16* __restrict__ him) {
  __shared__ __attribute__((aligned(16))) char smem[8 * 1296 + 64];
  const int tid = threadIdx.x, wid = tid >> 6, lane = tid & 63;
  if (wid == 0) STAMP(0, 0);
  if (blockIdx.x < 256) {
    const int koct = blockIdx.x, row = koct * 8 + wid;
    const u16* xr = x + row * D_;
    bf16x8 v0 = *(const bf16x8*)(xr + lane * 8);
    bf16x8 v1 = {0, 0, 0, 0, 0, 0, 0, 0};
    if (lane < 16) v1 = *(const bf16x8*)(xr + 512 + lane * 8);
    float f[16];
#pragma unroll
    for (int i = 0; i < 8; ++i) { f[i] = bf2f((u16)v0[i]); f[8 + i] = bf2f((u16)v1[i]); }
    float s = 0.f;
#pragma unroll
    for (int i = 0; i < 16; ++i) s += f[i];
#pragma unroll
    for (int o = 32; o >= 1; o >>= 1) s += __shfl_xor(s, o);
    const float mean = s * (1.0f / D_);
    float q = 0.f;
#pragma unroll
    for (int i = 0; i < 8; ++i) { const float d = f[i] - mean; q += d * d; }
    if (lane < 16) {
#pragma unroll
      for (int i = 8; i < 16; ++i) { const float d = f[i] - mean; q += d * d; }
    }
#pragma unroll
    for (int o = 32; o >= 1; o >>= 1) q += __shfl_xor(q, o);
    const float mb = rbf(mean), vb = rbf(q * (1.0f / D_));
    const float den = rbf(sqrtf(rbf(vb + 1e-5f)));
    const float rden = 1.0f / den;
    u32x4 w0, w1;
#pragma unroll
    for (int i = 0; i < 4; ++i) {
      w0[i] = pk2(rbf(f[2 * i] - mb) * rden, rbf(f[2 * i + 1] - mb) * rden);
      w1[i] = pk2(rbf(f[8 + 2 * i] - mb) * rden, rbf(f[9 + 2 * i] - mb) * rden);
    }
    char* sr = smem + wid * 1296;
    *(u32x4*)(sr + lane * 16) = w0;
    if (lane < 16) *(u32x4*)(sr + 1024 + lane * 16) = w1;
    __syncthreads();
    for (int d = tid; d < D_; d += 512) {
      bf16x8 o;
#pragma unroll
      for (int r = 0; r < 8; ++r) o[r] = *(const short*)(smem + r * 1296 + d * 2);
      const int j = d / 80, n = d % 80;
      *(bf16x8*)(xnI + (((size_t)j * 256 + koct) * 80 + n) * 8) = o;
    }
  } else {
    const int b = blockIdx.x - 256;
    LAS char* scr = (LAS char*)smem + __builtin_amdgcn_readfirstlane(wid) * 256;
    if (b < 256) {
      const int xcd = b & 7, part = b >> 3;
      const char* base = (const char*)((part & 16) ? sim : sre) + (size_t)xcd * (256 * S_ * 2) + (size_t)(part & 15) * 65536;
      touch<2>(base + tid * 128, scr);
    } else {
      const int l = (b - 256) * 512 + tid;
      if (l < 12800) touch<0>(l < 6400 ? (const char*)hre + l * 128 : (const char*)him + (l - 6400) * 128, scr);
    }
    wait_vm<0>();
  }
  if (wid == 0) { wait_vm<0>(); STAMP(0, 3); }
}

#define K1_WK 1
#define K1_NS 6
using C1 = Cfg<1, 128, 80, 64, 4 / K1_WK, 1, K1_WK, 2, K1_NS, 32, 1, 0, 1, 0>;
struct Src1 {
  const char* a; const char* b; const u16* wg; const u16* wu; float* csp; int bid;
  static constexpr int ldA = S_ * 2, ldB = 0, AUXA = K1_AUXA, AUXB = K1_AUXB;
  static constexpr int NPF = 0;
  __device__ __forceinline__ const char* baseA(int k) const { return a + k * 128; }
  __device__ __forceinline__ const char* baseB(int k, int) const { return b + k * (80 * 128); }
  __device__ __forceinline__ void prefetch(int, int, LAS char*) const {}
  __device__ __forceinline__ void after(int lw, int lane) const {
    if (bid >= 216) return;
    const int c = bid >> 2, q = bid & 3, mat = c / 27, c0 = (c % 27) * 64;
    const u16* w = (mat ? wu : wg) + (size_t)(q * 160 + lw * 80 + (lane >> 3)) * F_ + c0 + (lane & 7) * 8;
    bf16x8 v[10];
#pragma unroll
    for (int i = 0; i < 10; ++i) v[i] = *(const bf16x8*)(w + (size_t)i * 8 * F_);
    float s[8] = {0, 0, 0, 0, 0, 0, 0, 0};
#pragma unroll
    for (int i = 0; i < 10; ++i)
#pragma unroll
      for (int e = 0; e < 8; ++e) s[e] += bf2f((u16)v[i][e]);
#pragma unroll
    for (int e = 0; e < 8; ++e) { s[e] += __shfl_xor(s[e], 8); s[e] += __shfl_xor(s[e], 16); s[e] += __shfl_xor(s[e], 32); }
    if (lane < 8) {
      float* o = csp + (size_t)(q * 2 + lw) * (2 * F_) + mat * F_ + c0 + lane * 8;
      *(f32x4*)o = (f32x4){s[0], s[1], s[2], s[3]};
      *(f32x4*)(o + 4) = (f32x4){s[4], s[5], s[6], s[7]};
    }
  }
};
__global__ __launch_bounds__(C1::NT) void k1_seq(const u16* __restrict__ sre, const u16* __restrict__ sim,
                                                 const u16* __restrict__ xnI, u16* __restrict__ pqI,
                                                 const u16* __restrict__ wg, const u16* __restrict__ wu, float* __restrict__ csp) {
  __shared__ __attribute__((aligned(1024))) char smem[C1::LDS + 256];
  const int bid = blockIdx.x, xcd = bid & 7, idx = bid >> 3;
  const int j = idx & 7, mq = idx >> 3;
  const int isQ = mq >> 1, t0 = xcd * 256 + (mq & 1) * 128;
  Src1 src{(const char*)((isQ ? sim : sre) + (size_t)t0 * S_), (const char*)(xnI + (size_t)j * 256 * 80 * 8), wg, wu, csp, bid};
  GEMM_BODY(C1, Src1, src)
  L.run(acc);
  const int fr = L.lane & 15, fq = L.lane >> 4;
  const float sg = isQ ? -1.f : 1.f;
  auto& red = acc;
  const int r0 = t0 + L.wm * 32;
#pragma unroll
  for (int m = 0; m < 2; ++m) {
    const int t = r0 + m * 16 + fr, mt = t >> 6, r = t & 63;
#pragma unroll
    for (int n = 0; n < 5; ++n) {
      const int koct = isQ * 80 + j * 10 + n * 2 + (fq >> 1);
      u32x2 w; w[0] = pk2(sg * red[m][n][0], sg * red[m][n][1]); w[1] = pk2(sg * red[m][n][2], sg * red[m][n][3]);
      *(u32x2*)(pqI + (((size_t)mt * 160 + koct) * 64 + r) * 8 + (fq & 1) * 4) = w;
    }
  }
  if (wid_ == 0) { wait_vm<0>(); STAMP(1, 3); }
}

#define K2_BK 128
using C2 = Cfg<2, 64, 80, K2_BK, 2, 1, 2, 2, 512 / K2_BK, 1280 / K2_BK, 0, 1, 1, 0>;
struct Src2 {
  const char* a; const char* bre; const char* bim; const char* wd; int bid;
  static constexpr int ldA = 0, ldB = D_ * 2, AUXA = 0, AUXB = 0;
  static constexpr int NH = 640 / K2_BK;
  __device__ __forceinline__ const char* baseA(int k) const { return a + k * (64 * K2_BK * 2); }
  __device__ __forceinline__ const char* baseB(int k, int) const { return k < NH ? bre + k * (K2_BK * 2) : bim + (k - NH) * (K2_BK * 2); }
  static constexpr int NPF = 1;
  __device__ __forceinline__ void prefetch(int lw, int lane, LAS char* scr) const {
    int l = (bid * 2 + lw) * 34 + lane; l = (lane < 34 && l < 17280) ? l : 0;
    touch<0>(wd + (size_t)l * 128, scr);
  }
  __device__ __forceinline__ void after(int, int) const { wait_vm<0>(); }
};
__global__ __launch_bounds__(C2::NT) void k2_hid(const u16* __restrict__ pqI, const u16* __restrict__ hre, const u16* __restrict__ him,
                                                 const u16* __restrict__ x, u16* __restrict__ hI, f32x2* __restrict__ stats,
                                                 const u16* __restrict__ wd) {
  __shared__ __attribute__((aligned(1024))) char smem[C2::LDS + 256];
  const int bid = blockIdx.x, xcd = bid & 7, idx = bid >> 3;
  const int j = idx & 7, mt = xcd * 4 + (idx >> 3);
  Src2 src{(const char*)(pqI + (size_t)mt * 160 * 64 * 8), (const char*)(hre + (size_t)j * 80 * D_), (const char*)(him + (size_t)j * 80 * D_),
           (const char*)wd, bid};
  GEMM_BODY(C2, Src2, src)
  const int fr = L.lane & 15, fq = L.lane >> 4;
  const int t = mt * 64 + L.wm * 32 + L.wk * 16 + fr;
  bf16x4 xv[C2::NF];
#pragma unroll
  for (int n = 0; n < C2::NF; ++n) xv[n] = *(const bf16x4*)(x + (size_t)t * D_ + j * 80 + n * 16 + fq * 4);
#pragma unroll
  for (int n = 0; n < C2::NF; ++n) asm volatile("" : "+v"(xv[n]));
  L.run(acc);
  f32x4 red[1][5];
  L.reduce_k(acc, red);
  float hv[C2::NF][4];
  float s = 0.f;
#pragma unroll
  for (int n = 0; n < C2::NF; ++n) {
    u32x2 w;
    w[0] = pk2(red[0][n][0] + bf2f((u16)xv[n][0]), red[0][n][1] + bf2f((u16)xv[n][1]));
    w[1] = pk2(red[0][n][2] + bf2f((u16)xv[n][2]), red[0][n][3] + bf2f((u16)xv[n][3]));
    hv[n][0] = __uint_as_float(w[0] << 16); hv[n][1] = __uint_as_float(w[0] & 0xffff0000u);
    hv[n][2] = __uint_as_float(w[1] << 16); hv[n][3] = __uint_as_float(w[1] & 0xffff0000u);
    s += (hv[n][0] + hv[n][1]) + (hv[n][2] + hv[n][3]);
    const int koct = j * 10 + n * 2 + (fq >> 1);
    *(u32x2*)(hI + (((size_t)(t >> 8) * 80 + koct) * 256 + (t & 255)) * 8 + (fq & 1) * 4) = w;
  }
  s += __shfl_xor(s, 16); s += __shfl_xor(s, 32);
  const float mean = s * (1.0f / 80.0f);
  float q = 0.f;
#pragma unroll
  for (int n = 0; n < C2::NF; ++n)
#pragma unroll
    for (int r = 0; r < 4; ++r) { const float d = hv[n][r] - mean; q += d * d; }
  q += __shfl_xor(q, 16); q += __shfl_xor(q, 32);
  if (fq == 0) stats[t * 8 + j] = (f32x2){mean, q};
  if (wid_ == 0) { wait_vm<0>(); STAMP(2, 3); }
}

using C3 = Cfg<3, 256, 128, 32, 2, 2, 1, 2, 6, 20, 0, 3, 1, 0>;
struct Src3 {
  const char* a; const char* bg; const char* bu;
  static constexpr int ldA = 0, ldB = F_ * 2, AUXA = 0, AUXB = 0;
  static constexpr int NPF = 0;
  __device__ __forceinline__ const char* baseA(int k) const { return a + k * (256 * 64); }
  __device__ __forceinline__ const char* baseB(int k, int up) const { return (up ? bu : bg) + (size_t)k * (32 * F_ * 2); }
  __device__ __forceinline__ void prefetch(int, int, LAS char*) const {}
  __device__ __forceinline__ void after(int, int) const {}
};
__global__ __launch_bounds__(C3::NT) void k3_glu(const u16* __restrict__ hI, const u16* __restrict__ wg, const u16* __restrict__ wu,
                                                 const f32x2* __restrict__ stats, const float* __restrict__ csp, u16* __restrict__ hhI) {
  __shared__ __attribute__((aligned(1024))) char smem[C3::LDS + 256 + 4096 + 512];
  const int bid = blockIdx.x, xcd = bid & 7, idx = bid >> 3;
  const int mt = (xcd & 6) + (idx & 1), j = (xcd & 1) * 14 + (idx >> 1);
  if (j >= 27) return;
  Src3 src{(const char*)(hI + (size_t)mt * 80 * 256 * 8), (const char*)(wg + j * 64), (const char*)(wu + j * 64)};
  GEMM_BODY(C3, Src3, src)
  const int fr = L.lane & 15, fq = L.lane >> 4;
  LAS f32x4* rs = (LAS f32x4*)((LAS char*)smem + C3::LDS + 256);
  LAS float* csl = (LAS float*)((LAS char*)smem + C3::LDS + 256 + 4096);
  {
    const int r = wid_ * 64 + lane_;
    const f32x4* p = (const f32x4*)(stats + (mt * 256 + r) * 8);
    const f32x4 p0 = p[0], p1 = p[1], p2 = p[2], p3 = p[3];
    float cv[8];
    if (wid_ < 2) {
#pragma unroll
      for (int i = 0; i < 8; ++i) cv[i] = csp[i * (2 * F_) + wid_ * F_ + j * 64 + lane_];
    }
    const float mean = ((p0[0] + p0[2]) + (p1[0] + p1[2]) + (p2[0] + p2[2]) + (p3[0] + p3[2])) * 0.125f;
    float q = (p0[1] + p0[3]) + (p1[1] + p1[3]) + (p2[1] + p2[3]) + (p3[1] + p3[3]);
    float dd = 0.f, d;
    d = p0[0] - mean; dd += d * d; d = p0[2] - mean; dd += d * d; d = p1[0] - mean; dd += d * d; d = p1[2] - mean; dd += d * d;
    d = p2[0] - mean; dd += d * d; d = p2[2] - mean; dd += d * d; d = p3[0] - mean; dd += d * d; d = p3[2] - mean; dd += d * d;
    q += 80.0f * dd;
    const float vb = rbf(q * (1.0f / D_));
    const float mb = rbf(mean), rd = 1.0f / rbf(sqrtf(rbf(vb + 1e-5f)));
    const float nl = -1.44269504089f * rd;
    rs[r] = (f32x4){mb, rd, nl, -mb * nl};
    if (wid_ < 2) csl[wid_ * 64 + lane_] = ((cv[0] + cv[1]) + (cv[2] + cv[3])) + ((cv[4] + cv[5]) + (cv[6] + cv[7]));
    wait_lgkm0();
  }
  L.run(acc);
#pragma unroll
  for (int n = 0; n < 2; ++n) {
    const int fl = L.wn * 32 + n * 16 + fq * 4, f0 = j * 64 + fl;
    const f32x4 cg = *(const LAS f32x4*)(csl + fl), cu = *(const LAS f32x4*)(csl + 64 + fl);
#pragma unroll
    for (int m = 0; m < 8; ++m) {
      const int rl = L.wm * 128 + m * 16 + fr, t = mt * 256 + rl;
      const f32x4 st = rs[rl];
      const f32x4 e2 = acc[m][n] * st[2] + cg * st[3];
      const f32x4 u = (acc[m][n + 2] - cu * st[0]) * st[1];
      f32x4 o;
#pragma unroll
      for (int r = 0; r < 4; ++r) o[r] = __builtin_amdgcn_rcpf(1.0f + __builtin_amdgcn_exp2f(e2[r]));
      o = o * u;
      u32x2 w; w[0] = pk2(o[0], o[1]); w[1] = pk2(o[2], o[3]);
      const int t4 = t / 96;
      *(u32x2*)(hhI + (((size_t)t4 * 216 + (f0 >> 3)) * 96 + (t - t4 * 96)) * 8 + (fq & 1) * 4) = w;
    }
  }
  if (wid_ == 0) { wait_vm<0>(); STAMP(3, 3); }
}

using C4 = Cfg<4, 96, 64, 64, 2, 1, 2, 2, 6, 27, 0, 4, 1, 0>;
struct Src4 {
  const char* a; const char* b;
  static constexpr int ldA = 0, ldB = D_ * 2, AUXA = 0, AUXB = 0;
  static constexpr int NPF = 0;
  __device__ __forceinline__ const char* baseA(int k) const { return a + k * (96 * 128); }
  __device__ __forceinline__ const char* baseB(int k, int) const { return b + (size_t)k * (64 * D_ * 2); }
  __device__ __forceinline__ void prefetch(int, int, LAS char*) const {}
  __device__ __forceinline__ void after(int, int) const {}
};
__global__ __launch_bounds__(C4::NT) void k4_down(const u16* __restrict__ hhI, const u16* __restrict__ wd,
                                                  const u16* __restrict__ hI, u16* __restrict__ out) {
  __shared__ __attribute__((aligned(1024))) char smem[C4::LDS + 256];
  const int bid = blockIdx.x, lin = (bid & 7) * 28 + (bid >> 3);
  if (lin >= 220) return;
  const int mt = lin / 10, j = lin % 10;
  Src4 src{(const char*)(hhI + (size_t)mt * 216 * 96 * 8), (const char*)(wd + j * 64)};
  GEMM_BODY(C4, Src4, src)
  const int fr = L.lane & 15, fq = L.lane >> 4;
  const int tb = mt * 96 + L.wm * 48 + fr, c0 = j * 64 + L.wk * 32 + fq * 4;
  bf16x4 hv[3][2];
#pragma unroll
  for (int m = 0; m < 3; ++m)
#pragma unroll
    for (int n = 0; n < 2; ++n) {
      const int t = min(tb + m * 16, S_ - 1), c = c0 + n * 16;
      hv[m][n] = *(const bf16x4*)(hI + (((size_t)(t >> 8) * 80 + (c >> 3)) * 256 + (t & 255)) * 8 + (c & 4));
    }
#pragma unroll
  for (int m = 0; m < 3; ++m)
#pragma unroll
    for (int n = 0; n < 2; ++n) asm volatile("" : "+v"(hv[m][n]));
  L.run(acc);
  f32x4 red[3][2];
  L.reduce_k(acc, red);
#pragma unroll
  for (int m = 0; m < 3; ++m)
#pragma unroll
    for (int n = 0; n < 2; ++n) {
      const int t = tb + m * 16;
      u32x2 w;
      w[0] = pk2(red[m][n][0] + bf2f((u16)hv[m][n][0]), red[m][n][1] + bf2f((u16)hv[m][n][1]));
      w[1] = pk2(red[m][n][2] + bf2f((u16)hv[m][n][2]), red[m][n][3] + bf2f((u16)hv[m][n][3]));
      if (t < S_) *(u32x2*)(out + (size_t)t * D_ + c0 + n * 16) = w;
    }
  if (wid_ == 0) { wait_vm<0>(); STAMP(4, 3); }
}

extern "C" void kernel_launch(void* const* d_in, const int* in_sizes, int n_in, void* d_out, int out_size,
                              void* d_ws, size_t ws_size, hipStream_t stream) {
  const u16* x = (const u16*)d_in[0];
  const u16* sre = (const u16*)d_in[1];
  const u16* sim = (const u16*)d_in[2];
  const u16* hre = (const u16*)d_in[3];
  const u16* him = (const u16*)d_in[4];
  const u16* wg = (const u16*)d_in[6];
  const u16* wu = (const u16*)d_in[7];
  const u16* wd = (const u16*)d_in[8];
  char* ws = (char*)d_ws;
  u16* xnI = (u16*)(ws);
  u16* pqI = (u16*)(ws + 2621440);
  u16* hhI = (u16*)(ws);
  u16* hI = (u16*)(ws + (8u << 20));
  f32x2* stats = (f32x2*)(ws + (11u << 20));
  float* csp = (float*)(ws + (11u << 20) + 131072);
  k0_ln<<<512 + 25, 512, 0, stream>>>(x, xnI, sre, sim, hre, him);
  k1_seq<<<256, C1::NT, 0, stream>>>(sre, sim, xnI, pqI, wg, wu, csp);
  k2_hid<<<256, C2::NT, 0, stream>>>(pqI, hre, him, x, hI, stats, wd);
  k3_glu<<<224, C3::NT, 0, stream>>>(hI, wg, wu, stats, csp, hhI);
  k4_down<<<224, C4::NT, 0, stream>>>(hhI, wd, hI, (u16*)d_out);
}
```

```cpp
#include <hip/hip_runtime.h>
#include <hip/hip_bf16.h>
#define PF_W 1
#define PF_N 1

typedef unsigned short u16;
typedef short bf16x8 __attribute__((ext_vector_type(8)));
typedef short bf16x4 __attribute__((ext_vector_type(4)));
typedef float f32x4 __attribute__((ext_vector_type(4)));
typedef float f32x2 __attribute__((ext_vector_type(2)));
typedef unsigned u32x2 __attribute__((ext_vector_type(2)));
typedef unsigned u32x4 __attribute__((ext_vector_type(4)));
#define LAS __attribute__((address_space(3)))
#define GAS __attribute__((address_space(1)))

constexpr int S_ = 2048, D_ = 640, F_ = 1728;

__device__ __forceinline__ float bf2f(u16 v) { return __uint_as_float(((unsigned)v) << 16); }
__device__ __forceinline__ unsigned pk2(float lo, float hi) {
  unsigned r; asm volatile("v_cvt_pk_bf16_f32 %0, %1, %2" : "=v"(r) : "v"(lo), "v"(hi)); return r;
}
__device__ __forceinline__ float rbf(float v) { return __uint_as_float(pk2(v, v) << 16); }

#define CSTAMP(kid, i)
#define STAMP(kid, i)
template <int N> __device__ __forceinline__ void wait_vm() { asm volatile("s_waitcnt vmcnt(%0)" ::"n"(N) : "memory"); }
__device__ __forceinline__ void wait_lgkm0() { asm volatile("" ::: "memory"); __builtin_amdgcn_s_waitcnt(0xC07F); asm volatile("" ::: "memory"); }
__device__ __forceinline__ void bar() { asm volatile("" ::: "memory"); __builtin_amdgcn_s_barrier(); asm volatile("" ::: "memory"); }

__device__ __forceinline__ void glds16(const char* g, LAS char* l) {
  __builtin_amdgcn_global_load_lds((const GAS void*)g, (LAS void*)l, 16, 0, 0);
}

__device__ __forceinline__ void touch(const char* g, LAS char* scratch) {
  __builtin_amdgcn_global_load_lds((const GAS void*)g, (LAS void*)scratch, 4, 0, 0);
}

template <int KID_, int BM_, int BN_, int BK_, int WM_, int WN_, int WK_, int NL_, int NS_, int NK_, int AMODE_, int BMODE_>
struct Cfg {
  static constexpr int KID = KID_, BM = BM_, BN = BN_, BK = BK_, WM = WM_, WN = WN_, WK = WK_, NL = NL_, NS = NS_, NK = NK_, AMODE = AMODE_, BMODE = BMODE_;
  static constexpr int NC = WM * WN * WK, NW = NC + NL, NT = NW * 64;
  static constexpr int MF = BM / WM / 16, NF = BN / WN / 16, KSW = BK / 32 / WK;
  static constexpr int ABYTES = BM * BK * 2, BBYTES = BN * BK * 2, STAGE = ABYTES + BBYTES;
  static constexpr int PA = ABYTES / 1024, PB = BBYTES / 1024;
  static constexpr int GA = PA / NL, GB = PB / NL, G = GA + GB;
  static constexpr int LDS = NS * STAGE;
  static_assert(PA % NL == 0 && PB % NL == 0, "pieces");
  static_assert((NS - 2) * G <= 56, "vmcnt");
  static_assert(NS <= NK, "stages");
  static_assert(KSW >= 1, "ksw");
};

template <class C> struct Frags { bf16x8 a[C::KSW][C::MF]; bf16x8 b[C::KSW][C::NF]; };

template <class C, class Src>
struct Loader {
  LAS char* lds; const Src& src;
  int lw, lane;
  unsigned voffA[C::GA], voffB[C::GB];
  __device__ __forceinline__ Loader(LAS char* lds_, const Src& s, int lw_, int lane_) : lds(lds_), src(s), lw(lw_), lane(lane_) {
#pragma unroll
    for (int i = 0; i < C::GA; ++i) {
      const int p = (i * C::NL + lw) * 64 + lane;
      if constexpr (C::AMODE == 0) voffA[i] = p * 16;
      else { const int row = p >> 3, c = (p & 7) ^ ((row >> 1) & 7); voffA[i] = row * src.ldA + c * 16; }
    }
#pragma unroll
    for (int i = 0; i < C::GB; ++i) {
      const int p = (i * C::NL + lw) * 64 + lane;
      if constexpr (C::BMODE == 0) voffB[i] = p * 16;
      else if constexpr (C::BMODE == 1) { const int row = p >> 3, c = (p & 7) ^ ((row >> 1) & 7); voffB[i] = row * src.ldB + c * 16; }
      else if constexpr (C::BMODE == 2) { const int kp = p / 10, c = p % 10, k = kp ^ (((kp >> 3) & 1) << 2); voffB[i] = k * src.ldB + c * 16; }
      else { const int q = p % (C::BK * 8), k = q >> 3, c = (q & 7) ^ ((((k >> 3) & 1) << 2) | (((k >> 1) & 1) << 1)); voffB[i] = k * src.ldB + c * 16; }
    }
  }
  __device__ __forceinline__ void issue(int k, unsigned buf) {
    const char* ga = src.baseA(k);
    LAS char* la = lds + buf + lw * 1024;
#pragma unroll
    for (int i = 0; i < C::GA; ++i) glds16(ga + voffA[i], la + i * C::NL * 1024);
    LAS char* lb = la + C::ABYTES;
#pragma unroll
    for (int i = 0; i < C::GB; ++i) glds16(src.baseB(k, i >= C::GB / 2) + voffB[i], lb + i * C::NL * 1024);
  }
  template <int K> __device__ __forceinline__ void tail() {
    if constexpr (K + 1 < C::NK) {
      constexpr int outst = (C::NS - 2 < C::NK - 2 - K) ? C::NS - 2 : C::NK - 2 - K;
      wait_vm<outst * C::G + Src::NPF>();
      bar();
      tail<K + 1>();
    }
  }
  __device__ __forceinline__ void run() {
#pragma unroll 1
    for (int s = 0; s < C::NS; ++s) issue(s, s * C::STAGE);
    constexpr int W0 = (C::NS - 1) * C::G > 63 ? 63 : (C::NS - 1) * C::G;
    wait_vm<W0>();
    bar();
    unsigned cb = 0;
#pragma unroll 1
    for (int k = 0; k < C::NK - C::NS; ++k) {
      wait_vm<(C::NS - 2) * C::G>(); bar();
      issue(k + C::NS, cb);
      cb += C::STAGE; if (cb == C::LDS) cb = 0;
    }
    src.prefetch(lw, lane, lds + C::LDS);
    tail<C::NK - C::NS>();
    wait_vm<0>();
  }
};

template <class C>
struct Consumer {
  LAS char* lds;
  int wid, lane, wm, wn, wk;
  unsigned rdA[2], rdB[2];

  __device__ __forceinline__ Consumer(LAS char* lds_, int wid_, int lane_) : lds(lds_), wid(wid_), lane(lane_) {
    wk = wid % C::WK; wn = (wid / C::WK) % C::WN; wm = wid / (C::WK * C::WN);
    const int fr = lane & 15, fq = lane >> 4;
    const int arow = wm * (C::BM / C::WM) + fr;
#pragma unroll
    for (int s = 0; s < 2; ++s) {
      const int ks = (C::WK == 2) ? wk : s;
      if constexpr (C::AMODE == 0) rdA[s] = ((ks * 4 + fq) * C::BM + arow) * 16;
      else rdA[s] = arow * 128 + (((ks * 4 + fq) ^ ((fr >> 1) & 7)) * 16);
    }
    if constexpr (C::BMODE <= 1) {
      const int brow = wn * (C::BN / C::WN) + fr;
#pragma unroll
      for (int s = 0; s < 2; ++s) {
        const int ks = (C::WK == 2) ? wk : s;
        if constexpr (C::BMODE == 0) rdB[s] = C::ABYTES + ((ks * 4 + fq) * C::BN + brow) * 16;
        else rdB[s] = C::ABYTES + brow * 128 + (((ks * 4 + fq) ^ ((fr >> 1) & 7)) * 16);
      }
    } else if constexpr (C::BMODE == 2) {
      const int g = lane >> 4, q = (lane >> 2) & 3, p = lane & 3;
      const int ks0 = (C::WK == 2) ? wk : 0;
#pragma unroll
      for (int t = 0; t < 2; ++t) rdB[t] = C::ABYTES + (32 * ks0 + 8 * g + 4 * (t ^ (g & 1)) + q) * 160 + p * 8 + wn * (C::BN / C::WN) * 2;
    } else {
      const int g = lane >> 4, q = (lane >> 2) & 3, p = lane & 3;
      const int ks0 = (C::WK == 2) ? wk : 0;
#pragma unroll
      for (int j = 0; j < 2; ++j) {
        const int jj = 2 * wn + j;
        rdB[j] = C::ABYTES + (32 * ks0 + 8 * g + q) * 128 + ((((jj ^ (((g & 1) << 1) | (q >> 1))) << 1) | (p >> 1)) * 16) + 8 * (p & 1);
      }
    }
  }

  __device__ __forceinline__ void read(Frags<C>& f, unsigned buf) {
    LAS char* b = lds + buf;
#pragma unroll
    for (int s = 0; s < C::KSW; ++s) {
      if constexpr (C::BMODE <= 1) {
        constexpr int step = (C::BMODE == 0) ? 256 : 2048;
#pragma unroll
        for (int n = 0; n < C::NF; ++n) f.b[s][n] = *(const LAS bf16x8*)(b + rdB[s] + n * step);
      } else if constexpr (C::BMODE == 2) {
#pragma unroll
        for (int n = 0; n < C::NF; ++n) {
          bf16x4 lo = __builtin_amdgcn_ds_read_tr16_b64_v4i16((LAS bf16x4*)(b + rdB[0] + s * 5120 + n * 32));
          bf16x4 hi = __builtin_amdgcn_ds_read_tr16_b64_v4i16((LAS bf16x4*)(b + rdB[1] + s * 5120 + n * 32));
          f.b[s][n] = __builtin_shufflevector(lo, hi, 0, 1, 2, 3, 4, 5, 6, 7);
        }
      } else {
#pragma unroll
        for (int n = 0; n < C::NF; ++n) {
          const int j = n & 1, mat = n >> 1;
          bf16x4 lo = __builtin_amdgcn_ds_read_tr16_b64_v4i16((LAS bf16x4*)(b + rdB[j] + mat * (C::BK * 128) + s * 4096));
          bf16x4 hi = __builtin_amdgcn_ds_read_tr16_b64_v4i16((LAS bf16x4*)(b + rdB[j] + mat * (C::BK * 128) + s * 4096 + 512));
          f.b[s][n] = __builtin_shufflevector(lo, hi, 0, 1, 2, 3, 4, 5, 6, 7);
        }
      }
      constexpr int astep = (C::AMODE == 0) ? 256 : 2048;
#pragma unroll
      for (int m = 0; m < C::MF; ++m) f.a[s][m] = *(const LAS bf16x8*)(b + rdA[s] + m * astep);
    }
  }

  __device__ __forceinline__ void mma(f32x4 (&acc)[C::MF][C::NF], const Frags<C>& f) {
#pragma unroll
    for (int s = 0; s < C::KSW; ++s)
#pragma unroll
      for (int m = 0; m < C::MF; ++m)
#pragma unroll
        for (int n = 0; n < C::NF; ++n)
          acc[m][n] = __builtin_amdgcn_mfma_f32_16x16x32_bf16(f.b[s][n], f.a[s][m], acc[m][n], 0, 0, 0);
  }
  static constexpr int NRD = C::KSW * (C::MF + (C::BMODE >= 2 ? 2 : 1) * C::NF);
  static constexpr int NMM = C::KSW * C::MF * C::NF;
  __device__ __forceinline__ void interleave() {
    constexpr int RPM = (NRD + NMM - 3) / (NMM - 2);
#pragma unroll
    for (int i = 0; i < NMM; ++i) {
      __builtin_amdgcn_sched_group_barrier(0x100, RPM, 0);
      __builtin_amdgcn_sched_group_barrier(0x008, 1, 0);
    }
  }
  __device__ __forceinline__ void step(f32x4 (&acc)[C::MF][C::NF], Frags<C>& cur, Frags<C>& nxt, unsigned nb) {
    wait_lgkm0(); bar();
    __builtin_amdgcn_sched_barrier(0);
    read(nxt, nb);
    mma(acc, cur);
    interleave();
    __builtin_amdgcn_sched_barrier(0);
  }

  __device__ __forceinline__ void run(f32x4 (&acc)[C::MF][C::NF]) {
    Frags<C> f0, f1;
    bar();
    if (wid == 0) { STAMP(C::KID, 1); CSTAMP(C::KID, 8); }
    read(f0, 0);
    unsigned nb = 0;
#pragma unroll 1
    for (int k = 0; k + 2 < C::NK; k += 2) {
      nb += C::STAGE; if (nb == C::LDS) nb = 0;
      step(acc, f0, f1, nb);
      nb += C::STAGE; if (nb == C::LDS) nb = 0;
      step(acc, f1, f0, nb);
    }
    if constexpr (C::NK % 2 == 0) {
      nb += C::STAGE; if (nb == C::LDS) nb = 0;
      step(acc, f0, f1, nb);
      mma(acc, f1);
    } else {
      mma(acc, f0);
    }
    if (wid == 0) { STAMP(C::KID, 2); CSTAMP(C::KID, 9); }
  }

  __device__ __forceinline__ void reduce_k(f32x4 (&acc)[C::MF][C::NF]) {
    static_assert(C::WK == 2 && C::MF == 2, "");
    wait_lgkm0(); bar();
    LAS f32x4* x = (LAS f32x4*)lds;
#pragma unroll
    for (int n = 0; n < C::NF; ++n) x[(wid * C::NF + n) * 64 + lane] = wk ? acc[0][n] : acc[1][n];
    wait_lgkm0(); bar();
#pragma unroll
    for (int n = 0; n < C::NF; ++n) {
      const f32x4 o = x[((wid ^ 1) * C::NF + n) * 64 + lane];
      acc[0][n] = (wk ? acc[1][n] : acc[0][n]) + o;
    }
  }
};

#define GEMM_BODY(C, SRC, src)                                                          \
  const int wid_ = __builtin_amdgcn_readfirstlane(threadIdx.x >> 6), lane_ = threadIdx.x & 63; \
  if (wid_ == 0) STAMP(C::KID, 0);                                                      \
  if (wid_ >= C::NC) { Loader<C, SRC> ld((LAS char*)smem, src, wid_ - C::NC, lane_); ld.run(); return; } \
  Consumer<C> L((LAS char*)smem, wid_, lane_);                                          \
  f32x4 acc[C::MF][C::NF];                                                              \
  _Pragma("unroll") for (int m = 0; m < C::MF; ++m)                                     \
  _Pragma("unroll") for (int n = 0; n < C::NF; ++n) acc[m][n] = (f32x4){0.f, 0.f, 0.f, 0.f};

__global__ __launch_bounds__(512) void k0_ln(const u16* __restrict__ x, u16* __restrict__ xnI,
                                             const u16* __restrict__ wg, const u16* __restrict__ wu, float* __restrict__ cs,
                                             const u16* __restrict__ sre, const u16* __restrict__ sim,
                                             const u16* __restrict__ hre, const u16* __restrict__ him, const u16* __restrict__ wd) {
  __shared__ __attribute__((aligned(16))) char smem[8 * 1296 + 64];
  const int tid = threadIdx.x, wid = tid >> 6, lane = tid & 63;
  if (wid == 0) STAMP(0, 0);
  if (blockIdx.x < 256) {
    const int koct = blockIdx.x, row = koct * 8 + wid;
    const u16* xr = x + row * D_;
    bf16x8 v0 = *(const bf16x8*)(xr + lane * 8);
    bf16x8 v1 = {0, 0, 0, 0, 0, 0, 0, 0};
    if (lane < 16) v1 = *(const bf16x8*)(xr + 512 + lane * 8);
    float f[16];
#pragma unroll
    for (int i = 0; i < 8; ++i) { f[i] = bf2f((u16)v0[i]); f[8 + i] = bf2f((u16)v1[i]); }
    float s = 0.f;
#pragma unroll
    for (int i = 0; i < 16; ++i) s += f[i];
#pragma unroll
    for (int o = 32; o >= 1; o >>= 1) s += __shfl_xor(s, o);
    const float mean = s * (1.0f / D_);
    float q = 0.f;
#pragma unroll
    for (int i = 0; i < 8; ++i) { const float d = f[i] - mean; q += d * d; }
    if (lane < 16) {
#pragma unroll
      for (int i = 8; i < 16; ++i) { const float d = f[i] - mean; q += d * d; }
    }
#pragma unroll
    for (int o = 32; o >= 1; o >>= 1) q += __shfl_xor(q, o);
    const float mb = rbf(mean), vb = rbf(q * (1.0f / D_));
    const float den = rbf(sqrtf(rbf(vb + 1e-5f)));
    const float rden = 1.0f / den;
    u32x4 w0, w1;
#pragma unroll
    for (int i = 0; i < 4; ++i) {
      w0[i] = pk2(rbf(f[2 * i] - mb) * rden, rbf(f[2 * i + 1] - mb) * rden);
      w1[i] = pk2(rbf(f[8 + 2 * i] - mb) * rden, rbf(f[9 + 2 * i] - mb) * rden);
    }
    char* sr = smem + wid * 1296;
    *(u32x4*)(sr + lane * 16) = w0;
    if (lane < 16) *(u32x4*)(sr + 1024 + lane * 16) = w1;
    __syncthreads();
    for (int d = tid; d < D_; d += 512) {
      bf16x8 o;
#pragma unroll
      for (int r = 0; r < 8; ++r) o[r] = *(const short*)(smem + r * 1296 + d * 2);
      const int j = d / 80, n = d % 80;
      *(bf16x8*)(xnI + (((size_t)j * 256 + koct) * 80 + n) * 8) = o;
    }
  } else if (blockIdx.x >= 312) {
    const int b = blockIdx.x - 312;
    LAS char* scr = (LAS char*)smem + __builtin_amdgcn_readfirstlane(wid) * 256;
    if (b < 128) {
      const int xcd = b & 7, part = b >> 3;
      const char* base = (const char*)((part & 8) ? sim : sre) + (size_t)xcd * (256 * S_ * 2) + (size_t)(part & 7) * 131072;
#pragma unroll
      for (int i = 0; i < 4; ++i) touch(base + (i * 512 + tid) * 64, scr);
    } else {
      const int c = b - 128;
      const int sec = c * 2048 + tid;
#pragma unroll
      for (int i = 0; i < 4; ++i) {
        const int s = sec + i * 512;
        const char* p = s < 12800 ? (const char*)hre + s * 64 : (s < 25600 ? (const char*)him + (s - 12800) * 64 : (const char*)wd + (s - 25600) * 64);
        if (s < 25600 + 34560) touch(p, scr);
      }
    }
    wait_vm<0>();
  } else if (blockIdx.x < 310) {
    const int b = blockIdx.x - 256, mat = b / 27, c0 = (b % 27) * 64;
    const u16* w = (mat ? wu : wg) + c0 + (tid & 7) * 8;
    const int rg = tid >> 3;
    float a[8] = {0, 0, 0, 0, 0, 0, 0, 0};
    bf16x8 v[10];
#pragma unroll
    for (int i = 0; i < 10; ++i) v[i] = *(const bf16x8*)(w + (size_t)(rg + 64 * i) * F_);
#pragma unroll
    for (int i = 0; i < 10; ++i)
#pragma unroll
      for (int e = 0; e < 8; ++e) a[e] += bf2f((u16)v[i][e]);
#pragma unroll
    for (int e = 0; e < 8; ++e) {
      a[e] += __shfl_xor(a[e], 8); a[e] += __shfl_xor(a[e], 16); a[e] += __shfl_xor(a[e], 32);
    }
    float* sf = (float*)smem;
    if (lane < 8) {
#pragma unroll
      for (int e = 0; e < 8; ++e) sf[wid * 64 + lane * 8 + e] = a[e];
    }
    __syncthreads();
    if (tid < 64) {
      float t = 0.f;
#pragma unroll
      for (int w8 = 0; w8 < 8; ++w8) t += sf[w8 * 64 + tid];
      cs[mat * F_ + c0 + tid] = t;
    }
  }
  if (wid == 0) { wait_vm<0>(); STAMP(0, 3); }
}

using C1 = Cfg<1, 128, 80, 64, 4, 1, 1, 2, 6, 32, 1, 0>;
struct Src1 {
  const char* a; const char* b; const char* n0; const char* n1; int idx; static constexpr int ldA = S_ * 2, ldB = 0;
  static constexpr int NPF = PF_N ? 7 : 0;
  __device__ __forceinline__ void prefetch(int lw, int lane, LAS char* scr) const {
    const char* p = lw ? n1 : n0;
#pragma unroll
    for (int i = 0; i < NPF; ++i) { int s = idx * 400 + i * 64 + lane; s = s < (idx + 1) * 400 ? s : idx * 400; touch(p + s * 64, scr); }
  }
  __device__ __forceinline__ const char* baseA(int k) const { return a + k * 128; }
  __device__ __forceinline__ const char* baseB(int k, int) const { return b + k * (80 * 128); }
};
__global__ __launch_bounds__(C1::NT) void k1_seq(const u16* __restrict__ sre, const u16* __restrict__ sim,
                                                 const u16* __restrict__ xnI, u16* __restrict__ pqI,
                                                 const u16* __restrict__ hre, const u16* __restrict__ him) {
  __shared__ __attribute__((aligned(1024))) char smem[C1::LDS + 256];
  const int bid = blockIdx.x, xcd = bid & 7, idx = bid >> 3;
  const int j = idx & 7, mq = idx >> 3;
  const int isQ = mq >> 1, t0 = xcd * 256 + (mq & 1) * 128;
  Src1 src{(const char*)((isQ ? sim : sre) + (size_t)t0 * S_), (const char*)(xnI + (size_t)j * 256 * 80 * 8),
           (const char*)hre, (const char*)him, idx};
  GEMM_BODY(C1, Src1, src)
  L.run(acc);
  const int fr = L.lane & 15, fq = L.lane >> 4;
  const float sg = isQ ? -1.f : 1.f;
#pragma unroll
  for (int m = 0; m < C1::MF; ++m) {
    const int t = t0 + L.wm * 32 + m * 16 + fr, mt = t >> 6, r = t & 63;
#pragma unroll
    for (int n = 0; n < C1::NF; ++n) {
      const int koct = isQ * 80 + j * 10 + n * 2 + (fq >> 1);
      u32x2 w; w[0] = pk2(sg * acc[m][n][0], sg * acc[m][n][1]); w[1] = pk2(sg * acc[m][n][2], sg * acc[m][n][3]);
      *(u32x2*)(pqI + (((size_t)mt * 160 + koct) * 64 + r) * 8 + (fq & 1) * 4) = w;
    }
  }
  if (wid_ == 0) { wait_vm<0>(); STAMP(1, 3); }
}

using C2 = Cfg<2, 64, 80, 64, 2, 1, 2, 2, 8, 20, 0, 1>;
struct Src2 {
  const char* a; const char* bre; const char* bim; const char* n0; const char* n1; int idx, half; static constexpr int ldA = 0, ldB = D_ * 2;
  static constexpr int NPF = PF_N ? 9 : 0;
  __device__ __forceinline__ void prefetch(int lw, int lane, LAS char* scr) const {
    const char* p = (lw ? n1 : n0) + half * (14 * 128);
    const int per = half ? 26 : 28;
#pragma unroll
    for (int i = 0; i < NPF; ++i) { int s = i * 64 + lane; s = s < 20 * per ? s : 0; const int r = s / per, c = s % per; touch(p + (size_t)(idx * 20 + r) * (F_ * 2) + c * 64, scr); }
  }
  __device__ __forceinline__ const char* baseA(int k) const { return a + k * (64 * 128); }
  __device__ __forceinline__ const char* baseB(int k, int) const { return k < 10 ? bre + k * 128 : bim + (k - 10) * 128; }
};
__global__ __launch_bounds__(C2::NT) void k2_hid(const u16* __restrict__ pqI, const u16* __restrict__ hre, const u16* __restrict__ him,
                                                 const u16* __restrict__ x, u16* __restrict__ hI, f32x2* __restrict__ stats,
                                                 const u16* __restrict__ wg, const u16* __restrict__ wu) {
  __shared__ __attribute__((aligned(1024))) char smem[C2::LDS + 256];
  const int bid = blockIdx.x, xcd = bid & 7, idx = bid >> 3;
  const int j = idx & 7, mt = xcd * 4 + (idx >> 3);
  Src2 src{(const char*)(pqI + (size_t)mt * 160 * 64 * 8), (const char*)(hre + (size_t)j * 80 * D_), (const char*)(him + (size_t)j * 80 * D_),
           (const char*)wg, (const char*)wu, idx, xcd & 1};
  GEMM_BODY(C2, Src2, src)
  const int fr = L.lane & 15, fq = L.lane >> 4;
  const int t = mt * 64 + L.wm * 32 + L.wk * 16 + fr;
  bf16x4 xv[C2::NF];
#pragma unroll
  for (int n = 0; n < C2::NF; ++n) xv[n] = *(const bf16x4*)(x + (size_t)t * D_ + j * 80 + n * 16 + fq * 4);
#pragma unroll
  for (int n = 0; n < C2::NF; ++n) asm volatile("" : "+v"(xv[n]));
  L.run(acc);
  L.reduce_k(acc);
  float hv[C2::NF][4];
  float s = 0.f;
#pragma unroll
  for (int n = 0; n < C2::NF; ++n) {
    u32x2 w;
    w[0] = pk2(acc[0][n][0] + bf2f((u16)xv[n][0]), acc[0][n][1] + bf2f((u16)xv[n][1]));
    w[1] = pk2(acc[0][n][2] + bf2f((u16)xv[n][2]), acc[0][n][3] + bf2f((u16)xv[n][3]));
    hv[n][0] = __uint_as_float(w[0] << 16); hv[n][1] = __uint_as_float(w[0] & 0xffff0000u);
    hv[n][2] = __uint_as_float(w[1] << 16); hv[n][3] = __uint_as_float(w[1] & 0xffff0000u);
    s += (hv[n][0] + hv[n][1]) + (hv[n][2] + hv[n][3]);
    const int koct = j * 10 + n * 2 + (fq >> 1);
    *(u32x2*)(hI + (((size_t)(t >> 8) * 80 + koct) * 256 + (t & 255)) * 8 + (fq & 1) * 4) = w;
  }
  s += __shfl_xor(s, 16); s += __shfl_xor(s, 32);
  const float mean = s * (1.0f / 80.0f);
  float q = 0.f;
#pragma unroll
  for (int n = 0; n < C2::NF; ++n)
#pragma unroll
    for (int r = 0; r < 4; ++r) { const float d = hv[n][r] - mean; q += d * d; }
  q += __shfl_xor(q, 16); q += __shfl_xor(q, 32);
  if (fq == 0) stats[t * 8 + j] = (f32x2){mean, q};
  if (wid_ == 0) { wait_vm<0>(); STAMP(2, 3); }
}

using C3 = Cfg<3, 256, 128, 32, 2, 2, 1, 2, 6, 20, 0, 3>;
struct Src3 {
  const char* a; const char* bg; const char* bu; const char* n0; int idx; static constexpr int ldA = 0, ldB = F_ * 2;
  static constexpr int NPF = PF_N ? 11 : 0;
  __device__ __forceinline__ void prefetch(int lw, int lane, LAS char* scr) const {
#pragma unroll
    for (int i = 0; i < NPF; ++i) { int s = idx * 1344 + lw * 672 + i * 64 + lane; s = (i * 64 + lane < 672 && s < 34560) ? s : 0; touch(n0 + s * 64, scr); }
  }
  __device__ __forceinline__ const char* baseA(int k) const { return a + k * (256 * 64); }
  __device__ __forceinline__ const char* baseB(int k, int up) const { return (up ? bu : bg) + (size_t)k * (32 * F_ * 2); }
};
__global__ __launch_bounds__(C3::NT) void k3_glu(const u16* __restrict__ hI, const u16* __restrict__ wg, const u16* __restrict__ wu,
                                                 const f32x2* __restrict__ stats, const float* __restrict__ cs, u16* __restrict__ hhI,
                                                 const u16* __restrict__ wd) {
  __shared__ __attribute__((aligned(1024))) char smem[C3::LDS + 256 + 2048 + 512];
  const int bid = blockIdx.x, xcd = bid & 7, idx = bid >> 3;
  const int mt = (xcd & 6) + (idx & 1), j = (xcd & 1) * 14 + (idx >> 1);
  if (j >= 27) return;
  Src3 src{(const char*)(hI + (size_t)mt * 80 * 256 * 8), (const char*)(wg + j * 64), (const char*)(wu + j * 64), (const char*)wd, idx};
  GEMM_BODY(C3, Src3, src)
  const int fr = L.lane & 15, fq = L.lane >> 4;
  LAS f32x2* rs = (LAS f32x2*)((LAS char*)smem + C3::LDS + 256);
  LAS float* csl = (LAS float*)((LAS char*)smem + C3::LDS + 256 + 2048);
  {
    const int r = wid_ * 64 + lane_;
    const f32x4* p = (const f32x4*)(stats + (mt * 256 + r) * 8);
    const f32x4 p0 = p[0], p1 = p[1], p2 = p[2], p3 = p[3];
    float cv = 0.f;
    if (wid_ < 2) cv = cs[wid_ * F_ + j * 64 + lane_];
    const float mean = ((p0[0] + p0[2]) + (p1[0] + p1[2]) + (p2[0] + p2[2]) + (p3[0] + p3[2])) * 0.125f;
    float q = (p0[1] + p0[3]) + (p1[1] + p1[3]) + (p2[1] + p2[3]) + (p3[1] + p3[3]);
    float dd = 0.f, d;
    d = p0[0] - mean; dd += d * d; d = p0[2] - mean; dd += d * d; d = p1[0] - mean; dd += d * d; d = p1[2] - mean; dd += d * d;
    d = p2[0] - mean; dd += d * d; d = p2[2] - mean; dd += d * d; d = p3[0] - mean; dd += d * d; d = p3[2] - mean; dd += d * d;
    q += 80.0f * dd;
    const float vb = rbf(q * (1.0f / D_));
    rs[r] = (f32x2){rbf(mean), 1.0f / rbf(sqrtf(rbf(vb + 1e-5f)))};
    if (wid_ < 2) csl[wid_ * 64 + lane_] = cv;
    wait_lgkm0();
  }
  L.run(acc);
#pragma unroll
  for (int n = 0; n < 2; ++n) {
    const int fl = L.wn * 32 + n * 16 + fq * 4, f0 = j * 64 + fl;
    const f32x4 cg = *(const LAS f32x4*)(csl + fl), cu = *(const LAS f32x4*)(csl + 64 + fl);
#pragma unroll
    for (int m = 0; m < 8; ++m) {
      const int rl = L.wm * 128 + m * 16 + fr, t = mt * 256 + rl;
      const f32x2 st = rs[rl];
      float o[4];
#pragma unroll
      for (int r = 0; r < 4; ++r) {
        const float g = (acc[m][n][r] - st[0] * cg[r]) * st[1];
        const float u = (acc[m][n + 2][r] - st[0] * cu[r]) * st[1];
        o[r] = u * __builtin_amdgcn_rcpf(1.0f + __expf(-g));
      }
      u32x2 w; w[0] = pk2(o[0], o[1]); w[1] = pk2(o[2], o[3]);
      *(u32x2*)(hhI + (((size_t)(t >> 6) * 216 + (f0 >> 3)) * 64 + (t & 63)) * 8 + (fq & 1) * 4) = w;
    }
  }
  if (wid_ == 0) { wait_vm<0>(); STAMP(3, 3); }
}

using C4 = Cfg<4, 64, 80, 64, 2, 1, 2, 2, 8, 27, 0, 2>;
struct Src4 {
  const char* a; const char* b; static constexpr int ldA = 0, ldB = D_ * 2;
  static constexpr int NPF = 0;
  __device__ __forceinline__ void prefetch(int, int, LAS char*) const {}
  __device__ __forceinline__ const char* baseA(int k) const { return a + k * (64 * 128); }
  __device__ __forceinline__ const char* baseB(int k, int) const { return b + (size_t)k * (64 * D_ * 2); }
};
__global__ __launch_bounds__(C4::NT) void k4_down(const u16* __restrict__ hhI, const u16* __restrict__ wd,
                                                  const u16* __restrict__ hI, u16* __restrict__ out) {
  __shared__ __attribute__((aligned(1024))) char smem[C4::LDS + 256];
  const int bid = blockIdx.x, xcd = bid & 7, idx = bid >> 3;
  const int j = idx & 7, mt = xcd * 4 + (idx >> 3);
  Src4 src{(const char*)(hhI + (size_t)mt * 216 * 64 * 8), (const char*)(wd + j * 80)};
  GEMM_BODY(C4, Src4, src)
  const int fr = L.lane & 15, fq = L.lane >> 4;
  const int t = mt * 64 + L.wm * 32 + L.wk * 16 + fr;
  bf16x4 hv[C4::NF];
#pragma unroll
  for (int n = 0; n < C4::NF; ++n) {
    const int koct = j * 10 + n * 2 + (fq >> 1);
    hv[n] = *(const bf16x4*)(hI + (((size_t)(t >> 8) * 80 + koct) * 256 + (t & 255)) * 8 + (fq & 1) * 4);
  }
#pragma unroll
  for (int n = 0; n < C4::NF; ++n) asm volatile("" : "+v"(hv[n]));
  L.run(acc);
  L.reduce_k(acc);
#pragma unroll
  for (int n = 0; n < C4::NF; ++n) {
    u32x2 w;
    w[0] = pk2(acc[0][n][0] + bf2f((u16)hv[n][0]), acc[0][n][1] + bf2f((u16)hv[n][1]));
    w[1] = pk2(acc[0][n][2] + bf2f((u16)hv[n][2]), acc[0][n][3] + bf2f((u16)hv[n][3]));
    *(u32x2*)(out + (size_t)t * D_ + j * 80 + n * 16 + fq * 4) = w;
  }
  if (wid_ == 0) { wait_vm<0>(); STAMP(4, 3); }
}

extern "C" void kernel_launch(void* const* d_in, const int* in_sizes, int n_in, void* d_out, int out_size,
                              void* d_ws, size_t ws_size, hipStream_t stream) {
  const u16* x = (const u16*)d_in[0];
  const u16* sre = (const u16*)d_in[1];
  const u16* sim = (const u16*)d_in[2];
  const u16* hre = (const u16*)d_in[3];
  const u16* him = (const u16*)d_in[4];
  const u16* wg = (const u16*)d_in[6];
  const u16* wu = (const u16*)d_in[7];
  const u16* wd = (const u16*)d_in[8];
  char* ws = (char*)d_ws;
  u16* xnI = (u16*)(ws);
  u16* pqI = (u16*)(ws + (4u << 20));
  u16* hI = (u16*)(ws + (12u << 20));
  u16* hhI = (u16*)(ws + (16u << 20));
  f32x2* stats = (f32x2*)(ws + (24u << 20));
  float* cs = (float*)(ws + (25u << 20));
  k0_ln<<<312 + 128 + 30, 512, 0, stream>>>(x, xnI, wg, wu, cs, sre, sim, hre, him, wd);
  k1_seq<<<256, C1::NT, 0, stream>>>(sre, sim, xnI, pqI, hre, him);
  k2_hid<<<256, C2::NT, 0, stream>>>(pqI, hre, him, x, hI, stats, wg, wu);
  k3_glu<<<224, C3::NT, 0, stream>>>(hI, wg, wu, stats, cs, hhI, wd);
  k4_down<<<256, C4::NT, 0, stream>>>(hhI, wd, hI, (u16*)d_out);
}
```

```cpp
#include <hip/hip_runtime.h>
#include <hip/hip_bf16.h>
#define S_MODE 3
#define S_SPLIT 16
#define K1_AUXA 0
#define K1_AUXB 0

typedef unsigned short u16;
typedef short bf16x8 __attribute__((ext_vector_type(8)));
typedef short bf16x4 __attribute__((ext_vector_type(4)));
typedef __bf16 bf16x2n __attribute__((ext_vector_type(2)));
typedef float f32x4 __attribute__((ext_vector_type(4)));
typedef float f32x2 __attribute__((ext_vector_type(2)));
typedef unsigned u32x2 __attribute__((ext_vector_type(2)));
typedef unsigned u32x4 __attribute__((ext_vector_type(4)));
#define LAS __attribute__((address_space(3)))
#define GAS __attribute__((address_space(1)))

constexpr int S_ = 2048, D_ = 640, F_ = 1728;

__device__ __forceinline__ float bf2f(u16 v) { return __uint_as_float(((unsigned)v) << 16); }
__device__ __forceinline__ unsigned pk2(float lo, float hi) {
  unsigned r; asm("v_cvt_pk_bf16_f32 %0, %1, %2" : "=v"(r) : "v"(lo), "v"(hi)); return r;
}
__device__ __forceinline__ float rbf(float v) { return __uint_as_float(pk2(v, v) << 16); }

#define CSTAMP(kid, i)
#define STAMP(kid, i)
template <int N> __device__ __forceinline__ void wait_vm() { asm volatile("s_waitcnt vmcnt(%0)" ::"n"(N) : "memory"); }
__device__ __forceinline__ void wait_lgkm0() { asm volatile("" ::: "memory"); __builtin_amdgcn_s_waitcnt(0xC07F); asm volatile("" ::: "memory"); }
__device__ __forceinline__ void bar() { asm volatile("" ::: "memory"); __builtin_amdgcn_s_barrier(); asm volatile("" ::: "memory"); }

template <int AUX> __device__ __forceinline__ void glds16(const char* g, LAS char* l) {
  __builtin_amdgcn_global_load_lds((const GAS void*)g, (LAS void*)l, 16, 0, AUX);
}
template <int AUX> __device__ __forceinline__ void touch(const char* g, LAS char* scratch) {
  __builtin_amdgcn_global_load_lds((const GAS void*)g, (LAS void*)scratch, 4, 0, AUX);
}
__device__ __forceinline__ float dot_ones(const bf16x8& v, float acc) {
  const u32x4 u = __builtin_bit_cast(u32x4, v);
  const bf16x2n ones = __builtin_bit_cast(bf16x2n, 0x3F803F80u);
#pragma unroll
  for (int i = 0; i < 4; ++i) acc = __builtin_amdgcn_fdot2_f32_bf16(__builtin_bit_cast(bf16x2n, u[i]), ones, acc, false);
  return acc;
}

template <int KID_, int BM_, int BN_, int BK_, int WM_, int WN_, int WK_, int NL_, int NS_, int NK_, int AMODE_, int BMODE_, int SWAP_, int EXTRA_>
struct Cfg {
  static constexpr int KID = KID_, BM = BM_, BN = BN_, BK = BK_, WM = WM_, WN = WN_, WK = WK_, NL = NL_, NS = NS_, NK = NK_;
  static constexpr int AMODE = AMODE_, BMODE = BMODE_, SWAP = SWAP_, EXTRA = EXTRA_;
  static constexpr int NC = WM * WN * WK, NW = NC + NL, NT = NW * 64;
  static constexpr int MF = BM / WM / 16, NF = BN / WN / 16, KSW = BK / 32 / WK;
  static constexpr int ABYTES = BM * BK * 2, BBYTES = BN * BK * 2, STAGE = ABYTES + BBYTES;
  static constexpr int PA = ABYTES / 1024, PB = BBYTES / 1024;
  static constexpr int GA = PA / NL, GB = PB / NL;
  static constexpr int G = GA + GB;
  static constexpr int LDS = NS * STAGE;
  static_assert(PA % NL == 0 && PB % NL == 0, "pieces");
  static_assert((NS - 2) * G <= 63, "vmcnt");
  static_assert(NS <= NK, "stages");
  static_assert(KSW >= 1, "ksw");
};

template <class C> struct Frags { bf16x8 a[C::KSW][C::MF]; bf16x8 b[C::KSW][C::NF]; };

template <class C, class Src>
struct Loader {
  LAS char* lds; const Src& src;
  int lw, lane;
  unsigned voffA[C::GA], voffB[C::GB];
  template <int ROWS> static __device__ __forceinline__ unsigned rowmajor(int p, int ld) {
    const int sub = p / (ROWS * 8), q = p % (ROWS * 8), row = q >> 3, c = (q & 7) ^ ((row >> 1) & 7);
    return row * ld + sub * 128 + c * 16;
  }
  __device__ __forceinline__ Loader(LAS char* lds_, const Src& s, int lw_, int lane_) : lds(lds_), src(s), lw(lw_), lane(lane_) {
#pragma unroll
    for (int i = 0; i < C::GA; ++i) {
      const int p = (i * C::NL + lw) * 64 + lane;
      if constexpr (C::AMODE == 0) voffA[i] = p * 16;
      else voffA[i] = rowmajor<C::BM>(p, src.ldA);
    }
#pragma unroll
    for (int i = 0; i < C::GB; ++i) {
      const int p = (i * C::NL + lw) * 64 + lane;
      if constexpr (C::BMODE == 0) voffB[i] = p * 16;
      else if constexpr (C::BMODE == 1) voffB[i] = rowmajor<C::BN>(p, src.ldB);
      else if constexpr (C::BMODE == 2) { const int kp = p / 10, c = p % 10, k = kp ^ (((kp >> 3) & 1) << 2); voffB[i] = k * src.ldB + c * 16; }
      else { const int q = p % (C::BK * 8), k = q >> 3, c = (q & 7) ^ ((((k >> 3) & 1) << 2) | (((k >> 1) & 1) << 1)); voffB[i] = k * src.ldB + c * 16; }
    }
  }
  __device__ __forceinline__ void issue(int k, unsigned buf) {
    const char* ga = src.baseA(k);
    LAS char* la = lds + buf + lw * 1024;
#pragma unroll
    for (int i = 0; i < C::GA; ++i) glds16<Src::AUXA>(ga + voffA[i], la + i * C::NL * 1024);
    LAS char* lb = la + C::ABYTES;
#pragma unroll
    for (int i = 0; i < C::GB; ++i) glds16<Src::AUXB>(src.baseB(k, i >= C::GB / 2) + voffB[i], lb + i * C::NL * 1024);
  }
  template <int K> __device__ __forceinline__ void tail() {
    if constexpr (K + 1 < C::NK) {
      constexpr int outst = (C::NS - 2 < C::NK - 2 - K) ? C::NS - 2 : C::NK - 2 - K;
      wait_vm<outst * C::G + Src::NPF>();
      bar();
      tail<K + 1>();
    }
  }
  __device__ __forceinline__ void run() {
#pragma unroll 1
    for (int s = 0; s < C::NS; ++s) issue(s, s * C::STAGE);
    constexpr int W0 = (C::NS - 1) * C::G > 63 ? 63 : (C::NS - 1) * C::G;
    wait_vm<W0>();
    bar();
    unsigned cb = 0;
#pragma unroll 1
    for (int k = 0; k < C::NK - C::NS; ++k) {
      wait_vm<(C::NS - 2) * C::G>(); bar();
      issue(k + C::NS, cb);
      cb += C::STAGE; if (cb == C::LDS) cb = 0;
    }
    src.prefetch(lw, lane, lds + C::LDS);
    tail<C::NK - C::NS>();
    if constexpr (C::WK == 2) { bar(); bar(); }
    src.after(lw, lane);
  }
};

template <class C>
struct Consumer {
  LAS char* lds;
  int wid, lane, wm, wn, wk;
  unsigned rdA[2], rdB[4];
  f32x4 accS[C::MF], accQ[C::MF];
  float bsum[C::NF];

  __device__ __forceinline__ Consumer(LAS char* lds_, int wid_, int lane_) : lds(lds_), wid(wid_), lane(lane_) {
    wk = wid % C::WK; wn = (wid / C::WK) % C::WN; wm = wid / (C::WK * C::WN);
    const int fr = lane & 15, fq = lane >> 4;
    const int arow = wm * (C::BM / C::WM) + fr;
    const int ss0 = wk * C::KSW;
#pragma unroll
    for (int s = 0; s < 2; ++s) {
      const int ss = ss0 + s;
      if constexpr (C::AMODE == 0) rdA[s] = ((ss0 * 4 + fq) * C::BM + arow) * 16;
      else rdA[s] = (ss0 >> 1) * (C::BM * 128) + arow * 128 + ((((ss & 1) * 4 + fq) ^ ((fr >> 1) & 7)) * 16);
    }
    if constexpr (C::BMODE <= 1) {
      const int brow = wn * (C::BN / C::WN) + fr;
#pragma unroll
      for (int s = 0; s < 2; ++s) {
        const int ss = ss0 + s;
        if constexpr (C::BMODE == 0) rdB[s] = C::ABYTES + ((ss0 * 4 + fq) * C::BN + brow) * 16;
        else rdB[s] = C::ABYTES + (ss0 >> 1) * (C::BN * 128) + brow * 128 + ((((ss & 1) * 4 + fq) ^ ((fr >> 1) & 7)) * 16);
      }
    } else if constexpr (C::BMODE == 2) {
      const int g = lane >> 4, q = (lane >> 2) & 3, p = lane & 3;
#pragma unroll
      for (int t = 0; t < 2; ++t) rdB[t] = C::ABYTES + (32 * ss0 + 8 * g + 4 * (t ^ (g & 1)) + q) * 160 + p * 8 + wn * (C::BN / C::WN) * 2;
    } else {
      const int g = lane >> 4, q = (lane >> 2) & 3, p = lane & 3;
#pragma unroll
      for (int j = 0; j < (C::BMODE == 4 ? 4 : 2); ++j) {
        const int jj = (C::BMODE == 4) ? j : 2 * wn + j;
        rdB[j] = C::ABYTES + (32 * ss0 + 8 * g + q) * 128 + ((((jj ^ (((g & 1) << 1) | (q >> 1))) << 1) | (p >> 1)) * 16) + 8 * (p & 1);
      }
    }
#pragma unroll
    for (int m = 0; m < C::MF; ++m) { accS[m] = (f32x4){0.f, 0.f, 0.f, 0.f}; accQ[m] = accS[m]; }
#pragma unroll
    for (int n = 0; n < C::NF; ++n) bsum[n] = 0.f;
  }

  __device__ __forceinline__ void read(Frags<C>& f, unsigned buf) {
    static_assert(C::KSW <= 2 || (C::AMODE == 0 && C::BMODE != 1), "row-major images: at most two substeps per wave");
    LAS char* b = lds + buf;
#pragma unroll
    for (int s = 0; s < C::KSW; ++s) {
      if constexpr (C::BMODE == 0) {
#pragma unroll
        for (int n = 0; n < C::NF; ++n) f.b[s][n] = *(const LAS bf16x8*)(b + rdB[0] + s * (C::BN * 64) + n * 256);
      } else if constexpr (C::BMODE == 1) {
#pragma unroll
        for (int n = 0; n < C::NF; ++n) f.b[s][n] = *(const LAS bf16x8*)(b + rdB[s] + n * 2048);
      } else if constexpr (C::BMODE == 2) {
#pragma unroll
        for (int n = 0; n < C::NF; ++n) {
          bf16x4 lo = __builtin_amdgcn_ds_read_tr16_b64_v4i16((LAS bf16x4*)(b + rdB[0] + s * 5120 + n * 32));
          bf16x4 hi = __builtin_amdgcn_ds_read_tr16_b64_v4i16((LAS bf16x4*)(b + rdB[1] + s * 5120 + n * 32));
          f.b[s][n] = __builtin_shufflevector(lo, hi, 0, 1, 2, 3, 4, 5, 6, 7);
        }
      } else {
#pragma unroll
        for (int n = 0; n < C::NF; ++n) {
          const int j = (C::BMODE == 4) ? n : (n & 1), mat = (C::BMODE == 4) ? 0 : (n >> 1);
          bf16x4 lo = __builtin_amdgcn_ds_read_tr16_b64_v4i16((LAS bf16x4*)(b + rdB[j] + mat * (C::BK * 128) + s * 4096));
          bf16x4 hi = __builtin_amdgcn_ds_read_tr16_b64_v4i16((LAS bf16x4*)(b + rdB[j] + mat * (C::BK * 128) + s * 4096 + 512));
          f.b[s][n] = __builtin_shufflevector(lo, hi, 0, 1, 2, 3, 4, 5, 6, 7);
        }
      }
      if constexpr (C::AMODE == 0) {
#pragma unroll
        for (int m = 0; m < C::MF; ++m) f.a[s][m] = *(const LAS bf16x8*)(b + rdA[0] + s * (C::BM * 64) + m * 256);
      } else {
#pragma unroll
        for (int m = 0; m < C::MF; ++m) f.a[s][m] = *(const LAS bf16x8*)(b + rdA[s] + m * 2048);
      }
    }
  }

  __device__ __forceinline__ void mma(f32x4 (&acc)[C::MF][C::NF], const Frags<C>& f) {
#pragma unroll
    for (int s = 0; s < C::KSW; ++s) {
#pragma unroll
      for (int m = 0; m < C::MF; ++m)
#pragma unroll
        for (int n = 0; n < C::NF; ++n) {
          if constexpr (C::SWAP) acc[m][n] = __builtin_amdgcn_mfma_f32_16x16x32_bf16(f.b[s][n], f.a[s][m], acc[m][n], 0, 0, 0);
          else acc[m][n] = __builtin_amdgcn_mfma_f32_16x16x32_bf16(f.a[s][m], f.b[s][n], acc[m][n], 0, 0, 0);
        }
      if constexpr (C::EXTRA & 1) {
        const bf16x8 ones = {0x3F80, 0x3F80, 0x3F80, 0x3F80, 0x3F80, 0x3F80, 0x3F80, 0x3F80};
#pragma unroll
        for (int m = 0; m < C::MF; ++m) {
          accS[m] = __builtin_amdgcn_mfma_f32_16x16x32_bf16(f.a[s][m], ones, accS[m], 0, 0, 0);
          accQ[m] = __builtin_amdgcn_mfma_f32_16x16x32_bf16(f.a[s][m], f.a[s][m], accQ[m], 0, 0, 0);
        }
      }
      if constexpr (C::EXTRA & 2) {
#pragma unroll
        for (int n = 0; n < C::NF; ++n) bsum[n] = dot_ones(f.b[s][n], bsum[n]);
      }
    }
  }
  static constexpr int NRD = C::KSW * (C::MF + (C::BMODE >= 2 ? 2 : 1) * C::NF);
  static constexpr int NMM = C::KSW * C::MF * (C::NF + ((C::EXTRA & 1) ? 2 : 0));
  static constexpr int NVA = (C::EXTRA & 2) ? C::KSW * C::NF * 4 : 0;
  __device__ __forceinline__ void interleave() {
    constexpr int RPM = (NRD + NMM - 3) / (NMM - 2);
    constexpr int VPM = (NVA + NMM - 1) / NMM;
#pragma unroll
    for (int i = 0; i < NMM; ++i) {
      __builtin_amdgcn_sched_group_barrier(0x100, RPM, 0);
      __builtin_amdgcn_sched_group_barrier(0x008, 1, 0);
      if constexpr (VPM > 0) __builtin_amdgcn_sched_group_barrier(0x002, VPM, 0);
    }
  }
  __device__ __forceinline__ void step(f32x4 (&acc)[C::MF][C::NF], Frags<C>& cur, Frags<C>& nxt, unsigned nb) {
    wait_lgkm0(); bar();
    __builtin_amdgcn_sched_barrier(0);
    read(nxt, nb);
    mma(acc, cur);
    interleave();
    __builtin_amdgcn_sched_barrier(0);
  }

  __device__ __forceinline__ void run(f32x4 (&acc)[C::MF][C::NF]) {
    Frags<C> f0, f1;
    bar();
    if (wid == 0) { STAMP(C::KID, 1); CSTAMP(C::KID, 8); }
    read(f0, 0);
    unsigned nb = 0;
#pragma unroll 1
    for (int k = 0; k + 2 < C::NK; k += 2) {
      nb += C::STAGE; if (nb == C::LDS) nb = 0;
      step(acc, f0, f1, nb);
      nb += C::STAGE; if (nb == C::LDS) nb = 0;
      step(acc, f1, f0, nb);
    }
    if constexpr (C::NK % 2 == 0) {
      nb += C::STAGE; if (nb == C::LDS) nb = 0;
      step(acc, f0, f1, nb);
      mma(acc, f1);
    } else {
      mma(acc, f0);
    }
    if (wid == 0) { STAMP(C::KID, 2); CSTAMP(C::KID, 9); }
  }

  static constexpr bool SPLM = (C::MF % 2 == 0);
  static constexpr int RM = SPLM ? C::MF / 2 : C::MF, RN = SPLM ? C::NF : C::NF / 2;
  __device__ __forceinline__ void reduce_k(f32x4 (&acc)[C::MF][C::NF], f32x4 (&red)[RM][RN]) {
    static_assert(C::WK == 2, "");
    wait_lgkm0(); bar();
    LAS f32x4* x = (LAS f32x4*)lds;
#pragma unroll
    for (int m = 0; m < RM; ++m)
#pragma unroll
      for (int n = 0; n < RN; ++n) {
        const f32x4 lo = acc[m][n], hi = SPLM ? acc[m + RM][n] : acc[m][n + RN];
        x[(wid * RM * RN + m * RN + n) * 64 + lane] = wk ? lo : hi;
        red[m][n] = wk ? hi : lo;
      }
    wait_lgkm0(); bar();
#pragma unroll
    for (int m = 0; m < RM; ++m)
#pragma unroll
      for (int n = 0; n < RN; ++n) red[m][n] += x[((wid ^ 1) * RM * RN + m * RN + n) * 64 + lane];
  }
};

#define GEMM_BODY(C, SRC, src)                                                          \
  const int wid_ = __builtin_amdgcn_readfirstlane(threadIdx.x >> 6), lane_ = threadIdx.x & 63; \
  if (wid_ == 0) STAMP(C::KID, 0);                                                      \
  if (wid_ >= C::NC) { Loader<C, SRC> ld((LAS char*)smem, src, wid_ - C::NC, lane_); ld.run(); return; } \
  Consumer<C> L((LAS char*)smem, wid_, lane_);                                          \
  f32x4 acc[C::MF][C::NF];                                                              \
  _Pragma("unroll") for (int m = 0; m < C::MF; ++m)                                     \
  _Pragma("unroll") for (int n = 0; n < C::NF; ++n) acc[m][n] = (f32x4){0.f, 0.f, 0.f, 0.f};

__global__ __launch_bounds__(512) void k0_ln(const u16* __restrict__ x, u16* __restrict__ xnI,
                                             const u16* __restrict__ sre, const u16* __restrict__ sim,
                                             const u16* __restrict__ hre, const u16* __restrict__ him) {
  __shared__ __attribute__((aligned(16))) char smem[8 * 1296 + 64];
  const int tid = threadIdx.x, wid = tid >> 6, lane = tid & 63;
  if (wid == 0) STAMP(0, 0);
  if (blockIdx.x < 256) {
    const int koct = blockIdx.x, row = koct * 8 + wid;
    const u16* xr = x + row * D_;
    bf16x8 v0 = *(const bf16x8*)(xr + lane * 8);
    bf16x8 v1 = {0, 0, 0, 0, 0, 0, 0, 0};
    if (lane < 16) v1 = *(const bf16x8*)(xr + 512 + lane * 8);
    float f[16];
#pragma unroll
    for (int i = 0; i < 8; ++i) { f[i] = bf2f((u16)v0[i]); f[8 + i] = bf2f((u16)v1[i]); }
    float s = 0.f;
#pragma unroll
    for (int i = 0; i < 16; ++i) s += f[i];
#pragma unroll
    for (int o = 32; o >= 1; o >>= 1) s += __shfl_xor(s, o);
    const float mean = s * (1.0f / D_);
    float q = 0.f;
#pragma unroll
    for (int i = 0; i < 8; ++i) { const float d = f[i] - mean; q += d * d; }
    if (lane < 16) {
#pragma unroll
      for (int i = 8; i < 16; ++i) { const float d = f[i] - mean; q += d * d; }
    }
#pragma unroll
    for (int o = 32; o >= 1; o >>= 1) q += __shfl_xor(q, o);
    const float mb = rbf(mean), vb = rbf(q * (1.0f / D_));
    const float den = rbf(sqrtf(rbf(vb + 1e-5f)));
    const float rden = 1.0f / den;
    u32x4 w0, w1;
#pragma unroll
    for (int i = 0; i < 4; ++i) {
      w0[i] = pk2(rbf(f[2 * i] - mb) * rden, rbf(f[2 * i + 1] - mb) * rden);
      w1[i] = pk2(rbf(f[8 + 2 * i] - mb) * rden, rbf(f[9 + 2 * i] - mb) * rden);
    }
    char* sr = smem + wid * 1296;
    *(u32x4*)(sr + lane * 16) = w0;
    if (lane < 16) *(u32x4*)(sr + 1024 + lane * 16) = w1;
    __syncthreads();
    for (int d = tid; d < D_; d += 512) {
      bf16x8 o;
#pragma unroll
      for (int r = 0; r < 8; ++r) o[r] = *(const short*)(smem + r * 1296 + d * 2);
      const int j = d / 80, n = d % 80;
      *(bf16x8*)(xnI + (((size_t)j * 256 + koct) * 80 + n) * 8) = o;
    }
  } else {
    const int b = blockIdx.x - 256;
    LAS char* scr = (LAS char*)smem + __builtin_amdgcn_readfirstlane(wid) * 256;
    if (b < 256) {
      const int xcd = b & 7, part = b >> 3;
      const char* base = (const char*)((part & 16) ? sim : sre) + (size_t)xcd * (256 * S_ * 2) + (size_t)(part & 15) * 65536;
      touch<0>(base + tid * 128, scr);
    } else {
      const int l = (b - 256) * 512 + tid;
      if (l < 12800) touch<0>(l < 6400 ? (const char*)hre + l * 128 : (const char*)him + (l - 6400) * 128, scr);
    }
    wait_vm<0>();
  }
  if (wid == 0) { wait_vm<0>(); STAMP(0, 3); }
}

#define K1_WK 1
#define K1_NS 6
using C1 = Cfg<1, 128, 80, 64, 4 / K1_WK, 1, K1_WK, 2, K1_NS, 32, 1, 0, 1, 0>;
struct Src1 {
  const char* a; const char* b; const u16* wg; const u16* wu; float* csp; int bid;
  static constexpr int ldA = S_ * 2, ldB = 0, AUXA = K1_AUXA, AUXB = K1_AUXB;
  static constexpr int NPF = 0;
  __device__ __forceinline__ const char* baseA(int k) const { return a + k * 128; }
  __device__ __forceinline__ const char* baseB(int k, int) const { return b + k * (80 * 128); }
  __device__ __forceinline__ void prefetch(int, int, LAS char*) const {}
  __device__ __forceinline__ void after(int lw, int lane) const {
    if (bid >= 216) return;
    const int c = bid >> 2, q = bid & 3, mat = c / 27, c0 = (c % 27) * 64;
    const u16* w = (mat ? wu : wg) + (size_t)(q * 160 + lw * 80 + (lane >> 3)) * F_ + c0 + (lane & 7) * 8;
    bf16x8 v[10];
#pragma unroll
    for (int i = 0; i < 10; ++i) v[i] = *(const bf16x8*)(w + (size_t)i * 8 * F_);
    float s[8] = {0, 0, 0, 0, 0, 0, 0, 0};
#pragma unroll
    for (int i = 0; i < 10; ++i)
#pragma unroll
      for (int e = 0; e < 8; ++e) s[e] += bf2f((u16)v[i][e]);
#pragma unroll
    for (int e = 0; e < 8; ++e) { s[e] += __shfl_xor(s[e], 8); s[e] += __shfl_xor(s[e], 16); s[e] += __shfl_xor(s[e], 32); }
    if (lane < 8) {
      float* o = csp + (size_t)(q * 2 + lw) * (2 * F_) + mat * F_ + c0 + lane * 8;
      *(f32x4*)o = (f32x4){s[0], s[1], s[2], s[3]};
      *(f32x4*)(o + 4) = (f32x4){s[4], s[5], s[6], s[7]};
    }
  }
};
__global__ __launch_bounds__(C1::NT) void k1_seq(const u16* __restrict__ sre, const u16* __restrict__ sim,
                                                 const u16* __restrict__ xnI, u16* __restrict__ pqI,
                                                 const u16* __restrict__ wg, const u16* __restrict__ wu, float* __restrict__ csp) {
  __shared__ __attribute__((aligned(1024))) char smem[C1::LDS + 256];
  const int bid = blockIdx.x, xcd = bid & 7, idx = bid >> 3;
  const int j = idx & 7, mq = idx >> 3;
  const int isQ = mq >> 1, t0 = xcd * 256 + (mq & 1) * 128;
  Src1 src{(const char*)((isQ ? sim : sre) + (size_t)t0 * S_), (const char*)(xnI + (size_t)j * 256 * 80 * 8), wg, wu, csp, bid};
  GEMM_BODY(C1, Src1, src)
  L.run(acc);
  const int fr = L.lane & 15, fq = L.lane >> 4;
  const float sg = isQ ? -1.f : 1.f;
  auto& red = acc;
  const int r0 = t0 + L.wm * 32;
#pragma unroll
  for (int m = 0; m < 2; ++m) {
    const int t = r0 + m * 16 + fr, mt = t >> 6, r = t & 63;
#pragma unroll
    for (int n = 0; n < 5; ++n) {
      const int koct = isQ * 80 + j * 10 + n * 2 + (fq >> 1);
      u32x2 w; w[0] = pk2(sg * red[m][n][0], sg * red[m][n][1]); w[1] = pk2(sg * red[m][n][2], sg * red[m][n][3]);
      *(u32x2*)(pqI + (((size_t)mt * 160 + koct) * 64 + r) * 8 + (fq & 1) * 4) = w;
    }
  }
  if (wid_ == 0) { wait_vm<0>(); STAMP(1, 3); }
}

#define K2_BK 128
using C2 = Cfg<2, 64, 80, K2_BK, 2, 1, 2, 2, 512 / K2_BK, 1280 / K2_BK, 0, 1, 1, 0>;
struct Src2 {
  const char* a; const char* bre; const char* bim; const char* wd; int bid;
  static constexpr int ldA = 0, ldB = D_ * 2, AUXA = 0, AUXB = 0;
  static constexpr int NH = 640 / K2_BK;
  __device__ __forceinline__ const char* baseA(int k) const { return a + k * (64 * K2_BK * 2); }
  __device__ __forceinline__ const char* baseB(int k, int) const { return k < NH ? bre + k * (K2_BK * 2) : bim + (k - NH) * (K2_BK * 2); }
  static constexpr int NPF = 1;
  __device__ __forceinline__ void prefetch(int lw, int lane, LAS char* scr) const {
    int l = (bid * 2 + lw) * 34 + lane; l = (lane < 34 && l < 17280) ? l : 0;
    touch<0>(wd + (size_t)l * 128, scr);
  }
  __device__ __forceinline__ void after(int, int) const { wait_vm<0>(); }
};
__global__ __launch_bounds__(C2::NT) void k2_hid(const u16* __restrict__ pqI, const u16* __restrict__ hre, const u16* __restrict__ him,
                                                 const u16* __restrict__ x, u16* __restrict__ hI, f32x2* __restrict__ stats,
                                                 const u16* __restrict__ wd) {
  __shared__ __attribute__((aligned(1024))) char smem[C2::LDS + 256];
  const int bid = blockIdx.x, xcd = bid & 7, idx = bid >> 3;
  const int j = idx & 7, mt = xcd * 4 + (idx >> 3);
  Src2 src{(const char*)(pqI + (size_t)mt * 160 * 64 * 8), (const char*)(hre + (size_t)j * 80 * D_), (const char*)(him + (size_t)j * 80 * D_),
           (const char*)wd, bid};
  GEMM_BODY(C2, Src2, src)
  const int fr = L.lane & 15, fq = L.lane >> 4;
  const int t = mt * 64 + L.wm * 32 + L.wk * 16 + fr;
  bf16x4 xv[C2::NF];
#pragma unroll
  for (int n = 0; n < C2::NF; ++n) xv[n] = *(const bf16x4*)(x + (size_t)t * D_ + j * 80 + n * 16 + fq * 4);
#pragma unroll
  for (int n = 0; n < C2::NF; ++n) asm volatile("" : "+v"(xv[n]));
  L.run(acc);
  f32x4 red[1][5];
  L.reduce_k(acc, red);
  float hv[C2::NF][4];
  float s = 0.f;
#pragma unroll
  for (int n = 0; n < C2::NF; ++n) {
    u32x2 w;
    w[0] = pk2(red[0][n][0] + bf2f((u16)xv[n][0]), red[0][n][1] + bf2f((u16)xv[n][1]));
    w[1] = pk2(red[0][n][2] + bf2f((u16)xv[n][2]), red[0][n][3] + bf2f((u16)xv[n][3]));
    hv[n][0] = __uint_as_float(w[0] << 16); hv[n][1] = __uint_as_float(w[0] & 0xffff0000u);
    hv[n][2] = __uint_as_float(w[1] << 16); hv[n][3] = __uint_as_float(w[1] & 0xffff0000u);
    s += (hv[n][0] + hv[n][1]) + (hv[n][2] + hv[n][3]);
    const int koct = j * 10 + n * 2 + (fq >> 1);
    *(u32x2*)(hI + (((size_t)(t >> 8) * 80 + koct) * 256 + (t & 255)) * 8 + (fq & 1) * 4) = w;
  }
  s += __shfl_xor(s, 16); s += __shfl_xor(s, 32);
  const float mean = s * (1.0f / 80.0f);
  float q = 0.f;
#pragma unroll
  for (int n = 0; n < C2::NF; ++n)
#pragma unroll
    for (int r = 0; r < 4; ++r) { const float d = hv[n][r] - mean; q += d * d; }
  q += __shfl_xor(q, 16); q += __shfl_xor(q, 32);
  if (fq == 0) stats[t * 8 + j] = (f32x2){mean, q};
  if (wid_ == 0) { wait_vm<0>(); STAMP(2, 3); }
}

using C3 = Cfg<3, 256, 128, 32, 2, 2, 1, 2, 6, 20, 0, 3, 1, 0>;
struct Src3 {
  const char* a; const char* bg; const char* bu;
  static constexpr int ldA = 0, ldB = F_ * 2, AUXA = 0, AUXB = 0;
  static constexpr int NPF = 0;
  __device__ __forceinline__ const char* baseA(int k) const { return a + k * (256 * 64); }
  __device__ __forceinline__ const char* baseB(int k, int up) const { return (up ? bu : bg) + (size_t)k * (32 * F_ * 2); }
  __device__ __forceinline__ void prefetch(int, int, LAS char*) const {}
  __device__ __forceinline__ void after(int, int) const {}
};
__global__ __launch_bounds__(C3::NT) void k3_glu(const u16* __restrict__ hI, const u16* __restrict__ wg, const u16* __restrict__ wu,
                                                 const f32x2* __restrict__ stats, const float* __restrict__ csp, u16* __restrict__ hhI) {
  __shared__ __attribute__((aligned(1024))) char smem[C3::LDS + 256 + 4096 + 512];
  const int bid = blockIdx.x, xcd = bid & 7, idx = bid >> 3;
  const int mt = (xcd & 6) + (idx & 1), j = (xcd & 1) * 14 + (idx >> 1);
  if (j >= 27) return;
  Src3 src{(const char*)(hI + (size_t)mt * 80 * 256 * 8), (const char*)(wg + j * 64), (const char*)(wu + j * 64)};
  GEMM_BODY(C3, Src3, src)
  const int fr = L.lane & 15, fq = L.lane >> 4;
  LAS f32x4* rs = (LAS f32x4*)((LAS char*)smem + C3::LDS + 256);
  LAS float* csl = (LAS float*)((LAS char*)smem + C3::LDS + 256 + 4096);
  {
    const int r = wid_ * 64 + lane_;
    const f32x4* p = (const f32x4*)(stats + (mt * 256 + r) * 8);
    const f32x4 p0 = p[0], p1 = p[1], p2 = p[2], p3 = p[3];
    float cv[8];
    if (wid_ < 2) {
#pragma unroll
      for (int i = 0; i < 8; ++i) cv[i] = csp[i * (2 * F_) + wid_ * F_ + j * 64 + lane_];
    }
    const float mean = ((p0[0] + p0[2]) + (p1[0] + p1[2]) + (p2[0] + p2[2]) + (p3[0] + p3[2])) * 0.125f;
    float q = (p0[1] + p0[3]) + (p1[1] + p1[3]) + (p2[1] + p2[3]) + (p3[1] + p3[3]);
    float dd = 0.f, d;
    d = p0[0] - mean; dd += d * d; d = p0[2] - mean; dd += d * d; d = p1[0] - mean; dd += d * d; d = p1[2] - mean; dd += d * d;
    d = p2[0] - mean; dd += d * d; d = p2[2] - mean; dd += d * d; d = p3[0] - mean; dd += d * d; d = p3[2] - mean; dd += d * d;
    q += 80.0f * dd;
    const float vb = rbf(q * (1.0f / D_));
    const float mb = rbf(mean), rd = 1.0f / rbf(sqrtf(rbf(vb + 1e-5f)));
    const float nl = -1.44269504089f * rd;
    rs[r] = (f32x4){mb, rd, nl, -mb * nl};
    if (wid_ < 2) csl[wid_ * 64 + lane_] = ((cv[0] + cv[1]) + (cv[2] + cv[3])) + ((cv[4] + cv[5]) + (cv[6] + cv[7]));
    wait_lgkm0();
  }
  L.run(acc);
#pragma unroll
  for (int n = 0; n < 2; ++n) {
    const int fl = L.wn * 32 + n * 16 + fq * 4, f0 = j * 64 + fl;
    const f32x4 cg = *(const LAS f32x4*)(csl + fl), cu = *(const LAS f32x4*)(csl + 64 + fl);
#pragma unroll
    for (int m = 0; m < 8; ++m) {
      const int rl = L.wm * 128 + m * 16 + fr, t = mt * 256 + rl;
      const f32x4 st = rs[rl];
      const f32x4 e2 = acc[m][n] * st[2] + cg * st[3];
      const f32x4 u = (acc[m][n + 2] - cu * st[0]) * st[1];
      f32x4 o;
#pragma unroll
      for (int r = 0; r < 4; ++r) o[r] = __builtin_amdgcn_rcpf(1.0f + __builtin_amdgcn_exp2f(e2[r]));
      o = o * u;
      u32x2 w; w[0] = pk2(o[0], o[1]); w[1] = pk2(o[2], o[3]);
      const int t4 = t / 96;
      *(u32x2*)(hhI + (((size_t)t4 * 216 + (f0 >> 3)) * 96 + (t - t4 * 96)) * 8 + (fq & 1) * 4) = w;
    }
  }
  if (wid_ == 0) { wait_vm<0>(); STAMP(3, 3); }
}

using C4 = Cfg<4, 96, 64, 64, 2, 1, 2, 2, 6, 27, 0, 4, 1, 0>;
struct Src4 {
  const char* a; const char* b;
  static constexpr int ldA = 0, ldB = D_ * 2, AUXA = 0, AUXB = 0;
  static constexpr int NPF = 0;
  __device__ __forceinline__ const char* baseA(int k) const { return a + k * (96 * 128); }
  __device__ __forceinline__ const char* baseB(int k, int) const { return b + (size_t)k * (64 * D_ * 2); }
  __device__ __forceinline__ void prefetch(int, int, LAS char*) const {}
  __device__ __forceinline__ void after(int, int) const {}
};
__global__ __launch_bounds__(C4::NT) void k4_down(const u16* __restrict__ hhI, const u16* __restrict__ wd,
                                                  const u16* __restrict__ hI, u16* __restrict__ out) {
  __shared__ __attribute__((aligned(1024))) char smem[C4::LDS + 256];
  const int bid = blockIdx.x, lin = (bid & 7) * 28 + (bid >> 3);
  if (lin >= 220) return;
  const int mt = lin / 10, j = lin % 10;
  Src4 src{(const char*)(hhI + (size_t)mt * 216 * 96 * 8), (const char*)(wd + j * 64)};
  GEMM_BODY(C4, Src4, src)
  const int fr = L.lane & 15, fq = L.lane >> 4;
  const int tb = mt * 96 + L.wm * 48 + fr, c0 = j * 64 + L.wk * 32 + fq * 4;
  bf16x4 hv[3][2];
#pragma unroll
  for (int m = 0; m < 3; ++m)
#pragma unroll
    for (int n = 0; n < 2; ++n) {
      const int t = min(tb + m * 16, S_ - 1), c = c0 + n * 16;
      hv[m][n] = *(const bf16x4*)(hI + (((size_t)(t >> 8) * 80 + (c >> 3)) * 256 + (t & 255)) * 8 + (c & 4));
    }
#pragma unroll
  for (int m = 0; m < 3; ++m)
#pragma unroll
    for (int n = 0; n < 2; ++n) asm volatile("" : "+v"(hv[m][n]));
  L.run(acc);
  f32x4 red[3][2];
  L.reduce_k(acc, red);
#pragma unroll
  for (int m = 0; m < 3; ++m)
#pragma unroll
    for (int n = 0; n < 2; ++n) {
      const int t = tb + m * 16;
      u32x2 w;
      w[0] = pk2(red[m][n][0] + bf2f((u16)hv[m][n][0]), red[m][n][1] + bf2f((u16)hv[m][n][1]));
      w[1] = pk2(red[m][n][2] + bf2f((u16)hv[m][n][2]), red[m][n][3] + bf2f((u16)hv[m][n][3]));
      if (t < S_) *(u32x2*)(out + (size_t)t * D_ + c0 + n * 16) = w;
    }
  if (wid_ == 0) { wait_vm<0>(); STAMP(4, 3); }
}

extern "C" void kernel_launch(void* const* d_in, const int* in_sizes, int n_in, void* d_out, int out_size,
                              void* d_ws, size_t ws_size, hipStream_t stream) {
  const u16* x = (const u16*)d_in[0];
  const u16* sre = (const u16*)d_in[1];
  const u16* sim = (const u16*)d_in[2];
  const u16* hre = (const u16*)d_in[3];
  const u16* him = (const u16*)d_in[4];
  const u16* wg = (const u16*)d_in[6];
  const u16* wu = (const u16*)d_in[7];
  const u16* wd = (const u16*)d_in[8];
  char* ws = (char*)d_ws;
  u16* xnI = (u16*)(ws);
  u16* pqI = (u16*)(ws + 2621440);
  u16* hhI = (u16*)(ws);
  u16* hI = (u16*)(ws + (8u << 20));
  f32x2* stats = (f32x2*)(ws + (11u << 20));
  float* csp = (float*)(ws + (11u << 20) + 131072);
  k0_ln<<<544, 512, 0, stream>>>(x, xnI, sre, sim, hre, him);
  k1_seq<<<256, C1::NT, 0, stream>>>(sre, sim, xnI, pqI, wg, wu, csp);
  k2_hid<<<256, C2::NT, 0, stream>>>(pqI, hre, him, x, hI, stats, wd);
  k3_glu<<<224, C3::NT, 0, stream>>>(hI, wg, wu, stats, csp, hhI);
  k4_down<<<224, C4::NT, 0, stream>>>(hhI, wd, hI, (u16*)d_out);
}
```

```cpp
#include <hip/hip_runtime.h>
#include <hip/hip_bf16.h>

typedef unsigned short u16;
typedef unsigned long long u64;
typedef short bf16x8 __attribute__((ext_vector_type(8)));
typedef short bf16x4 __attribute__((ext_vector_type(4)));
typedef float f32x4 __attribute__((ext_vector_type(4)));
typedef float f32x2 __attribute__((ext_vector_type(2)));
typedef unsigned u32x2 __attribute__((ext_vector_type(2)));
typedef unsigned u32x4 __attribute__((ext_vector_type(4)));
#define LAS __attribute__((address_space(3)))
#define GAS __attribute__((address_space(1)))
#define RLX_AGENT __ATOMIC_RELAXED, __HIP_MEMORY_SCOPE_AGENT
#define RLX_WG __ATOMIC_RELAXED, __HIP_MEMORY_SCOPE_WORKGROUP

constexpr int S_ = 2048, D_ = 640, F_ = 1728;

__device__ __forceinline__ float bf2f(u16 v) { return __uint_as_float(((unsigned)v) << 16); }
__device__ __forceinline__ unsigned pk2(float lo, float hi) {
  unsigned r; asm("v_cvt_pk_bf16_f32 %0, %1, %2" : "=v"(r) : "v"(lo), "v"(hi)); return r;
}
__device__ __forceinline__ float rbf(float v) { return __uint_as_float(pk2(v, v) << 16); }

#define CSTAMP(kid, i)
#define STAMP(kid, i)
template <int N> __device__ __forceinline__ void wait_vm() { asm volatile("s_waitcnt vmcnt(%0)" ::"n"(N) : "memory"); }
__device__ __forceinline__ void wait_lgkm0() { asm volatile("" ::: "memory"); __builtin_amdgcn_s_waitcnt(0xC07F); asm volatile("" ::: "memory"); }
__device__ __forceinline__ void bar() { asm volatile("" ::: "memory"); __builtin_amdgcn_s_barrier(); asm volatile("" ::: "memory"); }
template <int N, int STEP> __device__ __forceinline__ void wait_dyn(int n) {
  if constexpr (N <= 0) wait_vm<0>();
  else { if (n >= N) wait_vm<N>(); else wait_dyn<N - STEP, STEP>(n); }
}

template <int AUX> __device__ __forceinline__ void glds16(const char* g, LAS char* l) {
  __builtin_amdgcn_global_load_lds((const GAS void*)g, (LAS void*)l, 16, 0, AUX);
}
template <int AUX> __device__ __forceinline__ void touch(const char* g, LAS char* scratch) {
  __builtin_amdgcn_global_load_lds((const GAS void*)g, (LAS void*)scratch, 4, 0, AUX);
}
__device__ __forceinline__ void st8(void* p, u32x2 v) { __hip_atomic_store((u64*)p, __builtin_bit_cast(u64, v), RLX_AGENT); }
__device__ __forceinline__ u32x2 ld8(const void* p) { return __builtin_bit_cast(u32x2, __hip_atomic_load((u64*)p, RLX_AGENT)); }

constexpr int NL = 2;
constexpr int NWAVES = 10;
constexpr int LDS_RING = 136 * 1024;
constexpr int LDS_X = LDS_RING;
constexpr int LDS_TOTAL = 160 * 1024;
constexpr int LDS_MISC = LDS_TOTAL - 512;

template <int KID_, int BM_, int BN_, int BK_, int WM_, int WN_, int WK_, int NSA_, int NSB_, int NK_, int AMODE_, int BMODE_>
struct Cfg {
  static constexpr int KID = KID_, BM = BM_, BN = BN_, BK = BK_, WM = WM_, WN = WN_, WK = WK_, NSA = NSA_, NSB = NSB_, NK = NK_;
  static constexpr int AMODE = AMODE_, BMODE = BMODE_;
  static constexpr int NC = WM * WN * WK;
  static constexpr int MF = BM / WM / 16, NF = BN / WN / 16, KSW = BK / 32 / WK;
  static constexpr int ABYTES = BM * BK * 2, BBYTES = BN * BK * 2;
  static constexpr int PA = ABYTES / 1024, PB = BBYTES / 1024;
  static constexpr int GA = PA / NL, GB = PB / NL, G = GA + GB;
  static constexpr int RA = NSA * ABYTES, RB = NSB * BBYTES;
  static constexpr int NBAR = NK + (WK == 2 ? 2 : 1);
  static_assert(PA % NL == 0 && PB % NL == 0, "pieces");
  static_assert(RA + RB <= LDS_RING, "lds");
  static_assert(NSA <= NK && NSB <= NK && NSB >= NSA, "stages");
  static_assert(KSW >= 1 && KSW <= 2, "ksw");
};

template <class C> struct Frags { bf16x8 a[C::KSW][C::MF]; bf16x8 b[C::KSW][C::NF]; };

template <class C, class Src>
struct Loader {
  LAS char* lds; const Src& src;
  int lw, lane;
  unsigned voffA[C::GA], voffB[C::GB];
  static __device__ __forceinline__ unsigned rowmajor(int p, int ld) {
    const int row = p >> 3, c = (p & 7) ^ ((row >> 1) & 7);
    return row * ld + c * 16;
  }
  __device__ __forceinline__ Loader(LAS char* lds_, const Src& s, int lw_, int lane_) : lds(lds_), src(s), lw(lw_), lane(lane_) {
#pragma unroll
    for (int i = 0; i < C::GA; ++i) {
      const int p = (i * NL + lw) * 64 + lane;
      if constexpr (C::AMODE == 0) voffA[i] = p * 16;
      else voffA[i] = rowmajor(p, Src::ldA);
    }
#pragma unroll
    for (int i = 0; i < C::GB; ++i) {
      const int p = (i * NL + lw) * 64 + lane;
      if constexpr (C::BMODE == 0) voffB[i] = p * 16;
      else if constexpr (C::BMODE == 1) voffB[i] = rowmajor(p, Src::ldB);
      else { const int q = p % (C::BK * 8), k = q >> 3, c = (q & 7) ^ ((((k >> 3) & 1) << 2) | (((k >> 1) & 1) << 1)); voffB[i] = k * Src::ldB + c * 16; }
    }
  }
  __device__ __forceinline__ void issueA(int k, unsigned buf) {
    const char* ga = src.baseA(k);
    LAS char* la = lds + buf + lw * 1024;
#pragma unroll
    for (int i = 0; i < C::GA; ++i) glds16<Src::AUXA>(ga + voffA[i], la + i * NL * 1024);
  }
  __device__ __forceinline__ void issueB(int k, unsigned buf) {
    LAS char* lb = lds + C::RA + buf + lw * 1024;
#pragma unroll
    for (int i = 0; i < C::GB; ++i) glds16<Src::AUXB>(src.baseB(k, i >= C::GB / 2) + voffB[i], lb + i * NL * 1024);
  }
  __device__ __forceinline__ void preloadB() {
#pragma unroll 1
    for (int s = 0; s < C::NSB; ++s) issueB(s, s * C::BBYTES);
  }
  static constexpr int cmin(int a, int b) { return a < b ? a : b; }
  static constexpr int cmax(int a, int b) { return a > b ? a : b; }
  static constexpr int T(int i) { return C::NSA * C::GA + cmin(i, C::NK - C::NSA) * C::GA + cmin(i, C::NK - C::NSB) * C::GB; }
  static constexpr int cnt(int k) {
    const int s = k + 1;
    const int pa = s < C::NSA ? (s + 1) * C::GA : T(s - C::NSA) + C::GA;
    const int pb = s < C::NSB ? 0 : T(s - C::NSB + 1);
    return T(k) - cmax(pa, pb);
  }
  static constexpr int key(int k) { return cnt(k) * 4 + (k + C::NSA < C::NK ? 2 : 0) + (k + C::NSB < C::NK ? 1 : 0); }
  static constexpr int seg_end(int k) { int e = k + 1; while (e < C::NK - 1 && key(e) == key(k)) ++e; return e; }
  unsigned ca, cb;
  template <int K> __device__ __forceinline__ void iter(int k) {
    static_assert(cnt(K) <= 63, "vmcnt");
    wait_vm<cnt(K)>();
    bar();
    if constexpr (K + C::NSA < C::NK) { issueA(k + C::NSA, ca); ca += C::ABYTES; if (ca == C::RA) ca = 0; }
    if constexpr (K + C::NSB < C::NK) { issueB(k + C::NSB, cb); cb += C::BBYTES; if (cb == C::RB) cb = 0; }
  }
  template <int K> __device__ __forceinline__ void seg() {
    if constexpr (K < C::NK - 1) {
      constexpr int E = seg_end(K);
      if constexpr (E - K == 1) iter<K>(K);
      else {
#pragma unroll 1
        for (int k = K; k < E; ++k) iter<K>(k);
      }
      seg<E>();
    }
  }
  template <class Hook> __device__ __forceinline__ void run(Hook hook) {
#pragma unroll 1
    for (int s = 0; s < C::NSA; ++s) issueA(s, s * C::ABYTES);
    wait_vm<(C::NSA - 1) * C::GA>();
    bar();
    hook();
    ca = 0; cb = 0;
    seg<0>();
  }
};

template <class C>
struct Consumer {
  LAS char* lds;
  int wid, lane, wm, wn, wk;
  unsigned rdA[2], rdB[4];

  __device__ __forceinline__ Consumer(LAS char* lds_, int wid_, int lane_) : lds(lds_), wid(wid_), lane(lane_) {
    wk = wid % C::WK; wn = (wid / C::WK) % C::WN; wm = wid / (C::WK * C::WN);
    const int fr = lane & 15, fq = lane >> 4;
    const int arow = wm * (C::BM / C::WM) + fr;
    const int ss0 = wk * C::KSW;
#pragma unroll
    for (int s = 0; s < 2; ++s) {
      const int ss = ss0 + s;
      if constexpr (C::AMODE == 0) rdA[s] = ((ss0 * 4 + fq) * C::BM + arow) * 16;
      else rdA[s] = arow * 128 + ((((ss & 1) * 4 + fq) ^ ((fr >> 1) & 7)) * 16);
    }
    if constexpr (C::BMODE <= 1) {
      const int brow = wn * (C::BN / C::WN) + fr;
#pragma unroll
      for (int s = 0; s < 2; ++s) {
        const int ss = ss0 + s;
        if constexpr (C::BMODE == 0) rdB[s] = C::RA + ((ss0 * 4 + fq) * C::BN + brow) * 16;
        else rdB[s] = C::RA + brow * 128 + ((((ss & 1) * 4 + fq) ^ ((fr >> 1) & 7)) * 16);
      }
    } else {
      const int g = lane >> 4, q = (lane >> 2) & 3, p = lane & 3;
#pragma unroll
      for (int j = 0; j < (C::BMODE == 4 ? 4 : 2); ++j) {
        const int jj = (C::BMODE == 4) ? j : 2 * wn + j;
        rdB[j] = C::RA + (32 * ss0 + 8 * g + q) * 128 + ((((jj ^ (((g & 1) << 1) | (q >> 1))) << 1) | (p >> 1)) * 16) + 8 * (p & 1);
      }
    }
  }

  __device__ __forceinline__ void read(Frags<C>& f, unsigned bufa, unsigned bufb) {
    LAS char* a = lds + bufa; LAS char* b = lds + bufb;
#pragma unroll
    for (int s = 0; s < C::KSW; ++s) {
      if constexpr (C::BMODE == 0) {
#pragma unroll
        for (int n = 0; n < C::NF; ++n) f.b[s][n] = *(const LAS bf16x8*)(b + rdB[0] + s * (C::BN * 64) + n * 256);
      } else if constexpr (C::BMODE == 1) {
#pragma unroll
        for (int n = 0; n < C::NF; ++n) f.b[s][n] = *(const LAS bf16x8*)(b + rdB[s] + n * 2048);
      } else {
#pragma unroll
        for (int n = 0; n < C::NF; ++n) {
          const int j = (C::BMODE == 4) ? n : (n & 1), mat = (C::BMODE == 4) ? 0 : (n >> 1);
          bf16x4 lo = __builtin_amdgcn_ds_read_tr16_b64_v4i16((LAS bf16x4*)(b + rdB[j] + mat * (C::BK * 128) + s * 4096));
          bf16x4 hi = __builtin_amdgcn_ds_read_tr16_b64_v4i16((LAS bf16x4*)(b + rdB[j] + mat * (C::BK * 128) + s * 4096 + 512));
          f.b[s][n] = __builtin_shufflevector(lo, hi, 0, 1, 2, 3, 4, 5, 6, 7);
        }
      }
      if constexpr (C::AMODE == 0) {
#pragma unroll
        for (int m = 0; m < C::MF; ++m) f.a[s][m] = *(const LAS bf16x8*)(a + rdA[0] + s * (C::BM * 64) + m * 256);
      } else {
#pragma unroll
        for (int m = 0; m < C::MF; ++m) f.a[s][m] = *(const LAS bf16x8*)(a + rdA[s] + m * 2048);
      }
    }
  }

  __device__ __forceinline__ void mma(f32x4 (&acc)[C::MF][C::NF], const Frags<C>& f) {
#pragma unroll
    for (int s = 0; s < C::KSW; ++s)
#pragma unroll
      for (int m = 0; m < C::MF; ++m)
#pragma unroll
        for (int n = 0; n < C::NF; ++n)
          acc[m][n] = __builtin_amdgcn_mfma_f32_16x16x32_bf16(f.b[s][n], f.a[s][m], acc[m][n], 0, 0, 0);
  }
  static constexpr int NRD = C::KSW * (C::MF + (C::BMODE >= 2 ? 2 : 1) * C::NF);
  static constexpr int NMM = C::KSW * C::MF * C::NF;
  __device__ __forceinline__ void interleave() {
    constexpr int RPM = (NRD + NMM - 3) / (NMM - 2);
#pragma unroll
    for (int i = 0; i < NMM; ++i) {
      __builtin_amdgcn_sched_group_barrier(0x100, RPM, 0);
      __builtin_amdgcn_sched_group_barrier(0x008, 1, 0);
    }
  }
  unsigned na, nb;
  __device__ __forceinline__ void step(f32x4 (&acc)[C::MF][C::NF], Frags<C>& cur, Frags<C>& nxt) {
    na += C::ABYTES; if (na == C::RA) na = 0;
    nb += C::BBYTES; if (nb == C::RB) nb = 0;
    wait_lgkm0(); bar();
    __builtin_amdgcn_sched_barrier(0);
    read(nxt, na, nb);
    mma(acc, cur);
    interleave();
    __builtin_amdgcn_sched_barrier(0);
  }

  template <class Hook> __device__ __forceinline__ void run(f32x4 (&acc)[C::MF][C::NF], Hook hook) {
    Frags<C> f0, f1;
#pragma unroll
    for (int m = 0; m < C::MF; ++m)
#pragma unroll
      for (int n = 0; n < C::NF; ++n) acc[m][n] = (f32x4){0.f, 0.f, 0.f, 0.f};
    bar();
    if (wid == 0) { STAMP(C::KID, 1); CSTAMP(C::KID, 8); }
    hook();
    read(f0, 0, 0);
    na = 0; nb = 0;
#pragma unroll 1
    for (int k = 0; k + 2 < C::NK; k += 2) {
      step(acc, f0, f1);
      step(acc, f1, f0);
    }
    if constexpr (C::NK % 2 == 0) {
      step(acc, f0, f1);
      wait_lgkm0(); bar();
      mma(acc, f1);
    } else {
      wait_lgkm0(); bar();
      mma(acc, f0);
    }
    if (wid == 0) { STAMP(C::KID, 2); CSTAMP(C::KID, 9); }
  }

  static constexpr bool SPLM = (C::MF % 2 == 0);
  static constexpr int RM = SPLM ? C::MF / 2 : C::MF, RN = SPLM ? C::NF : C::NF / 2;
  __device__ __forceinline__ void reduce_k(f32x4 (&acc)[C::MF][C::NF], f32x4 (&red)[RM][RN]) {
    static_assert(C::WK == 2, "");
    static_assert(LDS_X + C::NC * RM * RN * 1024 <= LDS_MISC || RM * RN == 6, "");
    LAS f32x4* x = (LAS f32x4*)(lds + LDS_X - (RM * RN == 6 ? 1024 : 0));
#pragma unroll
    for (int m = 0; m < RM; ++m)
#pragma unroll
      for (int n = 0; n < RN; ++n) {
        const f32x4 lo = acc[m][n], hi = SPLM ? acc[m + RM][n] : acc[m][n + RN];
        x[(wid * RM * RN + m * RN + n) * 64 + lane] = wk ? lo : hi;
        red[m][n] = wk ? hi : lo;
      }
    wait_lgkm0(); bar();
#pragma unroll
    for (int m = 0; m < RM; ++m)
#pragma unroll
      for (int n = 0; n < RN; ++n) red[m][n] += x[((wid ^ 1) * RM * RN + m * RN + n) * 64 + lane];
  }
};

constexpr int FL1 = 0;
constexpr int FL2 = 16 * 32;
constexpr int FL3 = 24 * 32;
constexpr int FL_WORDS = 32 * 32;

__device__ __forceinline__ void publish(LAS unsigned* arr, unsigned nwaves, unsigned* flag, int lane) {
  wait_vm<0>();
  if (lane == 0) {
    const unsigned old = __hip_atomic_fetch_add(arr, 1u, RLX_WG);
    if (old == nwaves - 1) __hip_atomic_store(flag, 1u, RLX_AGENT);
  }
}
__device__ __forceinline__ void await(const unsigned* word, bool need) {
  const u64 t0 = wall_clock64();
  for (;;) {
    const unsigned v = __hip_atomic_load(word, RLX_AGENT);
    if (__all(!need || v == 1u)) break;
    if (wall_clock64() - t0 > 2000000ull) break;
    __builtin_amdgcn_s_sleep(2);
  }
  __builtin_amdgcn_fence(__ATOMIC_ACQUIRE, "wavefront");
}

__global__ __launch_bounds__(512) void k0_ln(const u16* __restrict__ x, u16* __restrict__ xnI,
                                             const u16* __restrict__ sre, const u16* __restrict__ sim,
                                             const u16* __restrict__ hre, const u16* __restrict__ him,
                                             const u16* __restrict__ wg, const u16* __restrict__ wu, const u16* __restrict__ wd,
                                             float* __restrict__ cs, unsigned* __restrict__ flags) {
  __shared__ __attribute__((aligned(16))) char smem[8 * 1296 + 64];
  const int tid = threadIdx.x, wid = tid >> 6, lane = tid & 63;
  if (wid == 0) STAMP(0, 0);
  if (blockIdx.x < 256) {
    const int koct = blockIdx.x, row = koct * 8 + wid;
    const u16* xr = x + row * D_;
    bf16x8 v0 = *(const bf16x8*)(xr + lane * 8);
    bf16x8 v1 = {0, 0, 0, 0, 0, 0, 0, 0};
    if (lane < 16) v1 = *(const bf16x8*)(xr + 512 + lane * 8);
    float f[16];
#pragma unroll
    for (int i = 0; i < 8; ++i) { f[i] = bf2f((u16)v0[i]); f[8 + i] = bf2f((u16)v1[i]); }
    float s = 0.f;
#pragma unroll
    for (int i = 0; i < 16; ++i) s += f[i];
#pragma unroll
    for (int o = 32; o >= 1; o >>= 1) s += __shfl_xor(s, o);
    const float mean = s * (1.0f / D_);
    float q = 0.f;
#pragma unroll
    for (int i = 0; i < 8; ++i) { const float d = f[i] - mean; q += d * d; }
    if (lane < 16) {
#pragma unroll
      for (int i = 8; i < 16; ++i) { const float d = f[i] - mean; q += d * d; }
    }
#pragma unroll
    for (int o = 32; o >= 1; o >>= 1) q += __shfl_xor(q, o);
    const float mb = rbf(mean), vb = rbf(q * (1.0f / D_));
    const float den = rbf(sqrtf(rbf(vb + 1e-5f)));
    const float rden = 1.0f / den;
    u32x4 w0, w1;
#pragma unroll
    for (int i = 0; i < 4; ++i) {
      w0[i] = pk2(rbf(f[2 * i] - mb) * rden, rbf(f[2 * i + 1] - mb) * rden);
      w1[i] = pk2(rbf(f[8 + 2 * i] - mb) * rden, rbf(f[9 + 2 * i] - mb) * rden);
    }
    char* sr = smem + wid * 1296;
    *(u32x4*)(sr + lane * 16) = w0;
    if (lane < 16) *(u32x4*)(sr + 1024 + lane * 16) = w1;
    __syncthreads();
    for (int d = tid; d < D_; d += 512) {
      bf16x8 o;
#pragma unroll
      for (int r = 0; r < 8; ++r) o[r] = *(const short*)(smem + r * 1296 + d * 2);
      const int j = d / 80, n = d % 80;
      *(bf16x8*)(xnI + (((size_t)j * 256 + koct) * 80 + n) * 8) = o;
    }
  } else if (blockIdx.x < 512) {
    const int b = blockIdx.x - 256;
    LAS char* scr = (LAS char*)smem + __builtin_amdgcn_readfirstlane(wid) * 256;
    touch<0>((const char*)((b & 128) ? sim : sre) + (size_t)(b & 127) * 65536 + tid * 128, scr);
    wait_vm<0>();
  } else if (blockIdx.x < 512 + 54) {
    const int b = blockIdx.x - 512, mat = b / 27, c0 = (b % 27) * 64;
    const u16* w = (mat ? wu : wg) + c0 + (tid & 7) * 8;
    const int rg = tid >> 3;
    float a[8] = {0, 0, 0, 0, 0, 0, 0, 0};
    bf16x8 v[10];
#pragma unroll
    for (int i = 0; i < 10; ++i) v[i] = *(const bf16x8*)(w + (size_t)(rg + 64 * i) * F_);
#pragma unroll
    for (int i = 0; i < 10; ++i)
#pragma unroll
      for (int e = 0; e < 8; ++e) a[e] += bf2f((u16)v[i][e]);
#pragma unroll
    for (int e = 0; e < 8; ++e) {
      a[e] += __shfl_xor(a[e], 8); a[e] += __shfl_xor(a[e], 16); a[e] += __shfl_xor(a[e], 32);
    }
    float* sf = (float*)smem;
    if (lane < 8) {
#pragma unroll
      for (int e = 0; e < 8; ++e) sf[wid * 64 + lane * 8 + e] = a[e];
    }
    __syncthreads();
    if (tid < 64) {
      float t = 0.f;
#pragma unroll
      for (int w8 = 0; w8 < 8; ++w8) t += sf[w8 * 64 + tid];
      cs[mat * F_ + c0 + tid] = t;
    }
  } else {
    const int b = blockIdx.x - (512 + 54);
    LAS char* scr = (LAS char*)smem + __builtin_amdgcn_readfirstlane(wid) * 256;
    if (b == 0) { flags[tid] = 0u; flags[512 + tid] = 0u; }
    const int l = b * 512 + tid;
    if (l < 12800 + 17280)
      touch<0>(l < 6400 ? (const char*)hre + l * 128 : (l < 12800 ? (const char*)him + (l - 6400) * 128 : (const char*)wd + (size_t)(l - 12800) * 128), scr);
    wait_vm<0>();
  }
  if (wid == 0) { wait_vm<0>(); STAMP(0, 3); }
}

using C1 = Cfg<1, 128, 80, 64, 4, 1, 1, 5, 5, 32, 1, 0>;
using C2 = Cfg<2, 64, 80, 64, 2, 1, 2, 4, 10, 20, 0, 1>;
using C3 = Cfg<3, 256, 128, 32, 4, 2, 1, 4, 8, 20, 0, 3>;
using C4 = Cfg<4, 96, 64, 64, 2, 1, 2, 4, 10, 27, 0, 4>;
struct Src1 {
  const char* a; const char* b;
  static constexpr int ldA = S_ * 2, ldB = 0, AUXA = 0, AUXB = 0;
  __device__ __forceinline__ const char* baseA(int k) const { return a + k * 128; }
  __device__ __forceinline__ const char* baseB(int k, int) const { return b + k * (80 * 128); }
};
struct Src2 {
  const char* a; const char* bre; const char* bim;
  static constexpr int ldA = 0, ldB = D_ * 2, AUXA = 16, AUXB = 0;
  __device__ __forceinline__ const char* baseA(int k) const { return a + k * (64 * 128); }
  __device__ __forceinline__ const char* baseB(int k, int) const { return k < 10 ? bre + k * 128 : bim + (k - 10) * 128; }
};
struct Src3 {
  const char* a; const char* bg; const char* bu;
  static constexpr int ldA = 0, ldB = F_ * 2, AUXA = 16, AUXB = 0;
  __device__ __forceinline__ const char* baseA(int k) const { return a + k * (256 * 64); }
  __device__ __forceinline__ const char* baseB(int k, int up) const { return (up ? bu : bg) + (size_t)k * (32 * F_ * 2); }
};
struct Src4 {
  const char* a; const char* b;
  static constexpr int ldA = 0, ldB = D_ * 2, AUXA = 16, AUXB = 0;
  __device__ __forceinline__ const char* baseA(int k) const { return a + k * (96 * 128); }
  __device__ __forceinline__ const char* baseB(int k, int) const { return b + (size_t)k * (64 * D_ * 2); }
};

struct Params {
  const u16* x; const u16* sre; const u16* sim; const u16* hre; const u16* him; const u16* wg; const u16* wu; const u16* wd;
  const u16* xnI; u16* pqI; u16* hI; u16* hhI; f32x2* stats; const float* cs; unsigned* flags; u16* out;
};

template <int N> __device__ __forceinline__ void bars() {
#pragma unroll 1
  for (int i = 0; i < N; ++i) bar();
}

__global__ __launch_bounds__(NWAVES * 64) void mega(const Params p) {
  __shared__ __attribute__((aligned(1024))) char smem_[LDS_TOTAL];
  LAS char* lds = (LAS char*)smem_;
  const int wid = __builtin_amdgcn_readfirstlane(threadIdx.x >> 6), lane = threadIdx.x & 63;
  const int bid = blockIdx.x, g = bid & 7, idx = bid >> 3;
  LAS unsigned* arr = (LAS unsigned*)(lds + LDS_MISC + 256);
  if (wid == 0) STAMP(1, 0);
  if (threadIdx.x < 4) arr[threadIdx.x] = 0u;
  const int j1 = idx & 7, mq = idx >> 3, isQ = mq >> 1, t1 = g * 256 + (mq & 1) * 128;
  const int j2 = idx & 7, mt2 = g * 4 + (idx >> 3);
  const int mt3 = (g & 6) + (idx & 1), j3 = (g & 1) * 14 + (idx >> 1);
  const bool on3 = idx < 28 && j3 < 27;
  const int lin4 = g * 28 + idx, mt4 = lin4 / 10, j4 = lin4 % 10;
  const bool on4 = idx < 28 && lin4 < 220;
  LAS f32x4* rs = (LAS f32x4*)(lds + LDS_X);
  LAS float* csl = (LAS float*)(lds + LDS_X + 4096);

  if (wid >= 8) {
    const int lw = wid - 8;
    {
      Src1 s{(const char*)((isQ ? p.sim : p.sre) + (size_t)t1 * S_), (const char*)(p.xnI + (size_t)j1 * 256 * 80 * 8)};
      Loader<C1, Src1> L(lds, s, lw, lane);
      L.preloadB();
      L.run([] {});
      bar();
    }
    {
      Src2 s{(const char*)(p.pqI + (size_t)mt2 * 160 * 64 * 8), (const char*)(p.hre + (size_t)j2 * 80 * D_), (const char*)(p.him + (size_t)j2 * 80 * D_)};
      Loader<C2, Src2> L(lds, s, lw, lane);
      L.preloadB();
      await(p.flags + FL1 + (mt2 >> 1) * 32 + (lane & 15), true);
      if (lw == 0) STAMP(2, 0);
      wait_vm<0>();
      L.run([] {});
      bar();
    }
    if (on3) {
      Src3 s{(const char*)(p.hI + (size_t)mt3 * 80 * 256 * 8), (const char*)(p.wg + j3 * 64), (const char*)(p.wu + j3 * 64)};
      Loader<C3, Src3> L(lds, s, lw, lane);
      L.preloadB();
      bar();
      await(p.flags + FL2 + mt3 * 32 + (lane & 31), true);
      if (lw == 0) STAMP(3, 0);
      wait_vm<0>();
      L.run([&] {
#pragma unroll
        for (int h = 0; h < 2; ++h) {
          const int r = lw * 128 + h * 64 + lane;
          const f32x2* sp = p.stats + (mt3 * 256 + r) * 8;
          f32x2 pv[8];
#pragma unroll
          for (int i = 0; i < 8; ++i) pv[i] = __builtin_bit_cast(f32x2, ld8(sp + i));
          float mean = 0.f, q = 0.f, dd = 0.f;
#pragma unroll
          for (int i = 0; i < 8; ++i) { mean += pv[i][0]; q += pv[i][1]; }
          mean *= 0.125f;
#pragma unroll
          for (int i = 0; i < 8; ++i) { const float d = pv[i][0] - mean; dd += d * d; }
          q += 80.0f * dd;
          const float den = rbf(sqrtf(rbf(rbf(q * (1.0f / D_)) + 1e-5f)));
          rs[r] = (f32x4){-rbf(mean), den, -1.44269504089f / den, 0.f};
        }
        csl[lw * 64 + lane] = p.cs[lw * F_ + j3 * 64 + lane];
        wait_lgkm0();
      });
      bar();
    } else {
      bar();
    }
    if (on4) {
      Src4 s{(const char*)(p.hhI + (size_t)mt4 * 216 * 96 * 8), (const char*)(p.wd + j4 * 64)};
      Loader<C4, Src4> L(lds, s, lw, lane);
      L.preloadB();
      const int ra = (mt4 * 96) >> 8, rb = min(mt4 * 96 + 95, S_ - 1) >> 8;
      await(p.flags + FL3 + (lane < 32 ? ra : rb) * 32 + (lane & 31), (lane & 31) < 27);
      if (lw == 0) STAMP(4, 0);
      wait_vm<0>();
      L.run([] {});
      bar();
      bar();
    }
    return;
  }

  const int fr = lane & 15, fq = lane >> 4;
  if (wid < C1::NC) {
    Consumer<C1> L(lds, wid, lane);
    f32x4 acc[C1::MF][C1::NF];
    L.run(acc, [] {});
    const float sg = isQ ? -1.f : 1.f;
#pragma unroll
    for (int m = 0; m < 2; ++m) {
      const int t = t1 + L.wm * 32 + m * 16 + fr, mt = t >> 6, r = t & 63;
#pragma unroll
      for (int n = 0; n < 5; ++n) {
        const int koct = isQ * 80 + j1 * 10 + n * 2 + (fq >> 1);
        u32x2 w; w[0] = pk2(sg * acc[m][n][0], sg * acc[m][n][1]); w[1] = pk2(sg * acc[m][n][2], sg * acc[m][n][3]);
        st8(p.pqI + (((size_t)mt * 160 + koct) * 64 + r) * 8 + (fq & 1) * 4, w);
      }
    }
    publish(arr + 0, C1::NC, p.flags + FL1 + (t1 >> 7) * 32 + isQ * 8 + j1, lane);
    if (wid == 0) STAMP(1, 3);
  } else {
    bars<C1::NBAR>();
  }
  if (wid < C2::NC) {
    Consumer<C2> L(lds, wid, lane);
    f32x4 acc[C2::MF][C2::NF];
    const int t = mt2 * 64 + L.wm * 32 + L.wk * 16 + fr;
    bf16x4 xv[C2::NF];
#pragma unroll
    for (int n = 0; n < C2::NF; ++n) xv[n] = *(const bf16x4*)(p.x + (size_t)t * D_ + j2 * 80 + n * 16 + fq * 4);
#pragma unroll
    for (int n = 0; n < C2::NF; ++n) asm volatile("" : "+v"(xv[n]));
    L.run(acc, [] {});
    f32x4 red[1][5];
    L.reduce_k(acc, red);
    float hv[C2::NF][4];
    float s = 0.f;
#pragma unroll
    for (int n = 0; n < C2::NF; ++n) {
      u32x2 w;
      w[0] = pk2(red[0][n][0] + bf2f((u16)xv[n][0]), red[0][n][1] + bf2f((u16)xv[n][1]));
      w[1] = pk2(red[0][n][2] + bf2f((u16)xv[n][2]), red[0][n][3] + bf2f((u16)xv[n][3]));
      hv[n][0] = __uint_as_float(w[0] << 16); hv[n][1] = __uint_as_float(w[0] & 0xffff0000u);
      hv[n][2] = __uint_as_float(w[1] << 16); hv[n][3] = __uint_as_float(w[1] & 0xffff0000u);
      s += (hv[n][0] + hv[n][1]) + (hv[n][2] + hv[n][3]);
      const int koct = j2 * 10 + n * 2 + (fq >> 1);
      st8(p.hI + (((size_t)(t >> 8) * 80 + koct) * 256 + (t & 255)) * 8 + (fq & 1) * 4, w);
    }
    s += __shfl_xor(s, 16); s += __shfl_xor(s, 32);
    const float mean = s * (1.0f / 80.0f);
    float q = 0.f;
#pragma unroll
    for (int n = 0; n < C2::NF; ++n)
#pragma unroll
      for (int r = 0; r < 4; ++r) { const float d = hv[n][r] - mean; q += d * d; }
    q += __shfl_xor(q, 16); q += __shfl_xor(q, 32);
    if (fq == 0) st8(p.stats + t * 8 + j2, __builtin_bit_cast(u32x2, (f32x2){mean, q}));
    publish(arr + 1, C2::NC, p.flags + FL2 + g * 32 + idx, lane);
    if (wid == 0) STAMP(2, 3);
  } else {
    bars<C2::NBAR>();
  }
  if (on3) {
    Consumer<C3> L(lds, wid, lane);
    f32x4 acc[C3::MF][C3::NF];
    L.run(acc, [] {});
#pragma unroll
    for (int n = 0; n < 2; ++n) {
      const int fl = L.wn * 32 + n * 16 + fq * 4, f0 = j3 * 64 + fl;
      const f32x4 cg = *(const LAS f32x4*)(csl + fl), cu = *(const LAS f32x4*)(csl + 64 + fl);
#pragma unroll
      for (int m = 0; m < C3::MF; ++m) {
        const int rl = L.wm * 64 + m * 16 + fr, t = mt3 * 256 + rl;
        const f32x4 st = rs[rl];
        float o[4];
#pragma unroll
        for (int r = 0; r < 4; ++r) {
          const float e = __builtin_amdgcn_exp2f(__builtin_fmaf(cg[r], st[0], acc[m][n][r]) * st[2]);
          o[r] = __builtin_fmaf(cu[r], st[0], acc[m][n + 2][r]) * __builtin_amdgcn_rcpf(__builtin_fmaf(e, st[1], st[1]));
        }
        u32x2 w; w[0] = pk2(o[0], o[1]); w[1] = pk2(o[2], o[3]);
        const int t4 = t / 96;
        st8(p.hhI + (((size_t)t4 * 216 + (f0 >> 3)) * 96 + (t - t4 * 96)) * 8 + (fq & 1) * 4, w);
      }
    }
    publish(arr + 2, C3::NC, p.flags + FL3 + mt3 * 32 + j3, lane);
    if (wid == 0) STAMP(3, 3);
  }
  if (on4) {
    if (wid < C4::NC) {
      Consumer<C4> L(lds, wid, lane);
      f32x4 acc[C4::MF][C4::NF];
      const int tb = mt4 * 96 + L.wm * 48 + fr, c0 = j4 * 64 + L.wk * 32 + fq * 4;
      u32x2 hv[3][2];
      L.run(acc, [&] {
#pragma unroll
        for (int m = 0; m < 3; ++m)
#pragma unroll
          for (int n = 0; n < 2; ++n) {
            const int t = min(tb + m * 16, S_ - 1), c = c0 + n * 16;
            hv[m][n] = ld8(p.hI + (((size_t)(t >> 8) * 80 + (c >> 3)) * 256 + (t & 255)) * 8 + (c & 4));
          }
      });
      f32x4 red[3][2];
      L.reduce_k(acc, red);
#pragma unroll
      for (int m = 0; m < 3; ++m)
#pragma unroll
        for (int n = 0; n < 2; ++n) {
          const int t = tb + m * 16;
          u32x2 w;
          w[0] = pk2(red[m][n][0] + __uint_as_float(hv[m][n][0] << 16), red[m][n][1] + __uint_as_float(hv[m][n][0] & 0xffff0000u));
          w[1] = pk2(red[m][n][2] + __uint_as_float(hv[m][n][1] << 16), red[m][n][3] + __uint_as_float(hv[m][n][1] & 0xffff0000u));
          if (t < S_) *(u32x2*)(p.out + (size_t)t * D_ + c0 + n * 16) = w;
        }
      if (wid == 0) { wait_vm<0>(); STAMP(4, 3); }
    }
  }
}

extern "C" void kernel_launch(void* const* d_in, const int* in_sizes, int n_in, void* d_out, int out_size,
                              void* d_ws, size_t ws_size, hipStream_t stream) {
  char* ws = (char*)d_ws;
  Params p;
  p.x = (const u16*)d_in[0]; p.sre = (const u16*)d_in[1]; p.sim = (const u16*)d_in[2];
  p.hre = (const u16*)d_in[3]; p.him = (const u16*)d_in[4];
  p.wg = (const u16*)d_in[6]; p.wu = (const u16*)d_in[7]; p.wd = (const u16*)d_in[8];
  u16* xnI = (u16*)(ws);
  p.xnI = xnI;
  p.pqI = (u16*)(ws + (3u << 20));
  p.hI = (u16*)(ws + (9u << 20));
  p.hhI = (u16*)(ws + (12u << 20));
  p.stats = (f32x2*)(ws + (20u << 20));
  float* cs = (float*)(ws + (20u << 20) + 131072);
  p.cs = cs;
  p.flags = (unsigned*)(ws + (21u << 20));
  p.out = (u16*)d_out;
  k0_ln<<<512 + 54 + 59, 512, 0, stream>>>(p.x, xnI, p.sre, p.sim, p.hre, p.him, p.wg, p.wu, p.wd, cs, p.flags);
  mega<<<256, NWAVES * 64, 0, stream>>>(p);
}
```

```cpp
#include <hip/hip_runtime.h>
#include <hip/hip_bf16.h>
#define AUXH 16

typedef unsigned short u16;
typedef unsigned long long u64;
typedef short bf16x8 __attribute__((ext_vector_type(8)));
typedef short bf16x4 __attribute__((ext_vector_type(4)));
typedef float f32x4 __attribute__((ext_vector_type(4)));
typedef float f32x2 __attribute__((ext_vector_type(2)));
typedef unsigned u32x2 __attribute__((ext_vector_type(2)));
typedef unsigned u32x4 __attribute__((ext_vector_type(4)));
#define LAS __attribute__((address_space(3)))
#define GAS __attribute__((address_space(1)))
#define RLX_AGENT __ATOMIC_RELAXED, __HIP_MEMORY_SCOPE_AGENT
#define RLX_WG __ATOMIC_RELAXED, __HIP_MEMORY_SCOPE_WORKGROUP

constexpr int S_ = 2048, D_ = 640, F_ = 1728;

__device__ __forceinline__ float bf2f(u16 v) { return __uint_as_float(((unsigned)v) << 16); }
typedef __bf16 bf16x2n __attribute__((ext_vector_type(2)));
__device__ __forceinline__ unsigned pk2(float lo, float hi) {
  return __builtin_bit_cast(unsigned, __builtin_convertvector((f32x2){lo, hi}, bf16x2n));
}
__device__ __forceinline__ float rbf(float v) { return __uint_as_float(pk2(v, v) << 16); }

#define CSTAMP(kid, i)
#define STAMP(kid, i)
template <int N> __device__ __forceinline__ void wait_vm() { asm volatile("s_waitcnt vmcnt(%0)" ::"n"(N) : "memory"); }
__device__ __forceinline__ void wait_lgkm0() { asm volatile("" ::: "memory"); __builtin_amdgcn_s_waitcnt(0xC07F); asm volatile("" ::: "memory"); }
__device__ __forceinline__ void bar() { asm volatile("" ::: "memory"); __builtin_amdgcn_s_barrier(); asm volatile("" ::: "memory"); }
template <int N, int STEP> __device__ __forceinline__ void wait_dyn(int n) {
  if constexpr (N <= 0) wait_vm<0>();
  else { if (n >= N) wait_vm<N>(); else wait_dyn<N - STEP, STEP>(n); }
}

template <int AUX> __device__ __forceinline__ void glds16(const char* g, LAS char* l) {
  __builtin_amdgcn_global_load_lds((const GAS void*)g, (LAS void*)l, 16, 0, AUX);
}
template <int AUX> __device__ __forceinline__ void touch(const char* g, LAS char* scratch) {
  __builtin_amdgcn_global_load_lds((const GAS void*)g, (LAS void*)scratch, 4, 0, AUX);
}
__device__ __forceinline__ void st8(void* p, u32x2 v) { __hip_atomic_store((u64*)p, __builtin_bit_cast(u64, v), RLX_AGENT); }
__device__ __forceinline__ u32x2 ld8(const void* p) { return __builtin_bit_cast(u32x2, __hip_atomic_load((u64*)p, RLX_AGENT)); }

constexpr int NL = 2;
constexpr int NWAVES = 10;
constexpr int LDS_RING = 136 * 1024;
constexpr int LDS_X = LDS_RING;
constexpr int LDS_TOTAL = 160 * 1024;
constexpr int LDS_MISC = LDS_TOTAL - 512;

template <int KID_, int BM_, int BN_, int BK_, int WM_, int WN_, int WK_, int NSA_, int NSB_, int NK_, int AMODE_, int BMODE_, int SWAP_ = 1>
struct Cfg {
  static constexpr int KID = KID_, BM = BM_, BN = BN_, BK = BK_, WM = WM_, WN = WN_, WK = WK_, NSA = NSA_, NSB = NSB_, NK = NK_;
  static constexpr int AMODE = AMODE_, BMODE = BMODE_, SWAP = SWAP_;
  static constexpr int NC = WM * WN * WK;
  static constexpr int MF = BM / WM / 16, NF = BN / WN / 16, KSW = BK / 32 / WK;
  static constexpr int ABYTES = BM * BK * 2, BBYTES = BN * BK * 2;
  static constexpr int PA = ABYTES / 1024, PB = BBYTES / 1024;
  static constexpr int GA = PA / NL, GB = PB / NL, G = GA + GB;
  static constexpr int RA = NSA * ABYTES, RB = NSB * BBYTES;
  static constexpr int NBAR = NK + (WK == 2 ? 2 : 1);
  static_assert(PA % NL == 0 && PB % NL == 0, "pieces");
  static_assert(RA + RB <= LDS_RING, "lds");
  static_assert(NSA <= NK && NSB <= NK && NSB >= NSA, "stages");
  static_assert(KSW >= 1 && KSW <= 2, "ksw");
};

template <class C> struct Frags { bf16x8 a[C::KSW][C::MF]; bf16x8 b[C::KSW][C::NF]; };

template <class C, class Src>
struct Loader {
  LAS char* lds; const Src& src;
  int lw, lane;
  unsigned voffA[C::GA], voffB[C::GB];
  static __device__ __forceinline__ unsigned rowmajor(int p, int ld) {
    const int row = p >> 3, c = (p & 7) ^ ((row >> 1) & 7);
    return row * ld + c * 16;
  }
  __device__ __forceinline__ Loader(LAS char* lds_, const Src& s, int lw_, int lane_) : lds(lds_), src(s), lw(lw_), lane(lane_) {
#pragma unroll
    for (int i = 0; i < C::GA; ++i) {
      const int p = (i * NL + lw) * 64 + lane;
      if constexpr (C::AMODE == 0) { const int r = p % C::BM; voffA[i] = (p - r + (r < src.rows ? r : (r & 31))) * 16; }
      else voffA[i] = rowmajor(p, Src::ldA);
    }
#pragma unroll
    for (int i = 0; i < C::GB; ++i) {
      const int p = (i * NL + lw) * 64 + lane;
      if constexpr (C::BMODE == 0) voffB[i] = p * 16;
      else if constexpr (C::BMODE == 1) voffB[i] = rowmajor(p, Src::ldB);
      else { const int q = p % (C::BK * 8), k = q >> 3, c = (q & 7) ^ ((((k >> 3) & 1) << 2) | (((k >> 1) & 1) << 1)); voffB[i] = k * Src::ldB + c * 16; }
    }
  }
  __device__ __forceinline__ void issueA(int k, unsigned buf) {
    const char* ga = src.baseA(k);
    LAS char* la = lds + buf + lw * 1024;
#pragma unroll
    for (int i = 0; i < C::GA; ++i) glds16<Src::AUXA>(ga + voffA[i], la + i * NL * 1024);
  }
  __device__ __forceinline__ void issueB(int k, unsigned buf) {
    LAS char* lb = lds + C::RA + buf + lw * 1024;
#pragma unroll
    for (int i = 0; i < C::GB; ++i) glds16<Src::AUXB>(src.baseB(k, i >= C::GB / 2) + voffB[i], lb + i * NL * 1024);
  }
  __device__ __forceinline__ void preloadB() {
#pragma unroll 1
    for (int s = 0; s < C::NSB; ++s) issueB(s, s * C::BBYTES);
  }
  static constexpr int cmin(int a, int b) { return a < b ? a : b; }
  static constexpr int cmax(int a, int b) { return a > b ? a : b; }
  static constexpr int T(int i) { return C::NSA * C::GA + cmin(i, C::NK - C::NSA) * C::GA + cmin(i, C::NK - C::NSB) * C::GB; }
  static constexpr int cnt(int k) {
    const int s = k + 1;
    const int pa = s < C::NSA ? (s + 1) * C::GA : T(s - C::NSA) + C::GA;
    const int pb = s < C::NSB ? 0 : T(s - C::NSB + 1);
    return T(k) - cmax(pa, pb);
  }
  static constexpr int key(int k) { return cnt(k) * 4 + (k + C::NSA < C::NK ? 2 : 0) + (k + C::NSB < C::NK ? 1 : 0); }
  static constexpr int seg_end(int k) { int e = k + 1; while (e < C::NK - 1 && key(e) == key(k)) ++e; return e; }
  unsigned ca, cb;
  template <int K> __device__ __forceinline__ void iter(int k) {
    static_assert(cnt(K) <= 63, "vmcnt");
    wait_vm<cnt(K)>();
    bar();
    if constexpr (K + C::NSA < C::NK) { issueA(k + C::NSA, ca); ca += C::ABYTES; if (ca == C::RA) ca = 0; }
    if constexpr (K + C::NSB < C::NK) { issueB(k + C::NSB, cb); cb += C::BBYTES; if (cb == C::RB) cb = 0; }
  }
  template <int K> __device__ __forceinline__ void seg() {
    if constexpr (K < C::NK - 1) {
      constexpr int E = seg_end(K);
      if constexpr (E - K == 1) iter<K>(K);
      else {
#pragma unroll 1
        for (int k = K; k < E; ++k) iter<K>(k);
      }
      seg<E>();
    }
  }
  template <class Hook> __device__ __forceinline__ void run(Hook hook) {
#pragma unroll 1
    for (int s = 0; s < C::NSA; ++s) issueA(s, s * C::ABYTES);
    wait_vm<(C::NSA - 1) * C::GA>();
    bar();
    hook();
    ca = 0; cb = 0;
    seg<0>();
  }
};

template <class C>
struct Consumer {
  LAS char* lds;
  int wid, lane, wm, wn, wk;
  unsigned rdA[2], rdB[4];

  __device__ __forceinline__ Consumer(LAS char* lds_, int wid_, int lane_) : lds(lds_), wid(wid_), lane(lane_) {
    wk = wid % C::WK; wn = (wid / C::WK) % C::WN; wm = wid / (C::WK * C::WN);
    const int fr = lane & 15, fq = lane >> 4;
    const int arow = wm * (C::BM / C::WM) + fr;
    const int ss0 = wk * C::KSW;
#pragma unroll
    for (int s = 0; s < 2; ++s) {
      const int ss = ss0 + s;
      if constexpr (C::AMODE == 0) rdA[s] = ((ss0 * 4 + fq) * C::BM + arow) * 16;
      else rdA[s] = arow * 128 + ((((ss & 1) * 4 + fq) ^ ((fr >> 1) & 7)) * 16);
    }
    if constexpr (C::BMODE <= 1) {
      const int brow = wn * (C::BN / C::WN) + fr;
#pragma unroll
      for (int s = 0; s < 2; ++s) {
        const int ss = ss0 + s;
        if constexpr (C::BMODE == 0) rdB[s] = C::RA + ((ss0 * 4 + fq) * C::BN + brow) * 16;
        else rdB[s] = C::RA + brow * 128 + ((((ss & 1) * 4 + fq) ^ ((fr >> 1) & 7)) * 16);
      }
    } else {
      const int g = lane >> 4, q = (lane >> 2) & 3, p = lane & 3;
#pragma unroll
      for (int j = 0; j < (C::BMODE == 4 ? 4 : 2); ++j) {
        const int jj = (C::BMODE == 4) ? j : 2 * wn + j;
        rdB[j] = C::RA + (32 * ss0 + 8 * g + q) * 128 + ((((jj ^ (((g & 1) << 1) | (q >> 1))) << 1) | (p >> 1)) * 16) + 8 * (p & 1);
      }
    }
  }

  __device__ __forceinline__ void read(Frags<C>& f, unsigned bufa, unsigned bufb) {
    LAS char* a = lds + bufa; LAS char* b = lds + bufb;
#pragma unroll
    for (int s = 0; s < C::KSW; ++s) {
      if constexpr (C::BMODE == 0) {
#pragma unroll
        for (int n = 0; n < C::NF; ++n) f.b[s][n] = *(const LAS bf16x8*)(b + rdB[0] + s * (C::BN * 64) + n * 256);
      } else if constexpr (C::BMODE == 1) {
#pragma unroll
        for (int n = 0; n < C::NF; ++n) f.b[s][n] = *(const LAS bf16x8*)(b + rdB[s] + n * 2048);
      } else {
#pragma unroll
        for (int n = 0; n < C::NF; ++n) {
          const int j = (C::BMODE == 4) ? n : (n & 1), mat = (C::BMODE == 4) ? 0 : (n >> 1);
          bf16x4 lo = __builtin_amdgcn_ds_read_tr16_b64_v4i16((LAS bf16x4*)(b + rdB[j] + mat * (C::BK * 128) + s * 4096));
          bf16x4 hi = __builtin_amdgcn_ds_read_tr16_b64_v4i16((LAS bf16x4*)(b + rdB[j] + mat * (C::BK * 128) + s * 4096 + 512));
          f.b[s][n] = __builtin_shufflevector(lo, hi, 0, 1, 2, 3, 4, 5, 6, 7);
        }
      }
      if constexpr (C::AMODE == 0) {
#pragma unroll
        for (int m = 0; m < C::MF; ++m) f.a[s][m] = *(const LAS bf16x8*)(a + rdA[0] + s * (C::BM * 64) + m * 256);
      } else {
#pragma unroll
        for (int m = 0; m < C::MF; ++m) f.a[s][m] = *(const LAS bf16x8*)(a + rdA[s] + m * 2048);
      }
    }
  }

  __device__ __forceinline__ void mma(f32x4 (&acc)[C::MF][C::NF], const Frags<C>& f) {
#pragma unroll
    for (int s = 0; s < C::KSW; ++s)
#pragma unroll
      for (int m = 0; m < C::MF; ++m)
#pragma unroll
        for (int n = 0; n < C::NF; ++n)
          acc[m][n] = C::SWAP ? __builtin_amdgcn_mfma_f32_16x16x32_bf16(f.b[s][n], f.a[s][m], acc[m][n], 0, 0, 0)
                              : __builtin_amdgcn_mfma_f32_16x16x32_bf16(f.a[s][m], f.b[s][n], acc[m][n], 0, 0, 0);
  }
  static constexpr int NRD = C::KSW * (C::MF + (C::BMODE >= 2 ? 2 : 1) * C::NF);
  static constexpr int NMM = C::KSW * C::MF * C::NF;
  __device__ __forceinline__ void interleave() {
    constexpr int RPM = (NRD + NMM - 3) / (NMM - 2);
#pragma unroll
    for (int i = 0; i < NMM; ++i) {
      __builtin_amdgcn_sched_group_barrier(0x100, RPM, 0);
      __builtin_amdgcn_sched_group_barrier(0x008, 1, 0);
    }
  }
  unsigned na, nb;
  __device__ __forceinline__ void step(f32x4 (&acc)[C::MF][C::NF], Frags<C>& cur, Frags<C>& nxt) {
    na += C::ABYTES; if (na == C::RA) na = 0;
    nb += C::BBYTES; if (nb == C::RB) nb = 0;
    wait_lgkm0(); bar();
    __builtin_amdgcn_sched_barrier(0);
    read(nxt, na, nb);
    mma(acc, cur);
    interleave();
    __builtin_amdgcn_sched_barrier(0);
  }

  template <class Hook, class Hook2> __device__ __forceinline__ void run(f32x4 (&acc)[C::MF][C::NF], Hook hook, Hook2 hook2) {
    Frags<C> f0, f1;
#pragma unroll
    for (int m = 0; m < C::MF; ++m)
#pragma unroll
      for (int n = 0; n < C::NF; ++n) acc[m][n] = (f32x4){0.f, 0.f, 0.f, 0.f};
    bar();
    if (wid == 0) { STAMP(C::KID, 1); CSTAMP(C::KID, 8); }
    hook();
    read(f0, 0, 0);
    na = 0; nb = 0;
    if (wid == 0) STAMP(C::KID, 4);
#pragma unroll 1
    for (int k = 0; k + 2 < C::NK; k += 2) {
      step(acc, f0, f1);
      step(acc, f1, f0);
    }
    if (wid == 0) STAMP(C::KID, 5);
    hook2();
    if (wid == 0) STAMP(C::KID, 6);
    if constexpr (C::NK % 2 == 0) {
      step(acc, f0, f1);
      wait_lgkm0(); bar();
      mma(acc, f1);
    } else {
      wait_lgkm0(); bar();
      mma(acc, f0);
    }
    if (wid == 0) { STAMP(C::KID, 2); CSTAMP(C::KID, 9); }
  }

  static constexpr bool SPLM = (C::MF % 2 == 0);
  static constexpr int RM = SPLM ? C::MF / 2 : C::MF, RN = SPLM ? C::NF : C::NF / 2;
  __device__ __forceinline__ void reduce_k(f32x4 (&acc)[C::MF][C::NF], f32x4 (&red)[RM][RN]) {
    static_assert(C::WK == 2, "");
    static_assert(LDS_X + C::NC * RM * RN * 1024 <= LDS_MISC || RM * RN == 6, "");
    LAS f32x4* x = (LAS f32x4*)(lds + LDS_X - (RM * RN == 6 ? 1024 : 0));
#pragma unroll
    for (int m = 0; m < RM; ++m)
#pragma unroll
      for (int n = 0; n < RN; ++n) {
        const f32x4 lo = acc[m][n], hi = SPLM ? acc[m + RM][n] : acc[m][n + RN];
        x[(wid * RM * RN + m * RN + n) * 64 + lane] = wk ? lo : hi;
        red[m][n] = wk ? hi : lo;
      }
    wait_lgkm0(); bar();
#pragma unroll
    for (int m = 0; m < RM; ++m)
#pragma unroll
      for (int n = 0; n < RN; ++n) red[m][n] += x[((wid ^ 1) * RM * RN + m * RN + n) * 64 + lane];
  }
};

constexpr int FLE = 0;
constexpr int FL0 = 32;
constexpr int FL1 = FL0 + 2048;
constexpr int FL2 = FL1 + 16 * 16;
constexpr int FL3 = FL2 + 8 * 32;
constexpr int FLC = FL3 + 8 * 32;

__device__ __forceinline__ void publish(LAS unsigned* arr, unsigned nwaves, u64* flag, u64 tag, int lane, int kid) {
  if (threadIdx.x < 64) STAMP(kid, 7);
  wait_vm<0>();
  if (lane == 0) {
    const unsigned old = __hip_atomic_fetch_add(arr, 1u, RLX_WG);
    if (old == nwaves - 1) __hip_atomic_store(flag, tag, RLX_AGENT);
  }
}
__device__ __forceinline__ void lds_wait(LAS unsigned* w, unsigned want) {
  const unsigned a = (unsigned)(size_t)w;
#pragma unroll 1
  for (unsigned i = 0; i < (1u << 24); ++i) {
    unsigned v;
    asm volatile("ds_read_b32 %0, %1\n\ts_waitcnt lgkmcnt(0)" : "=v"(v) : "v"(a) : "memory");
    if (__builtin_amdgcn_readfirstlane(v) >= want) break;
    __builtin_amdgcn_s_sleep(1);
  }
}
template <int SLEEP, int NW>
__device__ __forceinline__ void await(const u64* word, bool need, u64 tag, int lw, LAS unsigned* go, unsigned epoch) {
  if (lw == 0) {
    const u64 t0 = wall_clock64();
    for (;;) {
      bool ok = true;
#pragma unroll
      for (int i = 0; i < NW; ++i) ok &= __hip_atomic_load(word + i * 64, RLX_AGENT) == tag;
      if (__all(!need || ok)) break;
      if (wall_clock64() - t0 > 2000000ull) break;
      __builtin_amdgcn_s_sleep(SLEEP);
    }
    __hip_atomic_store(go, epoch, RLX_WG);
  } else {
    lds_wait(go, epoch);
  }
  __builtin_amdgcn_fence(__ATOMIC_ACQUIRE, "wavefront");
}
#define P1_PRE 1
#define PRE_LATE 0
#define PSLEEP 4

using C1 = Cfg<1, 80, 128, 64, 1, 4, 1, 4, 6, 32, 0, 1, 0>;
#define P2A 4
#define P2B 10
using C2 = Cfg<2, 64, 80, 64, 2, 1, 2, P2A, P2B, 20, 0, 1>;
#define P3A 4
#define P3B 8
using C3 = Cfg<3, 256, 128, 32, 4, 2, 1, P3A, P3B, 20, 0, 3>;
#define P4A 4
#define P4B 10
using C4 = Cfg<4, 96, 64, 64, 2, 1, 2, P4A, P4B, 27, 0, 4>;
#define AUXS 0
struct Src1 {
  const char* a; const char* b;
  static constexpr int ldA = 0, ldB = S_ * 2, AUXA = AUXH, AUXB = AUXS, rows = 80;
  __device__ __forceinline__ const char* baseA(int k) const { return a + k * (80 * 128); }
  __device__ __forceinline__ const char* baseB(int k, int) const { return b + k * 128; }
};
struct Src2 {
  const char* a; const char* bre; const char* bim;
  static constexpr int ldA = 0, ldB = D_ * 2, AUXA = AUXH, AUXB = 0, rows = 64;
  __device__ __forceinline__ const char* baseA(int k) const { return a + k * (64 * 128); }
  __device__ __forceinline__ const char* baseB(int k, int) const { return k < 10 ? bre + k * 128 : bim + (k - 10) * 128; }
};
struct Src3 {
  const char* a; const char* bg; const char* bu;
  static constexpr int ldA = 0, ldB = F_ * 2, AUXA = AUXH, AUXB = 0, rows = 256;
  __device__ __forceinline__ const char* baseA(int k) const { return a + k * (256 * 64); }
  __device__ __forceinline__ const char* baseB(int k, int up) const { return (up ? bu : bg) + (size_t)k * (32 * F_ * 2); }
};
struct Src4 {
  const char* a; const char* b; int rows;
  static constexpr int ldA = 0, ldB = D_ * 2, AUXA = AUXH, AUXB = 0;
  __device__ __forceinline__ const char* baseA(int k) const { return a + k * (96 * 128); }
  __device__ __forceinline__ const char* baseB(int k, int) const { return b + (size_t)k * (64 * D_ * 2); }
};

struct Params {
  const u16* x; const u16* sre; const u16* sim; const u16* hre; const u16* him; const u16* wg; const u16* wu; const u16* wd;
  u16* xnI; u16* pqI; u16* hI; u16* hhI; f32x2* stats; float* csp; u64* flags; u16* out;
};

template <int N> __device__ __forceinline__ void bars() {
#pragma unroll 1
  for (int i = 0; i < N; ++i) bar();
}

__global__ __launch_bounds__(NWAVES * 64) void mega(const Params p) {
  __shared__ __attribute__((aligned(1024))) char smem_[LDS_TOTAL];
  LAS char* lds = (LAS char*)smem_;
  const int wid = __builtin_amdgcn_readfirstlane(threadIdx.x >> 6), lane = threadIdx.x & 63;
  const int bid = blockIdx.x, g = bid & 7, idx = bid >> 3;
  LAS unsigned* arr = (LAS unsigned*)(lds + LDS_MISC + 256);
  if (wid == 0) STAMP(0, 0);
  const u64 tag = __hip_atomic_load(p.flags + FLE, RLX_AGENT) + 1ull;
  if (threadIdx.x < 16) arr[threadIdx.x] = 0u;
  wait_lgkm0(); bar();
  const int j1 = idx & 7, mq = idx >> 3, isQ = mq >> 1, t1 = g * 256 + (mq & 1) * 128;
  const int j2 = idx & 7, mt2 = g * 4 + (idx >> 3);
  const int mt3 = (g & 6) + (idx & 1), j3 = (g & 1) * 14 + (idx >> 1);
  const bool on3 = idx < 28 && j3 < 27;
  const int lin4 = g * 28 + idx, mt4 = lin4 / 10, j4 = lin4 % 10;
  const bool on4 = idx < 28 && lin4 < 220;
  LAS f32x4* rs = (LAS f32x4*)(lds + LDS_X);
  LAS float* csl = (LAS float*)(lds + LDS_X + 4096);

  if (wid >= 8) {
    const int lw = wid - 8;
    {
      LAS char* scr = lds + LDS_MISC;
      asm volatile("" :: "v"(tag) : "memory");
      if (lane == 0) __hip_atomic_fetch_add(arr + 5, 1u, RLX_WG);
      const char* sb = (const char*)((idx & 16) ? p.sim : p.sre) + (size_t)g * (256 * S_ * 2) + (size_t)(idx & 15) * 65536 + lw * 32768 + lane * 128;
      lds_wait(arr + 4, 8);
      touch<0>(sb, scr); touch<0>(sb + 8192, scr);
      lds_wait(arr + 8, 8);
      touch<0>(sb + 16384, scr); touch<0>(sb + 24576, scr);
      if (lane < 25) touch<0>((const char*)(lw ? p.him : p.hre) + (bid * 25 + lane) * 128, scr);
      Src1 s{(const char*)(p.xnI + (size_t)j1 * 256 * 80 * 8), (const char*)((isQ ? p.sim : p.sre) + (size_t)t1 * S_)};
      Loader<C1, Src1> L(lds, s, lw, lane);
      L.preloadB();
      lds_wait(arr + 5, 10);
      await<PSLEEP, 4>(p.flags + FL0 + g * 256 + lane, true, tag, lw, arr + 3, 1u);
      if (bid == 0 && lw == 0 && lane == 0) __hip_atomic_store(p.flags + FLE, tag, RLX_AGENT);
      if (lw == 0) STAMP(1, 0);
      wait_vm<0>();
      L.run([] {});
      bar();
    }
    {
      Src2 s{(const char*)(p.pqI + (size_t)mt2 * 160 * 64 * 8), (const char*)(p.hre + (size_t)j2 * 80 * D_), (const char*)(p.him + (size_t)j2 * 80 * D_)};
      Loader<C2, Src2> L(lds, s, lw, lane);
      if (PRE_LATE) lds_wait(arr + 0, C1::NC);
      L.preloadB();
      lds_wait(arr + 0, C1::NC);
      await<PSLEEP, 1>(p.flags + FL1 + (mt2 >> 1) * 16 + (lane & 15), true, tag, lw, arr + 3, 2u);
      if (lw == 0) STAMP(2, 0);
      wait_vm<0>();
      L.run([] {});
      bar();
    }
    if (on3) {
      Src3 s{(const char*)(p.hI + (size_t)mt3 * 80 * 256 * 8), (const char*)(p.wg + j3 * 64), (const char*)(p.wu + j3 * 64)};
      Loader<C3, Src3> L(lds, s, lw, lane);
      bar();
      if (PRE_LATE) lds_wait(arr + 1, C2::NC);
      L.preloadB();
      lds_wait(arr + 1, C2::NC);
      await<PSLEEP, 1>(lane < 32 ? p.flags + FL2 + mt3 * 32 + lane : p.flags + FLC + (((lane >> 2) & 1) * 27 + j3) * 4 + (lane & 3), true, tag, lw, arr + 3, 3u);
      if (lw == 0) STAMP(3, 0);
      wait_vm<0>();
      L.run([] {});
      bar();
    } else {
      bar();
    }
    if (on4) {
      Src4 s{(const char*)(p.hhI + (size_t)mt4 * 216 * 96 * 8), (const char*)(p.wd + j4 * 64), min(96, S_ - mt4 * 96)};
      Loader<C4, Src4> L(lds, s, lw, lane);
      if (PRE_LATE && on3) lds_wait(arr + 2, C3::NC);
      L.preloadB();
      const int ra = (mt4 * 96) >> 8, rb = min(mt4 * 96 + 95, S_ - 1) >> 8;
      if (on3) {
        lds_wait(arr + 2, C3::NC);
        await<PSLEEP, 1>(p.flags + FL3 + (lane < 32 ? ra : rb) * 32 + (lane & 31), (lane & 31) < 27, tag, lw, arr + 3, 4u);
      } else {
        await<32, 1>(p.flags + FL3 + (lane < 32 ? ra : rb) * 32 + (lane & 31), (lane & 31) < 27, tag, lw, arr + 3, 4u);
      }
      if (lw == 0) STAMP(4, 0);
      wait_vm<0>();
      L.run([] {});
      bar();
      bar();
    }
    return;
  }

  const int fr = lane & 15, fq = lane >> 4;
  {
    const u16* xr = p.x + (bid * 8 + wid) * D_;
    bf16x8 v0 = *(const bf16x8*)(xr + lane * 8);
    bf16x8 v1 = *(const bf16x8*)(xr + 512 + (lane & 15) * 8);
    float f[16];
#pragma unroll
    for (int i = 0; i < 8; ++i) { f[i] = bf2f((u16)v0[i]); f[8 + i] = bf2f((u16)v1[i]); }
    float s = 0.f, s1 = 0.f;
#pragma unroll
    for (int i = 0; i < 8; ++i) { s += f[i]; s1 += f[8 + i]; }
    asm volatile("" :: "v"(s), "v"(s1) : "memory");
    if (lane == 0) __hip_atomic_fetch_add(arr + 4, 1u, RLX_WG);
    if (wid == 0) STAMP(0, 4);
    s += lane < 16 ? s1 : 0.f;
#pragma unroll
    for (int o = 32; o >= 1; o >>= 1) s += __shfl_xor(s, o);
    const float mean = s * (1.0f / D_);
    float q = 0.f, q1 = 0.f;
#pragma unroll
    for (int i = 0; i < 8; ++i) { const float d = f[i] - mean, e = f[8 + i] - mean; q += d * d; q1 += e * e; }
    q += lane < 16 ? q1 : 0.f;
#pragma unroll
    for (int o = 32; o >= 1; o >>= 1) q += __shfl_xor(q, o);
    const float mb = rbf(mean), vb = rbf(q * (1.0f / D_));
    const float rden = 1.0f / rbf(sqrtf(rbf(vb + 1e-5f)));
    u32x4 w0, w1;
#pragma unroll
    for (int i = 0; i < 4; ++i) {
      w0[i] = pk2(rbf(f[2 * i] - mb) * rden, rbf(f[2 * i + 1] - mb) * rden);
      w1[i] = pk2(rbf(f[8 + 2 * i] - mb) * rden, rbf(f[9 + 2 * i] - mb) * rden);
    }
    LAS char* tb = lds + LDS_X;
    *(LAS u32x4*)(tb + wid * 1296 + lane * 16) = w0;
    if (lane < 16) *(LAS u32x4*)(tb + wid * 1296 + 1024 + lane * 16) = w1;
    wait_lgkm0();
    if (lane == 0) __hip_atomic_fetch_add(arr + 7, 1u, RLX_WG);
    lds_wait(arr + 7, 8);
    auto rsrc = __builtin_amdgcn_make_buffer_rsrc(p.xnI, 0, S_ * D_ * 2, 0x00020000);
#pragma unroll
    for (int h = 0; h < 2; ++h) {
      const int d = h * 512 + wid * 64 + lane;
      if (d < D_) {
        bf16x8 o;
#pragma unroll
        for (int r = 0; r < 8; ++r) o[r] = *(const LAS short*)(tb + r * 1296 + d * 2);
        const int j = d / 80, n = d % 80;
        __builtin_amdgcn_raw_buffer_store_b128(__builtin_bit_cast(u32x4, o), rsrc, ((j * 256 + bid) * 80 + n) * 16, 0, 16);
      }
    }
    asm volatile("" ::: "memory");
    if (lane == 0) __hip_atomic_fetch_add(arr + 8, 1u, RLX_WG);
    if (wid == 0) STAMP(0, 7);
    wait_vm<0>();
    lds_wait(arr + 5, 2);
    unsigned old = 0;
    if (lane == 0) old = __hip_atomic_fetch_add(arr + 5, 1u, RLX_WG);
    if (__builtin_amdgcn_readfirstlane(old) == 9u && lane < 8) __hip_atomic_store(p.flags + FL0 + lane * 256 + bid, tag, RLX_AGENT);
    if (wid == 0) STAMP(0, 3);
  }
  if (wid < C1::NC) {
    Consumer<C1> L(lds, wid, lane);
    f32x4 acc[C1::MF][C1::NF];
    L.run(acc, [] {}, [] {});
    const float sg = isQ ? -1.f : 1.f;
#pragma unroll
    for (int n = 0; n < 2; ++n) {
      const int t = t1 + L.wn * 32 + n * 16 + fr, mt = t >> 6, r = t & 63;
#pragma unroll
      for (int m = 0; m < 5; ++m) {
        const int koct = isQ * 80 + j1 * 10 + m * 2 + (fq >> 1);
        u32x2 w; w[0] = pk2(sg * acc[m][n][0], sg * acc[m][n][1]); w[1] = pk2(sg * acc[m][n][2], sg * acc[m][n][3]);
        st8(p.pqI + (((size_t)mt * 160 + koct) * 64 + r) * 8 + (fq & 1) * 4, w);
      }
    }
    publish(arr + 0, C1::NC, p.flags + FL1 + (t1 >> 7) * 16 + isQ * 8 + j1, tag, lane, 1);
    if (wid == 0) STAMP(1, 3);
  } else {
    bars<C1::NBAR>();
    if (bid < 216) {
      const int c = bid >> 2, qd = bid & 3, mat = c / 27, c0 = (c % 27) * 64, w4 = wid - 4;
      const u16* w = (mat ? p.wu : p.wg) + (size_t)(qd * 160 + w4 * 40 + (lane >> 3)) * F_ + c0 + (lane & 7) * 8;
      bf16x8 v[5];
#pragma unroll
      for (int i = 0; i < 5; ++i) v[i] = *(const bf16x8*)(w + (size_t)i * 8 * F_);
      float a[8] = {0, 0, 0, 0, 0, 0, 0, 0};
#pragma unroll
      for (int i = 0; i < 5; ++i)
#pragma unroll
        for (int e = 0; e < 8; ++e) a[e] += bf2f((u16)v[i][e]);
#pragma unroll
      for (int e = 0; e < 8; ++e) { a[e] += __shfl_xor(a[e], 8); a[e] += __shfl_xor(a[e], 16); a[e] += __shfl_xor(a[e], 32); }
      LAS float* sf = (LAS float*)(lds + LDS_X + 20480);
      if (lane < 8) {
        *(LAS f32x4*)(sf + w4 * 64 + lane * 8) = (f32x4){a[0], a[1], a[2], a[3]};
        *(LAS f32x4*)(sf + w4 * 64 + lane * 8 + 4) = (f32x4){a[4], a[5], a[6], a[7]};
      }
      wait_lgkm0();
      unsigned old = 0;
      if (lane == 0) old = __hip_atomic_fetch_add(arr + 6, 1u, RLX_WG);
      if (__builtin_amdgcn_readfirstlane(old) == 3u) {
        const float t = (sf[lane] + sf[64 + lane]) + (sf[128 + lane] + sf[192 + lane]);
        __hip_atomic_store(p.csp + qd * (2 * F_) + mat * F_ + c0 + lane, t, RLX_AGENT);
        wait_vm<0>();
        if (lane == 0) __hip_atomic_store(p.flags + FLC + bid, tag, RLX_AGENT);
      }
    }
  }
  if (wid < C2::NC) {
    Consumer<C2> L(lds, wid, lane);
    f32x4 acc[C2::MF][C2::NF];
    const int t = mt2 * 64 + L.wm * 32 + L.wk * 16 + fr;
    bf16x4 xv[C2::NF];
#pragma unroll
    for (int n = 0; n < C2::NF; ++n) xv[n] = *(const bf16x4*)(p.x + (size_t)t * D_ + j2 * 80 + n * 16 + fq * 4);
#pragma unroll
    for (int n = 0; n < C2::NF; ++n) asm volatile("" : "+v"(xv[n]));
    L.run(acc, [] {}, [] {});
    f32x4 red[1][5];
    L.reduce_k(acc, red);
    float hv[C2::NF][4];
    float s = 0.f;
#pragma unroll
    for (int n = 0; n < C2::NF; ++n) {
      u32x2 w;
      w[0] = pk2(red[0][n][0] + bf2f((u16)xv[n][0]), red[0][n][1] + bf2f((u16)xv[n][1]));
      w[1] = pk2(red[0][n][2] + bf2f((u16)xv[n][2]), red[0][n][3] + bf2f((u16)xv[n][3]));
      hv[n][0] = __uint_as_float(w[0] << 16); hv[n][1] = __uint_as_float(w[0] & 0xffff0000u);
      hv[n][2] = __uint_as_float(w[1] << 16); hv[n][3] = __uint_as_float(w[1] & 0xffff0000u);
      s += (hv[n][0] + hv[n][1]) + (hv[n][2] + hv[n][3]);
      const int koct = j2 * 10 + n * 2 + (fq >> 1);
      st8(p.hI + (((size_t)(t >> 8) * 80 + koct) * 256 + (t & 255)) * 8 + (fq & 1) * 4, w);
    }
    s += __shfl_xor(s, 16); s += __shfl_xor(s, 32);
    const float mean = s * (1.0f / 80.0f);
    float q = 0.f;
#pragma unroll
    for (int n = 0; n < C2::NF; ++n)
#pragma unroll
      for (int r = 0; r < 4; ++r) { const float d = hv[n][r] - mean; q += d * d; }
    q += __shfl_xor(q, 16); q += __shfl_xor(q, 32);
    if (fq == 0) st8(p.stats + t * 8 + j2, __builtin_bit_cast(u32x2, (f32x2){mean, q}));
    publish(arr + 1, C2::NC, p.flags + FL2 + g * 32 + idx, tag, lane, 2);
    if (wid == 0) STAMP(2, 3);
  } else {
    bars<C2::NBAR>();
  }
  if (on3) {
    Consumer<C3> L(lds, wid, lane);
    f32x4 acc[C3::MF][C3::NF];
    f32x2 pv[4]; float cv[4] = {0.f, 0.f, 0.f, 0.f};
    const int sr = wid * 32 + (lane >> 1);
    L.run(acc, [&] {
      const f32x2* sp = p.stats + (mt3 * 256 + sr) * 8 + (lane & 1) * 4;
#pragma unroll
      for (int i = 0; i < 4; ++i) pv[i] = __builtin_bit_cast(f32x2, ld8(sp + i));
      if (wid < 2) {
#pragma unroll
        for (int i = 0; i < 4; ++i) cv[i] = __hip_atomic_load(p.csp + i * (2 * F_) + wid * F_ + j3 * 64 + lane, RLX_AGENT);
      }
    }, [&] {
      float mean = (pv[0][0] + pv[1][0]) + (pv[2][0] + pv[3][0]);
      mean = (mean + __shfl_xor(mean, 1)) * 0.125f;
      float q = (pv[0][1] + pv[1][1]) + (pv[2][1] + pv[3][1]), dd = 0.f;
#pragma unroll
      for (int i = 0; i < 4; ++i) { const float d = pv[i][0] - mean; dd += d * d; }
      q += 80.0f * dd;
      q += __shfl_xor(q, 1);
      const float den = rbf(__builtin_amdgcn_sqrtf(rbf(rbf(q * (1.0f / D_)) + 1e-5f)));
      if ((lane & 1) == 0) rs[sr] = (f32x4){-rbf(mean), den, -1.44269504089f * __builtin_amdgcn_rcpf(den), 0.f};
      if (wid < 2) csl[wid * 64 + lane] = (cv[0] + cv[1]) + (cv[2] + cv[3]);
    });
#pragma unroll
    for (int n = 0; n < 2; ++n) {
      const int fl = L.wn * 32 + n * 16 + fq * 4, f0 = j3 * 64 + fl;
      const f32x4 cg = *(const LAS f32x4*)(csl + fl), cu = *(const LAS f32x4*)(csl + 64 + fl);
#pragma unroll
      for (int m = 0; m < C3::MF; ++m) {
        const int rl = L.wm * 64 + m * 16 + fr, t = mt3 * 256 + rl;
        const f32x4 st = rs[rl];
        float o[4];
#pragma unroll
        for (int r = 0; r < 4; ++r) {
          const float e = __builtin_amdgcn_exp2f(__builtin_fmaf(cg[r], st[0], acc[m][n][r]) * st[2]);
          o[r] = __builtin_fmaf(cu[r], st[0], acc[m][n + 2][r]) * __builtin_amdgcn_rcpf(__builtin_fmaf(e, st[1], st[1]));
        }
        u32x2 w; w[0] = pk2(o[0], o[1]); w[1] = pk2(o[2], o[3]);
        const int t4 = t / 96;
        st8(p.hhI + (((size_t)t4 * 216 + (f0 >> 3)) * 96 + (t - t4 * 96)) * 8 + (fq & 1) * 4, w);
      }
    }
    publish(arr + 2, C3::NC, p.flags + FL3 + mt3 * 32 + j3, tag, lane, 3);
    if (wid == 0) STAMP(3, 3);
  } else if (idx >= 28) {
    const int l0 = ((g * 4 + idx - 28) * 8 + wid) * 68;
#pragma unroll
    for (int i = 0; i < 2; ++i) { const int o = i * 64 + lane; if (o < 68 && l0 + o < 17280) touch<0>((const char*)p.wd + (size_t)(l0 + o) * 128, lds + LDS_MISC); }
    wait_vm<0>();
  }
  if (on4) {
    if (wid < C4::NC) {
      Consumer<C4> L(lds, wid, lane);
      f32x4 acc[C4::MF][C4::NF];
      const int tb = mt4 * 96 + L.wm * 48 + fr, c0 = j4 * 64 + L.wk * 32 + fq * 4;
      u32x2 hv[3][2];
      L.run(acc, [&] {
#pragma unroll
        for (int m = 0; m < 3; ++m)
#pragma unroll
          for (int n = 0; n < 2; ++n) {
            const int t = min(tb + m * 16, S_ - 1), c = c0 + n * 16;
            hv[m][n] = ld8(p.hI + (((size_t)(t >> 8) * 80 + (c >> 3)) * 256 + (t & 255)) * 8 + (c & 4));
          }
      }, [] {});
      f32x4 red[3][2];
      L.reduce_k(acc, red);
#pragma unroll
      for (int m = 0; m < 3; ++m)
#pragma unroll
        for (int n = 0; n < 2; ++n) {
          const int t = tb + m * 16;
          u32x2 w;
          w[0] = pk2(red[m][n][0] + __uint_as_float(hv[m][n][0] << 16), red[m][n][1] + __uint_as_float(hv[m][n][0] & 0xffff0000u));
          w[1] = pk2(red[m][n][2] + __uint_as_float(hv[m][n][1] << 16), red[m][n][3] + __uint_as_float(hv[m][n][1] & 0xffff0000u));
          if (t < S_) *(u32x2*)(p.out + (size_t)t * D_ + c0 + n * 16) = w;
        }
      if (wid == 0) { wait_vm<0>(); STAMP(4, 3); }
    }
  }
}

extern "C" void kernel_launch(void* const* d_in, const int* in_sizes, int n_in, void* d_out, int out_size,
                              void* d_ws, size_t ws_size, hipStream_t stream) {
  char* ws = (char*)d_ws;
  Params p;
  p.x = (const u16*)d_in[0]; p.sre = (const u16*)d_in[1]; p.sim = (const u16*)d_in[2];
  p.hre = (const u16*)d_in[3]; p.him = (const u16*)d_in[4];
  p.wg = (const u16*)d_in[6]; p.wu = (const u16*)d_in[7]; p.wd = (const u16*)d_in[8];
  p.flags = (u64*)ws;
  p.csp = (float*)(ws + 65536);
  p.stats = (f32x2*)(ws + 131072);
  p.xnI = (u16*)(ws + (1u << 20));
  p.pqI = (u16*)(ws + (4u << 20));
  p.hI = (u16*)(ws + (10u << 20));
  p.hhI = (u16*)(ws + (13u << 20));
  p.out = (u16*)d_out;
  mega<<<256, NWAVES * 64, 0, stream>>>(p);
}
```

```cpp
#include <hip/hip_runtime.h>
#include <hip/hip_bf16.h>
#define AUXH 16

typedef unsigned short u16;
typedef unsigned long long u64;
typedef short bf16x8 __attribute__((ext_vector_type(8)));
typedef short bf16x4 __attribute__((ext_vector_type(4)));
typedef float f32x4 __attribute__((ext_vector_type(4)));
typedef float f32x2 __attribute__((ext_vector_type(2)));
typedef unsigned u32x2 __attribute__((ext_vector_type(2)));
typedef unsigned u32x4 __attribute__((ext_vector_type(4)));
#define LAS __attribute__((address_space(3)))
#define GAS __attribute__((address_space(1)))
#define RLX_AGENT __ATOMIC_RELAXED, __HIP_MEMORY_SCOPE_AGENT
#define RLX_WG __ATOMIC_RELAXED, __HIP_MEMORY_SCOPE_WORKGROUP

constexpr int S_ = 2048, D_ = 640, F_ = 1728;

__device__ __forceinline__ float bf2f(u16 v) { return __uint_as_float(((unsigned)v) << 16); }
typedef __bf16 bf16x2n __attribute__((ext_vector_type(2)));
__device__ __forceinline__ unsigned pk2(float lo, float hi) {
  return __builtin_bit_cast(unsigned, __builtin_convertvector((f32x2){lo, hi}, bf16x2n));
}
__device__ __forceinline__ float rbf(float v) { return __uint_as_float(pk2(v, v) << 16); }

#define CSTAMP(kid, i)
#define STAMP(kid, i)
template <int N> __device__ __forceinline__ void wait_vm() { asm volatile("s_waitcnt vmcnt(%0)" ::"n"(N) : "memory"); }
__device__ __forceinline__ void wait_lgkm0() { asm volatile("" ::: "memory"); __builtin_amdgcn_s_waitcnt(0xC07F); asm volatile("" ::: "memory"); }
__device__ __forceinline__ void bar() { asm volatile("" ::: "memory"); __builtin_amdgcn_s_barrier(); asm volatile("" ::: "memory"); }
template <int N, int STEP> __device__ __forceinline__ void wait_dyn(int n) {
  if constexpr (N <= 0) wait_vm<0>();
  else { if (n >= N) wait_vm<N>(); else wait_dyn<N - STEP, STEP>(n); }
}

template <int AUX> __device__ __forceinline__ void glds16(const char* g, LAS char* l) {
  __builtin_amdgcn_global_load_lds((const GAS void*)g, (LAS void*)l, 16, 0, AUX);
}
template <int AUX> __device__ __forceinline__ void touch(const char* g, LAS char* scratch) {
  __builtin_amdgcn_global_load_lds((const GAS void*)g, (LAS void*)scratch, 4, 0, AUX);
}
__device__ __forceinline__ void st8(void* p, u32x2 v) { __hip_atomic_store((u64*)p, __builtin_bit_cast(u64, v), RLX_AGENT); }
__device__ __forceinline__ u32x2 ld8(const void* p) { return __builtin_bit_cast(u32x2, __hip_atomic_load((u64*)p, RLX_AGENT)); }

constexpr int NL = 2;
constexpr int NWAVES = 10;
constexpr int LDS_RING = 136 * 1024;
constexpr int LDS_X = LDS_RING;
constexpr int LDS_TOTAL = 160 * 1024;
constexpr int LDS_MISC = LDS_TOTAL - 512;

template <int KID_, int BM_, int BN_, int BK_, int WM_, int WN_, int WK_, int NSA_, int NSB_, int NK_, int AMODE_, int BMODE_, int SWAP_ = 1>
struct Cfg {
  static constexpr int KID = KID_, BM = BM_, BN = BN_, BK = BK_, WM = WM_, WN = WN_, WK = WK_, NSA = NSA_, NSB = NSB_, NK = NK_;
  static constexpr int AMODE = AMODE_, BMODE = BMODE_, SWAP = SWAP_;
  static constexpr int NC = WM * WN * WK;
  static constexpr int MF = BM / WM / 16, NF = BN / WN / 16, KSW = BK / 32 / WK;
  static constexpr int ABYTES = BM * BK * 2, BBYTES = BN * BK * 2;
  static constexpr int PA = ABYTES / 1024, PB = BBYTES / 1024;
  static constexpr int GA = PA / NL, GB = PB / NL, G = GA + GB;
  static constexpr int RA = NSA * ABYTES, RB = NSB * BBYTES;
  static constexpr int NBAR = NK + (WK == 2 ? 2 : 1);
  static_assert(PA % NL == 0 && PB % NL == 0, "pieces");
  static_assert(RA + RB <= LDS_RING, "lds");
  static_assert(NSA <= NK && NSB <= NK && NSB >= NSA, "stages");
  static_assert(KSW >= 1 && KSW <= 2, "ksw");
};

template <class C> struct Frags { bf16x8 a[C::KSW][C::MF]; bf16x8 b[C::KSW][C::NF]; };

template <class C, class Src>
struct Loader {
  LAS char* lds; const Src& src;
  int lw, lane;
  unsigned voffA[C::GA], voffB[C::GB];
  static __device__ __forceinline__ unsigned rowmajor(int p, int ld) {
    const int row = p >> 3, c = (p & 7) ^ ((row >> 1) & 7);
    return row * ld + c * 16;
  }
  __device__ __forceinline__ Loader(LAS char* lds_, const Src& s, int lw_, int lane_) : lds(lds_), src(s), lw(lw_), lane(lane_) {
#pragma unroll
    for (int i = 0; i < C::GA; ++i) {
      const int p = (i * NL + lw) * 64 + lane;
      if constexpr (C::AMODE == 0) { const int r = p % C::BM; voffA[i] = (p - r + (r < src.rows ? r : (r & 31))) * 16; }
      else voffA[i] = rowmajor(p, Src::ldA);
    }
#pragma unroll
    for (int i = 0; i < C::GB; ++i) {
      const int p = (i * NL + lw) * 64 + lane;
      if constexpr (C::BMODE == 0) voffB[i] = p * 16;
      else if constexpr (C::BMODE == 1) voffB[i] = rowmajor(p, Src::ldB);
      else { const int q = p % (C::BK * 8), k = q >> 3, c = (q & 7) ^ ((((k >> 3) & 1) << 2) | (((k >> 1) & 1) << 1)); voffB[i] = k * Src::ldB + c * 16; }
    }
  }
  __device__ __forceinline__ void issueA(int k, unsigned buf) {
    const char* ga = src.baseA(k);
    LAS char* la = lds + buf + lw * 1024;
#pragma unroll
    for (int i = 0; i < C::GA; ++i) glds16<Src::AUXA>(ga + voffA[i], la + i * NL * 1024);
  }
  __device__ __forceinline__ void issueB(int k, unsigned buf) {
    LAS char* lb = lds + C::RA + buf + lw * 1024;
#pragma unroll
    for (int i = 0; i < C::GB; ++i) glds16<Src::AUXB>(src.baseB(k, i >= C::GB / 2) + voffB[i], lb + i * NL * 1024);
  }
  __device__ __forceinline__ void preloadB() {
#pragma unroll 1
    for (int s = 0; s < C::NSB; ++s) issueB(s, s * C::BBYTES);
  }
  static constexpr int cmin(int a, int b) { return a < b ? a : b; }
  static constexpr int cmax(int a, int b) { return a > b ? a : b; }
  static constexpr int T(int i) { return C::NSA * C::GA + cmin(i, C::NK - C::NSA) * C::GA + cmin(i, C::NK - C::NSB) * C::GB; }
  static constexpr int cnt(int k) {
    const int s = k + 1;
    const int pa = s < C::NSA ? (s + 1) * C::GA : T(s - C::NSA) + C::GA;
    const int pb = s < C::NSB ? 0 : T(s - C::NSB + 1);
    return T(k) - cmax(pa, pb);
  }
  static constexpr int key(int k) { return cnt(k) * 4 + (k + C::NSA < C::NK ? 2 : 0) + (k + C::NSB < C::NK ? 1 : 0); }
  static constexpr int seg_end(int k) { int e = k + 1; while (e < C::NK - 1 && key(e) == key(k)) ++e; return e; }
  unsigned ca, cb;
  template <int K> __device__ __forceinline__ void iter(int k) {
    static_assert(cnt(K) <= 63, "vmcnt");
    wait_vm<cnt(K)>();
    bar();
    if constexpr (K + C::NSA < C::NK) { issueA(k + C::NSA, ca); ca += C::ABYTES; if (ca == C::RA) ca = 0; }
    if constexpr (K + C::NSB < C::NK) { issueB(k + C::NSB, cb); cb += C::BBYTES; if (cb == C::RB) cb = 0; }
  }
  template <int K, class Hook> __device__ __forceinline__ void seg(Hook hook2) {
    if constexpr (K < C::NK - 1) {
      if constexpr (K == C::NK - C::NSB) hook2();
      constexpr int E = seg_end(K);
      if constexpr (E - K == 1) iter<K>(K);
      else {
#pragma unroll 1
        for (int k = K; k < E; ++k) iter<K>(k);
      }
      seg<E>(hook2);
    }
  }
  template <class Hook, class Hook2> __device__ __forceinline__ void run(Hook hook, Hook2 hook2) {
#pragma unroll 1
    for (int s = 0; s < C::NSA; ++s) issueA(s, s * C::ABYTES);
    wait_vm<(C::NSA - 1) * C::GA>();
    bar();
    hook();
    ca = 0; cb = 0;
    seg<0>(hook2);
  }
};

template <class C>
struct Consumer {
  LAS char* lds;
  int wid, lane, wm, wn, wk;
  unsigned rdA[2], rdB[4];
  f32x4 accC = {0.f, 0.f, 0.f, 0.f};

  __device__ __forceinline__ Consumer(LAS char* lds_, int wid_, int lane_) : lds(lds_), wid(wid_), lane(lane_) {
    wk = wid % C::WK; wn = (wid / C::WK) % C::WN; wm = wid / (C::WK * C::WN);
    const int fr = lane & 15, fq = lane >> 4;
    const int arow = wm * (C::BM / C::WM) + fr;
    const int ss0 = wk * C::KSW;
#pragma unroll
    for (int s = 0; s < 2; ++s) {
      const int ss = ss0 + s;
      if constexpr (C::AMODE == 0) rdA[s] = ((ss0 * 4 + fq) * C::BM + arow) * 16;
      else rdA[s] = arow * 128 + ((((ss & 1) * 4 + fq) ^ ((fr >> 1) & 7)) * 16);
    }
    if constexpr (C::BMODE <= 1) {
      const int brow = wn * (C::BN / C::WN) + fr;
#pragma unroll
      for (int s = 0; s < 2; ++s) {
        const int ss = ss0 + s;
        if constexpr (C::BMODE == 0) rdB[s] = C::RA + ((ss0 * 4 + fq) * C::BN + brow) * 16;
        else rdB[s] = C::RA + brow * 128 + ((((ss & 1) * 4 + fq) ^ ((fr >> 1) & 7)) * 16);
      }
    } else {
      const int g = lane >> 4, q = (lane >> 2) & 3, p = lane & 3;
#pragma unroll
      for (int j = 0; j < 4; ++j) {
        const int n = (j + wm) & 3;
        const int jj = (C::BMODE == 4) ? j : 2 * wn + (n & 1), mat = (C::BMODE == 4) ? 0 : (n >> 1);
        rdB[j] = C::RA + mat * (C::BK * 128) + (32 * ss0 + 8 * g + q) * 128 + ((((jj ^ (((g & 1) << 1) | (q >> 1))) << 1) | (p >> 1)) * 16) + 8 * (p & 1);
      }
    }
  }

  __device__ __forceinline__ void read(Frags<C>& f, unsigned bufa, unsigned bufb) {
    LAS char* a = lds + bufa; LAS char* b = lds + bufb;
#pragma unroll
    for (int s = 0; s < C::KSW; ++s) {
      if constexpr (C::BMODE == 0) {
#pragma unroll
        for (int n = 0; n < C::NF; ++n) f.b[s][n] = *(const LAS bf16x8*)(b + rdB[0] + s * (C::BN * 64) + n * 256);
      } else if constexpr (C::BMODE == 1) {
#pragma unroll
        for (int n = 0; n < C::NF; ++n) f.b[s][n] = *(const LAS bf16x8*)(b + rdB[s] + n * 2048);
      } else {
#pragma unroll
        for (int n = 0; n < C::NF; ++n) {
          bf16x4 lo = __builtin_amdgcn_ds_read_tr16_b64_v4i16((LAS bf16x4*)(b + rdB[n] + s * 4096));
          bf16x4 hi = __builtin_amdgcn_ds_read_tr16_b64_v4i16((LAS bf16x4*)(b + rdB[n] + s * 4096 + 512));
          f.b[s][n] = __builtin_shufflevector(lo, hi, 0, 1, 2, 3, 4, 5, 6, 7);
        }
      }
      if constexpr (C::AMODE == 0) {
#pragma unroll
        for (int m = 0; m < C::MF; ++m) f.a[s][m] = *(const LAS bf16x8*)(a + rdA[0] + s * (C::BM * 64) + m * 256);
      } else {
#pragma unroll
        for (int m = 0; m < C::MF; ++m) f.a[s][m] = *(const LAS bf16x8*)(a + rdA[s] + m * 2048);
      }
    }
  }

  __device__ __forceinline__ void mma(f32x4 (&acc)[C::MF][C::NF], const Frags<C>& f) {
#pragma unroll
    for (int s = 0; s < C::KSW; ++s)
#pragma unroll
      for (int m = 0; m < C::MF; ++m)
#pragma unroll
        for (int n = 0; n < C::NF; ++n)
          acc[m][n] = C::SWAP ? __builtin_amdgcn_mfma_f32_16x16x32_bf16(f.b[s][n], f.a[s][m], acc[m][n], 0, 0, 0)
                              : __builtin_amdgcn_mfma_f32_16x16x32_bf16(f.a[s][m], f.b[s][n], acc[m][n], 0, 0, 0);
    if constexpr (C::BMODE == 3) {
      const bf16x8 ones = {0x3F80, 0x3F80, 0x3F80, 0x3F80, 0x3F80, 0x3F80, 0x3F80, 0x3F80};
      accC = __builtin_amdgcn_mfma_f32_16x16x32_bf16(f.b[0][0], ones, accC, 0, 0, 0);
    }
  }
  static constexpr int NRD = C::KSW * (C::MF + (C::BMODE >= 2 ? 2 : 1) * C::NF);
  static constexpr int NMM = C::KSW * C::MF * C::NF + (C::BMODE == 3 ? 1 : 0);
  __device__ __forceinline__ void interleave() {
    constexpr int RPM = (NRD + NMM - 3) / (NMM - 2);
#pragma unroll
    for (int i = 0; i < NMM; ++i) {
      __builtin_amdgcn_sched_group_barrier(0x100, RPM, 0);
      __builtin_amdgcn_sched_group_barrier(0x008, 1, 0);
    }
  }
  unsigned na, nb;
  __device__ __forceinline__ void step(f32x4 (&acc)[C::MF][C::NF], Frags<C>& cur, Frags<C>& nxt) {
    na += C::ABYTES; if (na == C::RA) na = 0;
    nb += C::BBYTES; if (nb == C::RB) nb = 0;
    wait_lgkm0(); bar();
    __builtin_amdgcn_sched_barrier(0);
    read(nxt, na, nb);
    mma(acc, cur);
    interleave();
    __builtin_amdgcn_sched_barrier(0);
  }

  template <class Hook, class Hook2> __device__ __forceinline__ void run(f32x4 (&acc)[C::MF][C::NF], Hook hook, Hook2 hook2) {
    Frags<C> f0, f1;
#pragma unroll
    for (int m = 0; m < C::MF; ++m)
#pragma unroll
      for (int n = 0; n < C::NF; ++n) acc[m][n] = (f32x4){0.f, 0.f, 0.f, 0.f};
    bar();
    if (wid == 0) { STAMP(C::KID, 1); CSTAMP(C::KID, 8); }
    hook();
    read(f0, 0, 0);
    na = 0; nb = 0;
    if (wid == 0) STAMP(C::KID, 4);
#pragma unroll 1
    for (int k = 0; k + 2 < C::NK; k += 2) {
      step(acc, f0, f1);
      step(acc, f1, f0);
    }
    if (wid == 0) STAMP(C::KID, 5);
    hook2();
    if (wid == 0) STAMP(C::KID, 6);
    if constexpr (C::NK % 2 == 0) {
      step(acc, f0, f1);
      wait_lgkm0(); bar();
      mma(acc, f1);
    } else {
      wait_lgkm0(); bar();
      mma(acc, f0);
    }
    if (wid == 0) { STAMP(C::KID, 2); CSTAMP(C::KID, 9); }
  }

  static constexpr bool SPLM = (C::MF % 2 == 0);
  static constexpr int RM = SPLM ? C::MF / 2 : C::MF, RN = SPLM ? C::NF : C::NF / 2;
  __device__ __forceinline__ void reduce_k(f32x4 (&acc)[C::MF][C::NF], f32x4 (&red)[RM][RN]) {
    static_assert(C::WK == 2, "");
    static_assert(LDS_X + C::NC * RM * RN * 1024 <= LDS_MISC || RM * RN == 6, "");
    LAS f32x4* x = (LAS f32x4*)(lds + LDS_X - (RM * RN == 6 ? 1024 : 0));
#pragma unroll
    for (int m = 0; m < RM; ++m)
#pragma unroll
      for (int n = 0; n < RN; ++n) {
        const f32x4 lo = acc[m][n], hi = SPLM ? acc[m + RM][n] : acc[m][n + RN];
        x[(wid * RM * RN + m * RN + n) * 64 + lane] = wk ? lo : hi;
        red[m][n] = wk ? hi : lo;
      }
    wait_lgkm0(); bar();
#pragma unroll
    for (int m = 0; m < RM; ++m)
#pragma unroll
      for (int n = 0; n < RN; ++n) red[m][n] += x[((wid ^ 1) * RM * RN + m * RN + n) * 64 + lane];
  }
};

constexpr int FLE = 0;
constexpr int FL0 = 32;
constexpr int FL1 = FL0 + 2048;
constexpr int FL2 = FL1 + 16 * 16;
constexpr int FL3 = FL2 + 8 * 32;

__device__ __forceinline__ void publish(LAS unsigned* arr, unsigned nwaves, u64* flag, u64 tag, int lane, int kid) {
  if (threadIdx.x < 64) STAMP(kid, 7);
  wait_vm<0>();
  if (lane == 0) {
    const unsigned old = __hip_atomic_fetch_add(arr, 1u, RLX_WG);
    if (old == nwaves - 1) __hip_atomic_store(flag, tag, RLX_AGENT);
  }
}
__device__ __forceinline__ void lds_wait(LAS unsigned* w, unsigned want) {
  const unsigned a = (unsigned)(size_t)w;
#pragma unroll 1
  for (unsigned i = 0; i < (1u << 24); ++i) {
    unsigned v;
    asm volatile("ds_read_b32 %0, %1\n\ts_waitcnt lgkmcnt(0)" : "=v"(v) : "v"(a) : "memory");
    if (__builtin_amdgcn_readfirstlane(v) >= want) break;
    __builtin_amdgcn_s_sleep(1);
  }
}
template <int SLEEP, int NW>
__device__ __forceinline__ void await(const u64* word, bool need, u64 tag, int lw, LAS unsigned* go, unsigned epoch) {
  if (lw == 0) {
    const u64 t0 = wall_clock64();
    for (;;) {
      bool ok = true;
#pragma unroll
      for (int i = 0; i < NW; ++i) ok &= __hip_atomic_load(word + i * 64, RLX_AGENT) == tag;
      if (__all(!need || ok)) break;
      if (wall_clock64() - t0 > 2000000ull) break;
      __builtin_amdgcn_s_sleep(SLEEP);
    }
    __hip_atomic_store(go, epoch, RLX_WG);
  } else {
    lds_wait(go, epoch);
  }
  __builtin_amdgcn_fence(__ATOMIC_ACQUIRE, "wavefront");
}
#define P1_PRE 1
#define PRE_LATE 0
#define PSLEEP 4

using C1 = Cfg<1, 80, 128, 64, 1, 4, 1, 4, 6, 32, 0, 1, 0>;
#define P2A 4
#define P2B 10
using C2 = Cfg<2, 64, 80, 64, 2, 1, 2, P2A, P2B, 20, 0, 1>;
#define P3A 4
#define P3B 8
using C3 = Cfg<3, 256, 128, 32, 4, 2, 1, P3A, P3B, 20, 0, 3>;
#define P4A 4
#define P4B 10
using C4 = Cfg<4, 96, 64, 64, 2, 1, 2, P4A, P4B, 27, 0, 4>;
#define HPRIO 2
#define WARM 3
#define WARM_BACK 0
#define ROT 15
struct Src1 {
  const char* a; const char* b; int o, c;
  static constexpr int ldA = 0, ldB = S_ * 2, AUXA = AUXH, AUXB = 0, rows = 80;
  __device__ __forceinline__ int kt(int k) const { const int s = (k + o) & 31; return (ROT & 1) ? ((s & 24) | ((s + c) & 7)) : k; }
  __device__ __forceinline__ const char* baseA(int k) const { return a + kt(k) * (80 * 128); }
  __device__ __forceinline__ const char* baseB(int k, int) const { return b + kt(k) * 128; }
};
struct Src2 {
  const char* a; const char* bre; const char* bim; int rot;
  static constexpr int ldA = 0, ldB = D_ * 2, AUXA = AUXH, AUXB = 0, rows = 64;
  __device__ __forceinline__ int kt(int k) const { const int s = k + ((ROT & 2) ? rot : 0); return s >= 20 ? s - 20 : s; }
  __device__ __forceinline__ const char* baseA(int k) const { return a + kt(k) * (64 * 128); }
  __device__ __forceinline__ const char* baseB(int k, int) const { const int t = kt(k); return t < 10 ? bre + t * 128 : bim + (t - 10) * 128; }
};
struct Src3 {
  const char* a; const char* bg; const char* bu; int rot;
  static constexpr int ldA = 0, ldB = F_ * 2, AUXA = AUXH, AUXB = 0, rows = 256;
  __device__ __forceinline__ int kt(int k) const { const int s = k + ((ROT & 4) ? rot : 0); return s >= 20 ? s - 20 : s; }
  __device__ __forceinline__ const char* baseA(int k) const { return a + kt(k) * (256 * 64); }
  __device__ __forceinline__ const char* baseB(int k, int up) const { return (up ? bu : bg) + kt(k) * (32 * F_ * 2); }
};
struct Src4 {
  const char* a; const char* b; int rows, rot;
  static constexpr int ldA = 0, ldB = D_ * 2, AUXA = AUXH, AUXB = 0;
  __device__ __forceinline__ int kt(int k) const { const int s = k + ((ROT & 8) ? rot : 0); return s >= 27 ? s - 27 : s; }
  __device__ __forceinline__ const char* baseA(int k) const { return a + kt(k) * (96 * 128); }
  __device__ __forceinline__ const char* baseB(int k, int) const { return b + kt(k) * (64 * D_ * 2); }
};

struct Params {
  const u16* x; const u16* sre; const u16* sim; const u16* hre; const u16* him; const u16* wg; const u16* wu; const u16* wd;
  u16* xnI; u16* pqI; u16* hI; u16* hhI; f32x2* stats; u64* flags; u16* out;
};

template <int CTRL, int RMASK> __device__ __forceinline__ float dpp_add(float v) {
  return v + __int_as_float(__builtin_amdgcn_update_dpp(0, __float_as_int(v), CTRL, RMASK, 0xf, true));
}
__device__ __forceinline__ float wave_sum(float v) {
  v = dpp_add<0x111, 0xf>(v); v = dpp_add<0x112, 0xf>(v); v = dpp_add<0x114, 0xf>(v); v = dpp_add<0x118, 0xf>(v);
  v = dpp_add<0x142, 0xa>(v); v = dpp_add<0x143, 0xc>(v);
  return __int_as_float(__builtin_amdgcn_readlane(__float_as_int(v), 63));
}
template <int N> __device__ __forceinline__ void bars() {
#pragma unroll 1
  for (int i = 0; i < N; ++i) bar();
}

__global__ __launch_bounds__(NWAVES * 64) void mega(const Params p) {
  __shared__ __attribute__((aligned(1024))) char smem_[LDS_TOTAL];
  LAS char* lds = (LAS char*)smem_;
  u64 pc0; asm volatile("s_getpc_b64 %0" : "=s"(pc0));
  const int wid = __builtin_amdgcn_readfirstlane(threadIdx.x >> 6), lane = threadIdx.x & 63;
  const int bid = blockIdx.x, g = bid & 7, idx = bid >> 3;
  LAS unsigned* arr = (LAS unsigned*)(lds + LDS_MISC + 256);
  if (wid == 0) STAMP(0, 0);
  const u16* xr = p.x + (bid * 8 + (wid & 7)) * D_;
  const bf16x8 v0 = *(const bf16x8*)(xr + lane * 8);
  const bf16x8 v1 = *(const bf16x8*)(xr + 512 + (lane & 15) * 8);
  const u64 tag = __hip_atomic_load(p.flags + FLE, RLX_AGENT) + 1ull;
  if (threadIdx.x < 16) arr[threadIdx.x] = 0u;
  wait_lgkm0(); bar();
  const int j1 = idx & 7, mq = idx >> 3, isQ = mq >> 1, t1 = g * 256 + (mq & 1) * 128;
  const int j2 = idx & 7, mt2 = g * 4 + (idx >> 3);
  const int mt3 = (g & 6) + (idx & 1), j3 = (g & 1) * 14 + (idx >> 1);
  const bool on3 = idx < 28 && j3 < 27;
  const int lin4 = g * 28 + idx, mt4 = lin4 / 10, j4 = lin4 % 10;
  const bool on4 = idx < 28 && lin4 < 220;
  LAS f32x4* rs = (LAS f32x4*)(lds + LDS_X);
  LAS float* csl = (LAS float*)(lds + LDS_X + 4096);

  if (wid >= 8) {
    const int lw = wid - 8;
    {
      asm volatile("" :: "v"(tag) : "memory");
      if (lane == 0) __hip_atomic_fetch_add(arr + 5, 1u, RLX_WG);
      Src1 s{(const char*)(p.xnI + (size_t)j1 * 256 * 80 * 8), (const char*)((isQ ? p.sim : p.sre) + (size_t)t1 * S_), (g + 2 * mq) & 7, j1};
      Loader<C1, Src1> L(lds, s, lw, lane);
      if (lw == 1) {
        const char* a = (const char*)(pc0 & ~127ull) + (idx * 8 + lane) * 128;
        const int q = idx * 24 + lane - 8;
        if (lane >= 8) {
          a = (const char*)p.flags + q * 65536;
          if (q >= 336) a = (const char*)p.out + (q - 336) * 65536;
          if (q >= 376) a = (const char*)p.hre + (q - 376) * 65536;
          if (q >= 389) a = (const char*)p.him + (q - 389) * 65536;
          if (q >= 402) a = (const char*)p.wg + (q - 402) * 65536;
          if (q >= 436) a = (const char*)p.wu + (q - 436) * 65536;
          if (q >= 470) a = (const char*)p.wd + (q - 470) * 65536;
        }
        if (((WARM & 1) && lane < 8 && idx < 28) || ((WARM & 2) && lane >= 8 && lane < 32 && q < 504)) touch<0>(a, lds + LDS_MISC);
      }
      lds_wait(arr + 4, 8);
      L.preloadB();
      lds_wait(arr + 5, 10);
      await<PSLEEP, 4>(p.flags + FL0 + g * 256 + lane, true, tag, lw, arr + 3, 1u);
      if (bid == 0 && lw == 0 && lane == 0) __hip_atomic_store(p.flags + FLE, tag, RLX_AGENT);
      if (lw == 0) STAMP(1, 0);
      wait_vm<0>();
      L.run([] {}, [] {});
      bar();
    }
    {
      Src2 s{(const char*)(p.pqI + (size_t)mt2 * 160 * 64 * 8), (const char*)(p.hre + (size_t)j2 * 80 * D_), (const char*)(p.him + (size_t)j2 * 80 * D_), (mt2 * 5) >> 3};
      Loader<C2, Src2> L(lds, s, lw, lane);
      if (PRE_LATE) lds_wait(arr + 0, C1::NC);
      L.preloadB();
      lds_wait(arr + 0, C1::NC);
      await<PSLEEP, 1>(p.flags + FL1 + (mt2 >> 1) * 16 + (lane & 15), true, tag, lw, arr + 3, 2u);
      if (lw == 0) STAMP(2, 0);
      wait_vm<0>();
      L.run([] {}, [] {});
      bar();
    }
    if (on3) {
      Src3 s{(const char*)(p.hI + (size_t)mt3 * 80 * 256 * 8), (const char*)(p.wg + j3 * 64), (const char*)(p.wu + j3 * 64), (mt3 * 5) >> 1};
      Loader<C3, Src3> L(lds, s, lw, lane);
      bar();
      if (PRE_LATE) lds_wait(arr + 1, C2::NC);
      L.preloadB();
      lds_wait(arr + 1, C2::NC);
      await<PSLEEP, 1>(p.flags + FL2 + mt3 * 32 + (lane & 31), true, tag, lw, arr + 3, 3u);
      if (lw == 0) STAMP(3, 0);
      wait_vm<0>();
      f32x2 pv[2][8];
      L.run([&] {
#pragma unroll
        for (int h = 0; h < 2; ++h)
#pragma unroll
          for (int i = 0; i < 8; ++i) pv[h][i] = __builtin_bit_cast(f32x2, ld8(p.stats + i * S_ + mt3 * 256 + lw * 128 + h * 64 + lane));
      }, [&] {
#pragma unroll
        for (int h = 0; h < 2; ++h) {
          float mean = 0.f, q = 0.f, dd = 0.f;
#pragma unroll
          for (int i = 0; i < 8; ++i) { mean += pv[h][i][0]; q += pv[h][i][1]; }
          mean *= 0.125f;
#pragma unroll
          for (int i = 0; i < 8; ++i) { const float d = pv[h][i][0] - mean; dd += d * d; }
          q += 80.0f * dd;
          const float den = rbf(__builtin_amdgcn_sqrtf(rbf(rbf(q * (1.0f / D_)) + 1e-5f)));
          rs[lw * 128 + h * 64 + lane] = (f32x4){-rbf(mean), den, -1.44269504089f * __builtin_amdgcn_rcpf(den), 0.f};
        }
        wait_lgkm0();
      });
      bar();
    } else {
      bar();
    }
    if (on4) {
      Src4 s{(const char*)(p.hhI + (size_t)mt4 * 216 * 96 * 8), (const char*)(p.wd + j4 * 64), min(96, S_ - mt4 * 96), (mt4 * 27) / 22};
      Loader<C4, Src4> L(lds, s, lw, lane);
      if (PRE_LATE && on3) lds_wait(arr + 2, C3::NC);
      L.preloadB();
      const int ra = (mt4 * 96) >> 8, rb = min(mt4 * 96 + 95, S_ - 1) >> 8;
      if (on3) {
        lds_wait(arr + 2, C3::NC);
        await<PSLEEP, 1>(p.flags + FL3 + (lane < 32 ? ra : rb) * 32 + (lane & 31), (lane & 31) < 27, tag, lw, arr + 3, 4u);
      } else {
        await<32, 1>(p.flags + FL3 + (lane < 32 ? ra : rb) * 32 + (lane & 31), (lane & 31) < 27, tag, lw, arr + 3, 4u);
      }
      if (lw == 0) STAMP(4, 0);
      wait_vm<0>();
      L.run([] {}, [] {});
      bar();
      bar();
    }
    return;
  }

  const int fr = lane & 15, fq = lane >> 4;
  {
    float f[16];
#pragma unroll
    for (int i = 0; i < 8; ++i) { f[i] = bf2f((u16)v0[i]); f[8 + i] = bf2f((u16)v1[i]); }
    const float piv = __int_as_float(__builtin_amdgcn_readfirstlane(__float_as_int(f[0])));
    asm volatile("" :: "v"(f[8]) : "memory");
    if (lane == 0) __hip_atomic_fetch_add(arr + 4, 1u, RLX_WG);
    if (wid == 0) STAMP(0, 4);
    float s0 = 0.f, s1 = 0.f, q0 = 0.f, q1 = 0.f;
#pragma unroll
    for (int i = 0; i < 8; ++i) { const float d = f[i] - piv, e = f[8 + i] - piv; s0 += d; q0 += d * d; s1 += e; q1 += e * e; }
    const float sd = wave_sum(s0 + (lane < 16 ? s1 : 0.f)) * (1.0f / D_);
    const float qd = wave_sum(q0 + (lane < 16 ? q1 : 0.f)) * (1.0f / D_);
    const float mb = rbf(piv + sd), vb = rbf(fmaxf(qd - sd * sd, 0.f));
    const float rden = __builtin_amdgcn_rcpf(rbf(__builtin_amdgcn_sqrtf(rbf(vb + 1e-5f))));
    u32x4 w0, w1;
#pragma unroll
    for (int i = 0; i < 4; ++i) {
      w0[i] = pk2(rbf(f[2 * i] - mb) * rden, rbf(f[2 * i + 1] - mb) * rden);
      w1[i] = pk2(rbf(f[8 + 2 * i] - mb) * rden, rbf(f[9 + 2 * i] - mb) * rden);
    }
    LAS char* tb = lds + LDS_X;
    *(LAS u32x4*)(tb + wid * 1296 + lane * 16) = w0;
    if (lane < 16) *(LAS u32x4*)(tb + wid * 1296 + 1024 + lane * 16) = w1;
    wait_lgkm0();
    if (lane == 0) __hip_atomic_fetch_add(arr + 7, 1u, RLX_WG);
    lds_wait(arr + 7, 8);
    auto rsrc = __builtin_amdgcn_make_buffer_rsrc(p.xnI, 0, S_ * D_ * 2, 0x00020000);
#pragma unroll
    for (int h = 0; h < 2; ++h) {
      const int d = h * 512 + wid * 64 + lane;
      if (d < D_) {
        bf16x8 o;
#pragma unroll
        for (int r = 0; r < 8; ++r) o[r] = *(const LAS short*)(tb + r * 1296 + d * 2);
        const int j = d / 80, n = d % 80;
        __builtin_amdgcn_raw_buffer_store_b128(__builtin_bit_cast(u32x4, o), rsrc, ((j * 256 + bid) * 80 + n) * 16, 0, 16);
      }
    }
    if (wid == 0) STAMP(0, 7);
    wait_vm<0>();
    lds_wait(arr + 5, 2);
    unsigned old = 0;
    if (lane == 0) old = __hip_atomic_fetch_add(arr + 5, 1u, RLX_WG);
    if (__builtin_amdgcn_readfirstlane(old) == 9u && lane < 8) __hip_atomic_store(p.flags + FL0 + lane * 256 + bid, tag, RLX_AGENT);
    if (wid == 0) STAMP(0, 3);
  }
  if (wid < C1::NC) {
    Consumer<C1> L(lds, wid, lane);
    f32x4 acc[C1::MF][C1::NF];
    L.run(acc, [] {}, [] {});
    const float sg = isQ ? -1.f : 1.f;
#pragma unroll
    for (int n = 0; n < 2; ++n) {
      const int t = t1 + L.wn * 32 + n * 16 + fr, mt = t >> 6, r = t & 63;
#pragma unroll
      for (int m = 0; m < 5; ++m) {
        const int koct = isQ * 80 + j1 * 10 + m * 2 + (fq >> 1);
        u32x2 w; w[0] = pk2(sg * acc[m][n][0], sg * acc[m][n][1]); w[1] = pk2(sg * acc[m][n][2], sg * acc[m][n][3]);
        st8(p.pqI + (((size_t)mt * 160 + koct) * 64 + r) * 8 + (fq & 1) * 4, w);
      }
    }
    publish(arr + 0, C1::NC, p.flags + FL1 + (t1 >> 7) * 16 + isQ * 8 + j1, tag, lane, 1);
    if (wid == 0) STAMP(1, 3);
  } else {
    bars<C1::NBAR>();
  }
  if (wid < C2::NC) {
    Consumer<C2> L(lds, wid, lane);
    f32x4 acc[C2::MF][C2::NF];
    const int t = mt2 * 64 + L.wm * 32 + L.wk * 16 + fr;
    bf16x4 xv[C2::NF];
#pragma unroll
    for (int n = 0; n < C2::NF; ++n) xv[n] = *(const bf16x4*)(p.x + (size_t)t * D_ + j2 * 80 + n * 16 + fq * 4);
#pragma unroll
    for (int n = 0; n < C2::NF; ++n) asm volatile("" : "+v"(xv[n]));
    L.run(acc, [] {}, [] {});
    f32x4 red[1][5];
    L.reduce_k(acc, red);
    float hv[C2::NF][4];
    float s = 0.f;
#pragma unroll
    for (int n = 0; n < C2::NF; ++n) {
      u32x2 w;
      w[0] = pk2(red[0][n][0] + bf2f((u16)xv[n][0]), red[0][n][1] + bf2f((u16)xv[n][1]));
      w[1] = pk2(red[0][n][2] + bf2f((u16)xv[n][2]), red[0][n][3] + bf2f((u16)xv[n][3]));
      hv[n][0] = __uint_as_float(w[0] << 16); hv[n][1] = __uint_as_float(w[0] & 0xffff0000u);
      hv[n][2] = __uint_as_float(w[1] << 16); hv[n][3] = __uint_as_float(w[1] & 0xffff0000u);
      s += (hv[n][0] + hv[n][1]) + (hv[n][2] + hv[n][3]);
      const int koct = j2 * 10 + n * 2 + (fq >> 1);
      st8(p.hI + (((size_t)(t >> 8) * 80 + koct) * 256 + (t & 255)) * 8 + (fq & 1) * 4, w);
    }
    s += __shfl_xor(s, 16); s += __shfl_xor(s, 32);
    const float mean = s * (1.0f / 80.0f);
    float q = 0.f;
#pragma unroll
    for (int n = 0; n < C2::NF; ++n)
#pragma unroll
      for (int r = 0; r < 4; ++r) { const float d = hv[n][r] - mean; q += d * d; }
    q += __shfl_xor(q, 16); q += __shfl_xor(q, 32);
    if (fq == 0) st8(p.stats + j2 * S_ + t, __builtin_bit_cast(u32x2, (f32x2){mean, q}));
    publish(arr + 1, C2::NC, p.flags + FL2 + g * 32 + idx, tag, lane, 2);
    if (wid == 0) STAMP(2, 3);
  } else {
    bars<C2::NBAR>();
  }
  if (on3) {
    Consumer<C3> L(lds, wid, lane);
    f32x4 acc[C3::MF][C3::NF];
    L.run(acc, [] {}, [] {});
    if (fr == 0) *(LAS f32x4*)(csl + (L.wm >> 1) * 64 + L.wn * 32 + (L.wm & 1) * 16 + fq * 4) = L.accC;
    wait_lgkm0();
    if (lane == 0) __hip_atomic_fetch_add(arr + 6, 1u, RLX_WG);
    lds_wait(arr + 6, 8);
#pragma unroll
    for (int i = 0; i < 2; ++i) {
      const bool sw = ((i + L.wm) & 2) != 0;
      const int fl = L.wn * 32 + ((i + L.wm) & 1) * 16 + fq * 4, f0 = j3 * 64 + fl;
      const f32x4 cg = *(const LAS f32x4*)(csl + fl), cu = *(const LAS f32x4*)(csl + 64 + fl);
#pragma unroll
      for (int m = 0; m < C3::MF; ++m) {
        const int rl = L.wm * 64 + m * 16 + fr, t = mt3 * 256 + rl;
        const f32x4 st = rs[rl];
        const f32x4 ag = sw ? acc[m][i + 2] : acc[m][i], au = sw ? acc[m][i] : acc[m][i + 2];
        float o[4];
#pragma unroll
        for (int r = 0; r < 4; ++r) {
          const float e = __builtin_amdgcn_exp2f(__builtin_fmaf(cg[r], st[0], ag[r]) * st[2]);
          o[r] = __builtin_fmaf(cu[r], st[0], au[r]) * __builtin_amdgcn_rcpf(__builtin_fmaf(e, st[1], st[1]));
        }
        u32x2 w; w[0] = pk2(o[0], o[1]); w[1] = pk2(o[2], o[3]);
        const int t4 = t / 96;
        st8(p.hhI + (((size_t)t4 * 216 + (f0 >> 3)) * 96 + (t - t4 * 96)) * 8 + (fq & 1) * 4, w);
      }
    }
    publish(arr + 2, C3::NC, p.flags + FL3 + mt3 * 32 + j3, tag, lane, 3);
    if (wid == 0) STAMP(3, 3);
  }
  if (on4) {
    if (wid < C4::NC) {
      Consumer<C4> L(lds, wid, lane);
      f32x4 acc[C4::MF][C4::NF];
      const int tb = mt4 * 96 + L.wm * 48 + fr, c0 = j4 * 64 + L.wk * 32 + fq * 4;
      u32x2 hv[3][2];
      L.run(acc, [&] {
#pragma unroll
        for (int m = 0; m < 3; ++m)
#pragma unroll
          for (int n = 0; n < 2; ++n) {
            const int t = min(tb + m * 16, S_ - 1), c = c0 + n * 16;
            hv[m][n] = ld8(p.hI + (((size_t)(t >> 8) * 80 + (c >> 3)) * 256 + (t & 255)) * 8 + (c & 4));
          }
      }, [] {});
      f32x4 red[3][2];
      L.reduce_k(acc, red);
#pragma unroll
      for (int m = 0; m < 3; ++m)
#pragma unroll
        for (int n = 0; n < 2; ++n) {
          const int t = tb + m * 16;
          u32x2 w;
          w[0] = pk2(red[m][n][0] + __uint_as_float(hv[m][n][0] << 16), red[m][n][1] + __uint_as_float(hv[m][n][0] & 0xffff0000u));
          w[1] = pk2(red[m][n][2] + __uint_as_float(hv[m][n][1] << 16), red[m][n][3] + __uint_as_float(hv[m][n][1] & 0xffff0000u));
          if (t < S_) *(u32x2*)(p.out + (size_t)t * D_ + c0 + n * 16) = w;
        }
      if (wid == 0) { wait_vm<0>(); STAMP(4, 3); }
    }
  }
}

extern "C" void kernel_launch(void* const* d_in, const int* in_sizes, int n_in, void* d_out, int out_size,
                              void* d_ws, size_t ws_size, hipStream_t stream) {
  char* ws = (char*)d_ws;
  Params p;
  p.x = (const u16*)d_in[0]; p.sre = (const u16*)d_in[1]; p.sim = (const u16*)d_in[2];
  p.hre = (const u16*)d_in[3]; p.him = (const u16*)d_in[4];
  p.wg = (const u16*)d_in[6]; p.wu = (const u16*)d_in[7]; p.wd = (const u16*)d_in[8];
  p.flags = (u64*)ws;
  p.stats = (f32x2*)(ws + 131072);
  p.xnI = (u16*)(ws + (1u << 20));
  p.pqI = (u16*)(ws + (4u << 20));
  p.hI = (u16*)(ws + (10u << 20));
  p.hhI = (u16*)(ws + (13u << 20));
  p.out = (u16*)d_out;
  mega<<<256, NWAVES * 64, 0, stream>>>(p);
}
```

```cpp
#include <hip/hip_runtime.h>
#include <hip/hip_bf16.h>
#define AUXH 16

typedef unsigned short u16;
typedef unsigned long long u64;
typedef short bf16x8 __attribute__((ext_vector_type(8)));
typedef short bf16x4 __attribute__((ext_vector_type(4)));
typedef float f32x4 __attribute__((ext_vector_type(4)));
typedef float f32x2 __attribute__((ext_vector_type(2)));
typedef unsigned u32x2 __attribute__((ext_vector_type(2)));
typedef unsigned u32x4 __attribute__((ext_vector_type(4)));
#define LAS __attribute__((address_space(3)))
#define GAS __attribute__((address_space(1)))
#define RLX_AGENT __ATOMIC_RELAXED, __HIP_MEMORY_SCOPE_AGENT
#define RLX_WG __ATOMIC_RELAXED, __HIP_MEMORY_SCOPE_WORKGROUP

constexpr int S_ = 2048, D_ = 640, F_ = 1728;

__device__ __forceinline__ float bf2f(u16 v) { return __uint_as_float(((unsigned)v) << 16); }
typedef __bf16 bf16x2n __attribute__((ext_vector_type(2)));
__device__ __forceinline__ unsigned pk2(float lo, float hi) {
  return __builtin_bit_cast(unsigned, __builtin_convertvector((f32x2){lo, hi}, bf16x2n));
}
__device__ __forceinline__ float rbf(float v) { return __uint_as_float(pk2(v, v) << 16); }

#define CSTAMP(kid, i)
#define STAMP(kid, i)
template <int N> __device__ __forceinline__ void wait_vm() { asm volatile("s_waitcnt vmcnt(%0)" ::"n"(N) : "memory"); }
__device__ __forceinline__ void wait_lgkm0() { asm volatile("" ::: "memory"); __builtin_amdgcn_s_waitcnt(0xC07F); asm volatile("" ::: "memory"); }
__device__ __forceinline__ void bar() { asm volatile("" ::: "memory"); __builtin_amdgcn_s_barrier(); asm volatile("" ::: "memory"); }
template <int N, int STEP> __device__ __forceinline__ void wait_dyn(int n) {
  if constexpr (N <= 0) wait_vm<0>();
  else { if (n >= N) wait_vm<N>(); else wait_dyn<N - STEP, STEP>(n); }
}

template <int AUX> __device__ __forceinline__ void glds16(const char* g, LAS char* l) {
  __builtin_amdgcn_global_load_lds((const GAS void*)g, (LAS void*)l, 16, 0, AUX);
}
template <int AUX> __device__ __forceinline__ void touch(const char* g, LAS char* scratch) {
  __builtin_amdgcn_global_load_lds((const GAS void*)g, (LAS void*)scratch, 4, 0, AUX);
}
__device__ __forceinline__ void st8(void* p, u32x2 v) { __hip_atomic_store((u64*)p, __builtin_bit_cast(u64, v), RLX_AGENT); }
__device__ __forceinline__ u32x2 ld8(const void* p) { return __builtin_bit_cast(u32x2, __hip_atomic_load((u64*)p, RLX_AGENT)); }

constexpr int NL = 2;
constexpr int NWAVES = 10;
constexpr int LDS_RING = 136 * 1024;
constexpr int LDS_X = LDS_RING;
constexpr int LDS_TOTAL = 160 * 1024;
constexpr int LDS_MISC = LDS_TOTAL - 512;

template <int KID_, int BM_, int BN_, int BK_, int WM_, int WN_, int WK_, int NSA_, int NSB_, int NK_, int AMODE_, int BMODE_, int SWAP_, int NPRE_>
struct Cfg {
  static constexpr int KID = KID_, BM = BM_, BN = BN_, BK = BK_, WM = WM_, WN = WN_, WK = WK_, NSA = NSA_, NSB = NSB_, NK = NK_;
  static constexpr int AMODE = AMODE_, BMODE = BMODE_, SWAP = SWAP_, NPRE = NPRE_;
  static constexpr int NC = WM * WN * WK;
  static constexpr int MF = BM / WM / 16, NF = BN / WN / 16, KSW = BK / 32 / WK;
  static constexpr int ABYTES = BM * BK * 2, BBYTES = BN * BK * 2;
  static constexpr int PA = ABYTES / 1024, PB = BBYTES / 1024;
  static constexpr int GA = PA / NL, GB = PB / NL, G = GA + GB;
  static constexpr int RA = NSA * ABYTES, RB = NSB * BBYTES;
  static constexpr int NBAR = NK + (WK == 2 ? 2 : 1);
  static_assert(PA % NL == 0 && PB % NL == 0, "pieces");
  static_assert(RA + RB <= LDS_RING, "lds");
  static_assert(NSA <= NK && NSB <= NK && NSB >= NSA, "stages");
  static_assert(KSW >= 1 && KSW <= 2, "ksw");
};

template <class C> struct Frags { bf16x8 a[C::KSW][C::MF]; bf16x8 b[C::KSW][C::NF]; };

template <class C, class Src>
struct Loader {
  LAS char* lds; const Src& src;
  int lw, lane;
  unsigned voffA[C::GA], voffB[C::GB];
  static __device__ __forceinline__ unsigned rowmajor(int p, int ld) {
    const int row = p >> 3, c = (p & 7) ^ ((row >> 1) & 7);
    return row * ld + c * 16;
  }
  __device__ __forceinline__ Loader(LAS char* lds_, const Src& s, int lw_, int lane_) : lds(lds_), src(s), lw(lw_), lane(lane_) {
#pragma unroll
    for (int i = 0; i < C::GA; ++i) {
      const int p = (i * NL + lw) * 64 + lane;
      if constexpr (C::AMODE == 0) { const int r = p % C::BM; voffA[i] = (p - r + (r < src.rows ? r : (r & 31))) * 16; }
      else voffA[i] = rowmajor(p, Src::ldA);
    }
#pragma unroll
    for (int i = 0; i < C::GB; ++i) {
      const int p = (i * NL + lw) * 64 + lane;
      if constexpr (C::BMODE == 0) voffB[i] = p * 16;
      else if constexpr (C::BMODE == 1) voffB[i] = rowmajor(p, Src::ldB);
      else { const int q = p % (C::BK * 8), k = q >> 3, c = (q & 7) ^ ((((k >> 3) & 1) << 2) | (((k >> 1) & 1) << 1)); voffB[i] = k * Src::ldB + c * 16; }
    }
  }
  __device__ __forceinline__ void issueA(int k, unsigned buf) {
    const char* ga = src.baseA(k);
    LAS char* la = lds + buf + lw * 1024;
#pragma unroll
    for (int i = 0; i < C::GA; ++i) glds16<Src::AUXA>(ga + voffA[i], la + i * NL * 1024);
  }
  __device__ __forceinline__ void issueB(int k, unsigned buf) {
    LAS char* lb = lds + C::RA + buf + lw * 1024;
#pragma unroll
    for (int i = 0; i < C::GB; ++i) glds16<Src::AUXB>(src.baseB(k, i >= C::GB / 2) + voffB[i], lb + i * NL * 1024);
    src.extra(k, lw, lane, lds + LDS_MISC);
  }
  __device__ __forceinline__ void preloadB() {
#pragma unroll 1
    for (int s = 0; s < C::NPRE; ++s) issueB(s, s * C::BBYTES);
  }
  static constexpr int cmin(int a, int b) { return a < b ? a : b; }
  static constexpr int cmax(int a, int b) { return a > b ? a : b; }
  static constexpr int PP(int s) { return cmin(s, C::NSA) * C::GA + cmax(cmin(s, C::NSB) - C::NPRE, 0) * C::GB; }
  static constexpr int T(int i) { return PP(C::NSB) + cmin(i, C::NK - C::NSA) * C::GA + cmin(i, C::NK - C::NSB) * C::GB; }
  static constexpr int need(int s) {
    const int pa = s < C::NSA ? PP(s) + C::GA : T(s - C::NSA) + C::GA;
    const int pb = s < C::NPRE ? 0 : (s < C::NSB ? PP(s + 1) : T(s - C::NSB + 1));
    return cmax(pa, pb);
  }
  static constexpr int cnt(int k) { return cmin(T(k) - need(k + 1), 63); }
  static constexpr int key(int k) { return cnt(k) * 4 + (k + C::NSA < C::NK ? 2 : 0) + (k + C::NSB < C::NK ? 1 : 0); }
  static constexpr int seg_end(int k) { int e = k + 1; while (e < C::NK - 1 && key(e) == key(k)) ++e; return e; }
  unsigned ca, cb;
  template <int K> __device__ __forceinline__ void iter(int k) {
    static_assert(cnt(K) <= 63, "vmcnt");
    wait_vm<cnt(K)>();
    bar();
    if constexpr (K + C::NSA < C::NK) { issueA(k + C::NSA, ca); ca += C::ABYTES; if (ca == C::RA) ca = 0; }
    if constexpr (K + C::NSB < C::NK) { issueB(k + C::NSB, cb); cb += C::BBYTES; if (cb == C::RB) cb = 0; }
  }
  template <int K, class Hook> __device__ __forceinline__ void seg(Hook hook2) {
    if constexpr (K < C::NK - 1) {
      if constexpr (K == C::NK - C::NSB) hook2();
      constexpr int E = seg_end(K);
      if constexpr (E - K == 1) iter<K>(K);
      else {
#pragma unroll 1
        for (int k = K; k < E; ++k) iter<K>(k);
      }
      seg<E>(hook2);
    }
  }
  template <class Hook, class Hook2> __device__ __forceinline__ void run(Hook hook, Hook2 hook2) {
#pragma unroll 1
    for (int s = 0; s < C::NSB; ++s) {
      if (s < C::NSA) issueA(s, s * C::ABYTES);
      if (s >= C::NPRE) issueB(s, s * C::BBYTES);
    }
    wait_vm<cmin(T(0) - need(0), 63)>();
    bar();
    hook();
    ca = 0; cb = 0;
    seg<0>(hook2);
  }
};

template <class C>
struct Consumer {
  LAS char* lds;
  int wid, lane, wm, wn, wk;
  unsigned rdA[2], rdB[4];
  f32x4 accC = {0.f, 0.f, 0.f, 0.f};

  __device__ __forceinline__ Consumer(LAS char* lds_, int wid_, int lane_) : lds(lds_), wid(wid_), lane(lane_) {
    wk = wid % C::WK; wn = (wid / C::WK) % C::WN; wm = wid / (C::WK * C::WN);
    const int fr = lane & 15, fq = lane >> 4;
    const int arow = wm * (C::BM / C::WM) + fr;
    const int ss0 = wk * C::KSW;
#pragma unroll
    for (int s = 0; s < 2; ++s) {
      const int ss = ss0 + s;
      if constexpr (C::AMODE == 0) rdA[s] = ((ss0 * 4 + fq) * C::BM + arow) * 16;
      else rdA[s] = arow * 128 + ((((ss & 1) * 4 + fq) ^ ((fr >> 1) & 7)) * 16);
    }
    if constexpr (C::BMODE <= 1) {
      const int brow = wn * (C::BN / C::WN) + fr;
#pragma unroll
      for (int s = 0; s < 2; ++s) {
        const int ss = ss0 + s;
        if constexpr (C::BMODE == 0) rdB[s] = C::RA + ((ss0 * 4 + fq) * C::BN + brow) * 16;
        else rdB[s] = C::RA + brow * 128 + ((((ss & 1) * 4 + fq) ^ ((fr >> 1) & 7)) * 16);
      }
    } else {
      const int g = lane >> 4, q = (lane >> 2) & 3, p = lane & 3;
#pragma unroll
      for (int j = 0; j < 4; ++j) {
        const int n = (j + wm) & 3;
        const int jj = (C::BMODE == 4) ? j : 2 * wn + (n & 1), mat = (C::BMODE == 4) ? 0 : (n >> 1);
        rdB[j] = C::RA + mat * (C::BK * 128) + (32 * ss0 + 8 * g + q) * 128 + ((((jj ^ (((g & 1) << 1) | (q >> 1))) << 1) | (p >> 1)) * 16) + 8 * (p & 1);
      }
    }
  }

  __device__ __forceinline__ void read(Frags<C>& f, unsigned bufa, unsigned bufb) {
    LAS char* a = lds + bufa; LAS char* b = lds + bufb;
#pragma unroll
    for (int s = 0; s < C::KSW; ++s) {
      if constexpr (C::BMODE == 0) {
#pragma unroll
        for (int n = 0; n < C::NF; ++n) f.b[s][n] = *(const LAS bf16x8*)(b + rdB[0] + s * (C::BN * 64) + n * 256);
      } else if constexpr (C::BMODE == 1) {
#pragma unroll
        for (int n = 0; n < C::NF; ++n) f.b[s][n] = *(const LAS bf16x8*)(b + rdB[s] + n * 2048);
      } else {
#pragma unroll
        for (int n = 0; n < C::NF; ++n) {
          bf16x4 lo = __builtin_amdgcn_ds_read_tr16_b64_v4i16((LAS bf16x4*)(b + rdB[n] + s * 4096));
          bf16x4 hi = __builtin_amdgcn_ds_read_tr16_b64_v4i16((LAS bf16x4*)(b + rdB[n] + s * 4096 + 512));
          f.b[s][n] = __builtin_shufflevector(lo, hi, 0, 1, 2, 3, 4, 5, 6, 7);
        }
      }
      if constexpr (C::AMODE == 0) {
#pragma unroll
        for (int m = 0; m < C::MF; ++m) f.a[s][m] = *(const LAS bf16x8*)(a + rdA[0] + s * (C::BM * 64) + m * 256);
      } else {
#pragma unroll
        for (int m = 0; m < C::MF; ++m) f.a[s][m] = *(const LAS bf16x8*)(a + rdA[s] + m * 2048);
      }
    }
  }

  __device__ __forceinline__ void mma(f32x4 (&acc)[C::MF][C::NF], const Frags<C>& f) {
#pragma unroll
    for (int s = 0; s < C::KSW; ++s)
#pragma unroll
      for (int m = 0; m < C::MF; ++m)
#pragma unroll
        for (int n = 0; n < C::NF; ++n)
          acc[m][n] = C::SWAP ? __builtin_amdgcn_mfma_f32_16x16x32_bf16(f.b[s][n], f.a[s][m], acc[m][n], 0, 0, 0)
                              : __builtin_amdgcn_mfma_f32_16x16x32_bf16(f.a[s][m], f.b[s][n], acc[m][n], 0, 0, 0);
    if constexpr (C::BMODE == 3) {
      const bf16x8 ones = {0x3F80, 0x3F80, 0x3F80, 0x3F80, 0x3F80, 0x3F80, 0x3F80, 0x3F80};
      accC = __builtin_amdgcn_mfma_f32_16x16x32_bf16(f.b[0][0], ones, accC, 0, 0, 0);
    }
  }
  static constexpr int NRD = C::KSW * (C::MF + (C::BMODE >= 2 ? 2 : 1) * C::NF);
  static constexpr int NMM = C::KSW * C::MF * C::NF + (C::BMODE == 3 ? 1 : 0);
  __device__ __forceinline__ void interleave() {
    constexpr int RPM = (NRD + NMM - 3) / (NMM - 2);
#pragma unroll
    for (int i = 0; i < NMM; ++i) {
      __builtin_amdgcn_sched_group_barrier(0x100, RPM, 0);
      __builtin_amdgcn_sched_group_barrier(0x008, 1, 0);
    }
  }
  unsigned na, nb;
  __device__ __forceinline__ void step(f32x4 (&acc)[C::MF][C::NF], Frags<C>& cur, Frags<C>& nxt) {
    na += C::ABYTES; if (na == C::RA) na = 0;
    nb += C::BBYTES; if (nb == C::RB) nb = 0;
    wait_lgkm0(); bar();
    __builtin_amdgcn_sched_barrier(0);
    read(nxt, na, nb);
    mma(acc, cur);
    interleave();
    __builtin_amdgcn_sched_barrier(0);
  }

  template <class Hook, class Hook2> __device__ __forceinline__ void run(f32x4 (&acc)[C::MF][C::NF], Hook hook, Hook2 hook2) {
    Frags<C> f0, f1;
#pragma unroll
    for (int m = 0; m < C::MF; ++m)
#pragma unroll
      for (int n = 0; n < C::NF; ++n) acc[m][n] = (f32x4){0.f, 0.f, 0.f, 0.f};
    bar();
    if (wid == 0) { STAMP(C::KID, 1); CSTAMP(C::KID, 8); }
    hook();
    read(f0, 0, 0);
    na = 0; nb = 0;
    if (wid == 0) STAMP(C::KID, 4);
#pragma unroll 1
    for (int k = 0; k + 2 < C::NK; k += 2) {
      step(acc, f0, f1);
      step(acc, f1, f0);
    }
    if (wid == 0) STAMP(C::KID, 5);
    hook2();
    if (wid == 0) STAMP(C::KID, 6);
    if constexpr (C::NK % 2 == 0) {
      step(acc, f0, f1);
      wait_lgkm0(); bar();
      mma(acc, f1);
    } else {
      wait_lgkm0(); bar();
      mma(acc, f0);
    }
    if (wid == 0) { STAMP(C::KID, 2); CSTAMP(C::KID, 9); }
  }

  static constexpr bool SPLM = (C::MF % 2 == 0);
  static constexpr int RM = SPLM ? C::MF / 2 : C::MF, RN = SPLM ? C::NF : C::NF / 2;
  __device__ __forceinline__ void reduce_k(f32x4 (&acc)[C::MF][C::NF], f32x4 (&red)[RM][RN]) {
    static_assert(C::WK == 2, "");
    static_assert(LDS_X + C::NC * RM * RN * 1024 <= LDS_MISC || RM * RN == 6, "");
    LAS f32x4* x = (LAS f32x4*)(lds + LDS_X - (RM * RN == 6 ? 1024 : 0));
#pragma unroll
    for (int m = 0; m < RM; ++m)
#pragma unroll
      for (int n = 0; n < RN; ++n) {
        const f32x4 lo = acc[m][n], hi = SPLM ? acc[m + RM][n] : acc[m][n + RN];
        x[(wid * RM * RN + m * RN + n) * 64 + lane] = wk ? lo : hi;
        red[m][n] = wk ? hi : lo;
      }
    wait_lgkm0(); bar();
#pragma unroll
    for (int m = 0; m < RM; ++m)
#pragma unroll
      for (int n = 0; n < RN; ++n) red[m][n] += x[((wid ^ 1) * RM * RN + m * RN + n) * 64 + lane];
  }
};

constexpr int FLE = 0;
constexpr int FL0 = 32;
constexpr int FL1 = FL0 + 2048;
constexpr int FL2 = FL1 + 16 * 16;
constexpr int FL3 = FL2 + 8 * 32;

__device__ __forceinline__ void publish(LAS unsigned* arr, unsigned nwaves, u64* flag, u64 tag, int lane, int kid) {
  if (threadIdx.x < 64) STAMP(kid, 7);
  asm volatile("" ::: "memory");
  if (lane == 0) __hip_atomic_fetch_add(arr + 8, 1u, RLX_WG);
  wait_vm<0>();
  if (lane == 0) {
    const unsigned old = __hip_atomic_fetch_add(arr, 1u, RLX_WG);
    if (old == nwaves - 1) __hip_atomic_store(flag, tag, RLX_AGENT);
  }
}
__device__ __forceinline__ void lds_wait(LAS unsigned* w, unsigned want) {
  const unsigned a = (unsigned)(size_t)w;
#pragma unroll 1
  for (unsigned i = 0; i < (1u << 24); ++i) {
    unsigned v;
    asm volatile("ds_read_b32 %0, %1\n\ts_waitcnt lgkmcnt(0)" : "=v"(v) : "v"(a) : "memory");
    if (__builtin_amdgcn_readfirstlane(v) >= want) break;
    __builtin_amdgcn_s_sleep(1);
  }
}
template <int SLEEP, int NW>
__device__ __forceinline__ void await(const u64* word, bool need, u64 tag, int lw, LAS unsigned* go, unsigned epoch) {
  if (lw == 0) {
    const u64 t0 = wall_clock64();
    for (;;) {
      bool ok = true;
#pragma unroll
      for (int i = 0; i < NW; ++i) ok &= __hip_atomic_load(word + i * 64, RLX_AGENT) == tag;
      if (__all(!need || ok)) break;
      if (wall_clock64() - t0 > 2000000ull) break;
      __builtin_amdgcn_s_sleep(SLEEP);
    }
    __hip_atomic_store(go, epoch, RLX_WG);
  } else {
    lds_wait(go, epoch);
  }
  __builtin_amdgcn_fence(__ATOMIC_ACQUIRE, "wavefront");
}
#define P1_PRE 1
#define PRE_LATE 2
#define PSLEEP 4

#define PRE1 2
#define PRE2 P2B
#define PRE3 P3B
#define PRE4 P4B
using C1 = Cfg<1, 80, 128, 64, 1, 4, 1, 4, 6, 32, 0, 1, 0, PRE1>;
#define P2A 4
#define P2B 10
using C2 = Cfg<2, 64, 80, 64, 2, 1, 2, P2A, P2B, 20, 0, 1, 1, PRE2>;
#define P3A 4
#define P3B 8
using C3 = Cfg<3, 256, 128, 32, 4, 2, 1, P3A, P3B, 20, 0, 3, 1, PRE3>;
#define P4A 4
#define P4B 10
using C4 = Cfg<4, 96, 64, 64, 2, 1, 2, P4A, P4B, 27, 0, 4, 1, PRE4>;
#define HPRIO 2
#define WARM 0
#define WARM_BACK 0
#define WWARM 0
#define ROT 15
struct Src1 {
  const char* a; const char* b; int o, c; const char* w[5]; int bid;
  static constexpr int ldA = 0, ldB = S_ * 2, AUXA = AUXH, AUXB = 0, rows = 80;
  __device__ __forceinline__ void extra(int k, int lw, int lane, LAS char* scr) const {
    if (WWARM && lw == 0 && ((k + o) & 7) == 0) {
      const int l = (k >> 3) * 16384 + bid * 64 + lane;
      const char* a = w[0] + l * 128;
      if (l >= 6400) a = w[1] + (l - 6400) * 128;
      if (l >= 12800) a = w[2] + (l - 12800) * 128;
      if (l >= 30080) a = w[3] + (l - 30080) * 128;
      if (l >= 47360) a = w[4] + (l - 47360) * 128;
      if (l < 64640) touch<0>(a, scr);
    }
  }
  __device__ __forceinline__ int kt(int k) const { const int s = (k + o) & 31; return (ROT & 1) ? ((s & 24) | ((s + c) & 7)) : k; }
  __device__ __forceinline__ const char* baseA(int k) const { return a + kt(k) * (80 * 128); }
  __device__ __forceinline__ const char* baseB(int k, int) const { return b + kt(k) * 128; }
};
struct Src2 {
  const char* a; const char* bre; const char* bim; int rot;
  static constexpr int ldA = 0, ldB = D_ * 2, AUXA = AUXH, AUXB = 0, rows = 64;
  __device__ __forceinline__ void extra(int, int, int, LAS char*) const {}
  __device__ __forceinline__ int kt(int k) const { const int s = k + ((ROT & 2) ? rot : 0); return s >= 20 ? s - 20 : s; }
  __device__ __forceinline__ const char* baseA(int k) const { return a + kt(k) * (64 * 128); }
  __device__ __forceinline__ const char* baseB(int k, int) const { const int t = kt(k); return t < 10 ? bre + t * 128 : bim + (t - 10) * 128; }
};
struct Src3 {
  const char* a; const char* bg; const char* bu; int rot;
  static constexpr int ldA = 0, ldB = F_ * 2, AUXA = AUXH, AUXB = 0, rows = 256;
  __device__ __forceinline__ void extra(int, int, int, LAS char*) const {}
  __device__ __forceinline__ int kt(int k) const { const int s = k + ((ROT & 4) ? rot : 0); return s >= 20 ? s - 20 : s; }
  __device__ __forceinline__ const char* baseA(int k) const { return a + kt(k) * (256 * 64); }
  __device__ __forceinline__ const char* baseB(int k, int up) const { return (up ? bu : bg) + kt(k) * (32 * F_ * 2); }
};
struct Src4 {
  const char* a; const char* b; int rows, rot;
  static constexpr int ldA = 0, ldB = D_ * 2, AUXA = AUXH, AUXB = 0;
  __device__ __forceinline__ void extra(int, int, int, LAS char*) const {}
  __device__ __forceinline__ int kt(int k) const { const int s = k + ((ROT & 8) ? rot : 0); return s >= 27 ? s - 27 : s; }
  __device__ __forceinline__ const char* baseA(int k) const { return a + kt(k) * (96 * 128); }
  __device__ __forceinline__ const char* baseB(int k, int) const { return b + kt(k) * (64 * D_ * 2); }
};

struct Params {
  const u16* x; const u16* sre; const u16* sim; const u16* hre; const u16* him; const u16* wg; const u16* wu; const u16* wd;
  u16* xnI; u16* pqI; u16* hI; u16* hhI; f32x2* stats; u64* flags; u16* out;
};

template <int CTRL, int RMASK> __device__ __forceinline__ float dpp_add(float v) {
  return v + __int_as_float(__builtin_amdgcn_update_dpp(0, __float_as_int(v), CTRL, RMASK, 0xf, true));
}
__device__ __forceinline__ float wave_sum(float v) {
  v = dpp_add<0x111, 0xf>(v); v = dpp_add<0x112, 0xf>(v); v = dpp_add<0x114, 0xf>(v); v = dpp_add<0x118, 0xf>(v);
  v = dpp_add<0x142, 0xa>(v); v = dpp_add<0x143, 0xc>(v);
  return __int_as_float(__builtin_amdgcn_readlane(__float_as_int(v), 63));
}
template <int N> __device__ __forceinline__ void bars() {
#pragma unroll 1
  for (int i = 0; i < N; ++i) bar();
}

__global__ __launch_bounds__(NWAVES * 64) void mega(const Params p) {
  __shared__ __attribute__((aligned(1024))) char smem_[LDS_TOTAL];
  LAS char* lds = (LAS char*)smem_;
  u64 pc0; asm volatile("s_getpc_b64 %0" : "=s"(pc0));
  const int wid = __builtin_amdgcn_readfirstlane(threadIdx.x >> 6), lane = threadIdx.x & 63;
  const int bid = blockIdx.x, g = bid & 7, idx = bid >> 3;
  LAS unsigned* arr = (LAS unsigned*)(lds + LDS_MISC + 256);
  if (wid == 0) STAMP(0, 0);
  const u16* xr = p.x + (bid * 8 + (wid & 7)) * D_;
  const bf16x8 v0 = *(const bf16x8*)(xr + lane * 8);
  const bf16x8 v1 = *(const bf16x8*)(xr + 512 + (lane & 15) * 8);
  const u64 tag = __hip_atomic_load(p.flags + FLE, RLX_AGENT) + 1ull;
  if (threadIdx.x < 16) arr[threadIdx.x] = 0u;
  wait_lgkm0(); bar();
  const int j1 = idx & 7, mq = idx >> 3, isQ = mq >> 1, t1 = g * 256 + (mq & 1) * 128;
  const int j2 = idx & 7, mt2 = g * 4 + (idx >> 3);
  const int mt3 = (g & 6) + (idx & 1), j3 = (g & 1) * 14 + (idx >> 1);
  const bool on3 = idx < 28 && j3 < 27;
  const int lin4 = g * 28 + idx, mt4 = lin4 / 10, j4 = lin4 % 10;
  const bool on4 = idx < 28 && lin4 < 220;
  LAS f32x4* rs = (LAS f32x4*)(lds + LDS_X);
  LAS float* csl = (LAS float*)(lds + LDS_X + 4096);

  if (wid >= 8) {
    const int lw = wid - 8;
    {
      asm volatile("" :: "v"(tag) : "memory");
      if (lane == 0) __hip_atomic_fetch_add(arr + 5, 1u, RLX_WG);
      Src1 s{(const char*)(p.xnI + (size_t)j1 * 256 * 80 * 8), (const char*)((isQ ? p.sim : p.sre) + (size_t)t1 * S_), (g + 2 * mq) & 7, j1,
             {(const char*)p.hre, (const char*)p.him, (const char*)p.wg, (const char*)p.wu, (const char*)p.wd}, bid};
      Loader<C1, Src1> L(lds, s, lw, lane);
      lds_wait(arr + 4, 8);
      L.preloadB();
      lds_wait(arr + 5, 10);
      await<PSLEEP, 4>(p.flags + FL0 + g * 256 + lane, true, tag, lw, arr + 3, 1u);
      if (bid == 0 && lw == 0 && lane == 0) __hip_atomic_store(p.flags + FLE, tag, RLX_AGENT);
      if (lw == 0) STAMP(1, 0);
      wait_vm<0>();
      L.run([] {}, [] {});
      bar();
    }
    {
      Src2 s{(const char*)(p.pqI + (size_t)mt2 * 160 * 64 * 8), (const char*)(p.hre + (size_t)j2 * 80 * D_), (const char*)(p.him + (size_t)j2 * 80 * D_), (mt2 * 5) >> 3};
      Loader<C2, Src2> L(lds, s, lw, lane);
      if (PRE_LATE == 1) lds_wait(arr + 0, C1::NC);
      if (PRE_LATE == 2) lds_wait(arr + 8, C1::NC);
      L.preloadB();
      lds_wait(arr + 0, C1::NC);
      await<PSLEEP, 1>(p.flags + FL1 + (mt2 >> 1) * 16 + (lane & 15), true, tag, lw, arr + 3, 2u);
      if (lw == 0) STAMP(2, 0);
      wait_vm<0>();
      L.run([] {}, [] {});
      bar();
    }
    if (on3) {
      Src3 s{(const char*)(p.hI + (size_t)mt3 * 80 * 256 * 8), (const char*)(p.wg + j3 * 64), (const char*)(p.wu + j3 * 64), (mt3 * 5) >> 1};
      Loader<C3, Src3> L(lds, s, lw, lane);
      bar();
      if (PRE_LATE == 1) lds_wait(arr + 1, C2::NC);
      if (PRE_LATE == 2) lds_wait(arr + 9, C2::NC);
      L.preloadB();
      lds_wait(arr + 1, C2::NC);
      await<PSLEEP, 1>(p.flags + FL2 + mt3 * 32 + (lane & 31), true, tag, lw, arr + 3, 3u);
      if (lw == 0) STAMP(3, 0);
      wait_vm<0>();
      f32x2 pv[2][8];
      L.run([&] {
#pragma unroll
        for (int h = 0; h < 2; ++h)
#pragma unroll
          for (int i = 0; i < 8; ++i) pv[h][i] = __builtin_bit_cast(f32x2, ld8(p.stats + i * S_ + mt3 * 256 + lw * 128 + h * 64 + lane));
      }, [&] {
#pragma unroll
        for (int h = 0; h < 2; ++h) {
          float mean = 0.f, q = 0.f, dd = 0.f;
#pragma unroll
          for (int i = 0; i < 8; ++i) { mean += pv[h][i][0]; q += pv[h][i][1]; }
          mean *= 0.125f;
#pragma unroll
          for (int i = 0; i < 8; ++i) { const float d = pv[h][i][0] - mean; dd += d * d; }
          q += 80.0f * dd;
          const float den = rbf(__builtin_amdgcn_sqrtf(rbf(rbf(q * (1.0f / D_)) + 1e-5f)));
          rs[lw * 128 + h * 64 + lane] = (f32x4){-rbf(mean), den, -1.44269504089f * __builtin_amdgcn_rcpf(den), 0.f};
        }
        wait_lgkm0();
      });
      bar();
    } else {
      bar();
    }
    if (on4) {
      Src4 s{(const char*)(p.hhI + (size_t)mt4 * 216 * 96 * 8), (const char*)(p.wd + j4 * 64), min(96, S_ - mt4 * 96), (mt4 * 27) / 22};
      Loader<C4, Src4> L(lds, s, lw, lane);
      if (PRE_LATE == 1 && on3) lds_wait(arr + 2, C3::NC);
      if (PRE_LATE == 2 && on3) lds_wait(arr + 10, C3::NC);
      L.preloadB();
      const int ra = (mt4 * 96) >> 8, rb = min(mt4 * 96 + 95, S_ - 1) >> 8;
      if (on3) {
        lds_wait(arr + 2, C3::NC);
        await<PSLEEP, 1>(p.flags + FL3 + (lane < 32 ? ra : rb) * 32 + (lane & 31), (lane & 31) < 27, tag, lw, arr + 3, 4u);
      } else {
        await<32, 1>(p.flags + FL3 + (lane < 32 ? ra : rb) * 32 + (lane & 31), (lane & 31) < 27, tag, lw, arr + 3, 4u);
      }
      if (lw == 0) STAMP(4, 0);
      wait_vm<0>();
      L.run([] {}, [] {});
      bar();
      bar();
    }
    return;
  }

  const int fr = lane & 15, fq = lane >> 4;
  {
    float f[16];
#pragma unroll
    for (int i = 0; i < 8; ++i) { f[i] = bf2f((u16)v0[i]); f[8 + i] = bf2f((u16)v1[i]); }
    const float piv = __int_as_float(__builtin_amdgcn_readfirstlane(__float_as_int(f[0])));
    asm volatile("" :: "v"(f[8]) : "memory");
    if (lane == 0) __hip_atomic_fetch_add(arr + 4, 1u, RLX_WG);
    if (wid == 0) STAMP(0, 4);
    float s0 = 0.f, s1 = 0.f, q0 = 0.f, q1 = 0.f;
#pragma unroll
    for (int i = 0; i < 8; ++i) { const float d = f[i] - piv, e = f[8 + i] - piv; s0 += d; q0 += d * d; s1 += e; q1 += e * e; }
    const float sd = wave_sum(s0 + (lane < 16 ? s1 : 0.f)) * (1.0f / D_);
    const float qd = wave_sum(q0 + (lane < 16 ? q1 : 0.f)) * (1.0f / D_);
    const float mb = rbf(piv + sd), vb = rbf(fmaxf(qd - sd * sd, 0.f));
    const float rden = __builtin_amdgcn_rcpf(rbf(__builtin_amdgcn_sqrtf(rbf(vb + 1e-5f))));
    u32x4 w0, w1;
#pragma unroll
    for (int i = 0; i < 4; ++i) {
      w0[i] = pk2(rbf(f[2 * i] - mb) * rden, rbf(f[2 * i + 1] - mb) * rden);
      w1[i] = pk2(rbf(f[8 + 2 * i] - mb) * rden, rbf(f[9 + 2 * i] - mb) * rden);
    }
    LAS char* tb = lds + LDS_X;
    *(LAS u32x4*)(tb + wid * 1296 + lane * 16) = w0;
    if (lane < 16) *(LAS u32x4*)(tb + wid * 1296 + 1024 + lane * 16) = w1;
    wait_lgkm0();
    if (lane == 0) __hip_atomic_fetch_add(arr + 7, 1u, RLX_WG);
    lds_wait(arr + 7, 8);
    auto rsrc = __builtin_amdgcn_make_buffer_rsrc(p.xnI, 0, S_ * D_ * 2, 0x00020000);
#pragma unroll
    for (int h = 0; h < 2; ++h) {
      const int d = h * 512 + wid * 64 + lane;
      if (d < D_) {
        bf16x8 o;
#pragma unroll
        for (int r = 0; r < 8; ++r) o[r] = *(const LAS short*)(tb + r * 1296 + d * 2);
        const int j = d / 80, n = d % 80;
        __builtin_amdgcn_raw_buffer_store_b128(__builtin_bit_cast(u32x4, o), rsrc, ((j * 256 + bid) * 80 + n) * 16, 0, 16);
      }
    }
    if (wid == 0) STAMP(0, 7);
    wait_vm<0>();
    lds_wait(arr + 5, 2);
    unsigned old = 0;
    if (lane == 0) old = __hip_atomic_fetch_add(arr + 5, 1u, RLX_WG);
    if (__builtin_amdgcn_readfirstlane(old) == 9u && lane < 8) __hip_atomic_store(p.flags + FL0 + lane * 256 + bid, tag, RLX_AGENT);
    if (wid == 0) STAMP(0, 3);
  }
  if (wid < C1::NC) {
    Consumer<C1> L(lds, wid, lane);
    f32x4 acc[C1::MF][C1::NF];
    L.run(acc, [] {}, [] {});
    const float sg = isQ ? -1.f : 1.f;
#pragma unroll
    for (int n = 0; n < 2; ++n) {
      const int t = t1 + L.wn * 32 + n * 16 + fr, mt = t >> 6, r = t & 63;
#pragma unroll
      for (int m = 0; m < 5; ++m) {
        const int koct = isQ * 80 + j1 * 10 + m * 2 + (fq >> 1);
        u32x2 w; w[0] = pk2(sg * acc[m][n][0], sg * acc[m][n][1]); w[1] = pk2(sg * acc[m][n][2], sg * acc[m][n][3]);
        st8(p.pqI + (((size_t)mt * 160 + koct) * 64 + r) * 8 + (fq & 1) * 4, w);
      }
    }
    publish(arr + 0, C1::NC, p.flags + FL1 + (t1 >> 7) * 16 + isQ * 8 + j1, tag, lane, 1);
    if (wid == 0) STAMP(1, 3);
  } else {
    bars<C1::NBAR>();
  }
  if (wid < C2::NC) {
    Consumer<C2> L(lds, wid, lane);
    f32x4 acc[C2::MF][C2::NF];
    const int t = mt2 * 64 + L.wm * 32 + L.wk * 16 + fr;
    bf16x4 xv[C2::NF];
#pragma unroll
    for (int n = 0; n < C2::NF; ++n) xv[n] = *(const bf16x4*)(p.x + (size_t)t * D_ + j2 * 80 + n * 16 + fq * 4);
#pragma unroll
    for (int n = 0; n < C2::NF; ++n) asm volatile("" : "+v"(xv[n]));
    L.run(acc, [] {}, [] {});
    f32x4 red[1][5];
    L.reduce_k(acc, red);
    float hv[C2::NF][4];
    float s = 0.f;
#pragma unroll
    for (int n = 0; n < C2::NF; ++n) {
      u32x2 w;
      w[0] = pk2(red[0][n][0] + bf2f((u16)xv[n][0]), red[0][n][1] + bf2f((u16)xv[n][1]));
      w[1] = pk2(red[0][n][2] + bf2f((u16)xv[n][2]), red[0][n][3] + bf2f((u16)xv[n][3]));
      hv[n][0] = __uint_as_float(w[0] << 16); hv[n][1] = __uint_as_float(w[0] & 0xffff0000u);
      hv[n][2] = __uint_as_float(w[1] << 16); hv[n][3] = __uint_as_float(w[1] & 0xffff0000u);
      s += (hv[n][0] + hv[n][1]) + (hv[n][2] + hv[n][3]);
      const int koct = j2 * 10 + n * 2 + (fq >> 1);
      st8(p.hI + (((size_t)(t >> 8) * 80 + koct) * 256 + (t & 255)) * 8 + (fq & 1) * 4, w);
    }
    s += __shfl_xor(s, 16); s += __shfl_xor(s, 32);
    const float mean = s * (1.0f / 80.0f);
    float q = 0.f;
#pragma unroll
    for (int n = 0; n < C2::NF; ++n)
#pragma unroll
      for (int r = 0; r < 4; ++r) { const float d = hv[n][r] - mean; q += d * d; }
    q += __shfl_xor(q, 16); q += __shfl_xor(q, 32);
    if (fq == 0) st8(p.stats + j2 * S_ + t, __builtin_bit_cast(u32x2, (f32x2){mean, q}));
    publish(arr + 1, C2::NC, p.flags + FL2 + g * 32 + idx, tag, lane, 2);
    if (wid == 0) STAMP(2, 3);
  } else {
    bars<C2::NBAR>();
  }
  if (on3) {
    Consumer<C3> L(lds, wid, lane);
    f32x4 acc[C3::MF][C3::NF];
    L.run(acc, [] {}, [] {});
    if (fr == 0) *(LAS f32x4*)(csl + (L.wm >> 1) * 64 + L.wn * 32 + (L.wm & 1) * 16 + fq * 4) = L.accC;
    wait_lgkm0();
    if (lane == 0) __hip_atomic_fetch_add(arr + 6, 1u, RLX_WG);
    lds_wait(arr + 6, 8);
#pragma unroll
    for (int i = 0; i < 2; ++i) {
      const bool sw = ((i + L.wm) & 2) != 0;
      const int fl = L.wn * 32 + ((i + L.wm) & 1) * 16 + fq * 4, f0 = j3 * 64 + fl;
      const f32x4 cg = *(const LAS f32x4*)(csl + fl), cu = *(const LAS f32x4*)(csl + 64 + fl);
#pragma unroll
      for (int m = 0; m < C3::MF; ++m) {
        const int rl = L.wm * 64 + m * 16 + fr, t = mt3 * 256 + rl;
        const f32x4 st = rs[rl];
        const f32x4 ag = sw ? acc[m][i + 2] : acc[m][i], au = sw ? acc[m][i] : acc[m][i + 2];
        float o[4];
#pragma unroll
        for (int r = 0; r < 4; ++r) {
          const float e = __builtin_amdgcn_exp2f(__builtin_fmaf(cg[r], st[0], ag[r]) * st[2]);
          o[r] = __builtin_fmaf(cu[r], st[0], au[r]) * __builtin_amdgcn_rcpf(__builtin_fmaf(e, st[1], st[1]));
        }
        u32x2 w; w[0] = pk2(o[0], o[1]); w[1] = pk2(o[2], o[3]);
        const int t4 = t / 96;
        st8(p.hhI + (((size_t)t4 * 216 + (f0 >> 3)) * 96 + (t - t4 * 96)) * 8 + (fq & 1) * 4, w);
      }
    }
    publish(arr + 2, C3::NC, p.flags + FL3 + mt3 * 32 + j3, tag, lane, 3);
    if (wid == 0) STAMP(3, 3);
  }
  if (on4) {
    if (wid < C4::NC) {
      Consumer<C4> L(lds, wid, lane);
      f32x4 acc[C4::MF][C4::NF];
      const int tb = mt4 * 96 + L.wm * 48 + fr, c0 = j4 * 64 + L.wk * 32 + fq * 4;
      u32x2 hv[3][2];
      L.run(acc, [&] {
#pragma unroll
        for (int m = 0; m < 3; ++m)
#pragma unroll
          for (int n = 0; n < 2; ++n) {
            const int t = min(tb + m * 16, S_ - 1), c = c0 + n * 16;
            hv[m][n] = ld8(p.hI + (((size_t)(t >> 8) * 80 + (c >> 3)) * 256 + (t & 255)) * 8 + (c & 4));
          }
      }, [] {});
      f32x4 red[3][2];
      L.reduce_k(acc, red);
#pragma unroll
      for (int m = 0; m < 3; ++m)
#pragma unroll
        for (int n = 0; n < 2; ++n) {
          const int t = tb + m * 16;
          u32x2 w;
          w[0] = pk2(red[m][n][0] + __uint_as_float(hv[m][n][0] << 16), red[m][n][1] + __uint_as_float(hv[m][n][0] & 0xffff0000u));
          w[1] = pk2(red[m][n][2] + __uint_as_float(hv[m][n][1] << 16), red[m][n][3] + __uint_as_float(hv[m][n][1] & 0xffff0000u));
          if (t < S_) *(u32x2*)(p.out + (size_t)t * D_ + c0 + n * 16) = w;
        }
      if (wid == 0) { wait_vm<0>(); STAMP(4, 3); }
    }
  }
}

extern "C" void kernel_launch(void* const* d_in, const int* in_sizes, int n_in, void* d_out, int out_size,
                              void* d_ws, size_t ws_size, hipStream_t stream) {
  char* ws = (char*)d_ws;
  Params p;
  p.x = (const u16*)d_in[0]; p.sre = (const u16*)d_in[1]; p.sim = (const u16*)d_in[2];
  p.hre = (const u16*)d_in[3]; p.him = (const u16*)d_in[4];
  p.wg = (const u16*)d_in[6]; p.wu = (const u16*)d_in[7]; p.wd = (const u16*)d_in[8];
  p.flags = (u64*)ws;
  p.stats = (f32x2*)(ws + 131072);
  p.xnI = (u16*)(ws + (1u << 20));
  p.pqI = (u16*)(ws + (4u << 20));
  p.hI = (u16*)(ws + (10u << 20));
  p.hhI = (u16*)(ws + (13u << 20));
  p.out = (u16*)d_out;
  mega<<<256, NWAVES * 64, 0, stream>>>(p);
}
```

```cpp
#include <hip/hip_runtime.h>
#include <hip/hip_bf16.h>
#include <type_traits>
#define PSLEEP 4
#define ISLEEP 16
#define NRAMP 3
#define AUXH 16

typedef unsigned short u16;
typedef unsigned long long u64;
typedef short bf16x8 __attribute__((ext_vector_type(8)));
typedef short bf16x4 __attribute__((ext_vector_type(4)));
typedef float f32x4 __attribute__((ext_vector_type(4)));
typedef float f32x2 __attribute__((ext_vector_type(2)));
typedef unsigned u32x2 __attribute__((ext_vector_type(2)));
typedef unsigned u32x4 __attribute__((ext_vector_type(4)));
#define LAS __attribute__((address_space(3)))
#define GAS __attribute__((address_space(1)))
#define RLX_AGENT __ATOMIC_RELAXED, __HIP_MEMORY_SCOPE_AGENT
#define RLX_WG __ATOMIC_RELAXED, __HIP_MEMORY_SCOPE_WORKGROUP

constexpr int S_ = 2048, D_ = 640, F_ = 1728;

__device__ __forceinline__ float bf2f(u16 v) { return __uint_as_float(((unsigned)v) << 16); }
typedef __bf16 bf16x2n __attribute__((ext_vector_type(2)));
__device__ __forceinline__ unsigned pk2(float lo, float hi) {
  return __builtin_bit_cast(unsigned, __builtin_convertvector((f32x2){lo, hi}, bf16x2n));
}
__device__ __forceinline__ float rbf(float v) { return __uint_as_float(pk2(v, v) << 16); }

#define CSTAMP(kid, i)
#define STAMP(kid, i)
template <int N> __device__ __forceinline__ void wait_vm() { asm volatile("s_waitcnt vmcnt(%0)" ::"n"(N) : "memory"); }
__device__ __forceinline__ void wait_lgkm0() { asm volatile("" ::: "memory"); __builtin_amdgcn_s_waitcnt(0xC07F); asm volatile("" ::: "memory"); }
__device__ __forceinline__ void bar() { asm volatile("" ::: "memory"); __builtin_amdgcn_s_barrier(); asm volatile("" ::: "memory"); }
template <int N, int STEP> __device__ __forceinline__ void wait_dyn(int n) {
  if constexpr (N <= 0) wait_vm<0>();
  else { if (n >= N) wait_vm<N>(); else wait_dyn<N - STEP, STEP>(n); }
}

template <int AUX> __device__ __forceinline__ void glds16(const char* g, LAS char* l) {
  __builtin_amdgcn_global_load_lds((const GAS void*)g, (LAS void*)l, 16, 0, AUX);
}
template <int AUX> __device__ __forceinline__ void touch(const char* g, LAS char* scratch) {
  __builtin_amdgcn_global_load_lds((const GAS void*)g, (LAS void*)scratch, 4, 0, AUX);
}
__device__ __forceinline__ void st8(void* p, u32x2 v) { __hip_atomic_store((u64*)p, __builtin_bit_cast(u64, v), RLX_AGENT); }
__device__ __forceinline__ u32x2 ld8(const void* p) { return __builtin_bit_cast(u32x2, __hip_atomic_load((u64*)p, RLX_AGENT)); }

constexpr int NL = 2;
constexpr int NWAVES = 8;
constexpr int LDS_RING = 136 * 1024;
constexpr int LDS_X = LDS_RING;
constexpr int LDS_TOTAL = 160 * 1024;
constexpr int LDS_MISC = LDS_TOTAL - 512;

template <int KID_, int BM_, int BN_, int BK_, int WM_, int WN_, int WK_, int NSA_, int NSB_, int NK_, int AMODE_, int BMODE_, int SWAP_, int NPRE_>
struct Cfg {
  static constexpr int KID = KID_, BM = BM_, BN = BN_, BK = BK_, WM = WM_, WN = WN_, WK = WK_, NSA = NSA_, NSB = NSB_, NK = NK_;
  static constexpr int AMODE = AMODE_, BMODE = BMODE_, SWAP = SWAP_, NPRE = NPRE_;
  static constexpr int NC = WM * WN * WK;
  static constexpr int MF = BM / WM / 16, NF = BN / WN / 16, KSW = BK / 32 / WK;
  static constexpr int ABYTES = BM * BK * 2, BBYTES = BN * BK * 2;
  static constexpr int PA = ABYTES / 1024, PB = BBYTES / 1024;
  static constexpr int GA = PA / NL, GB = PB / NL, G = GA + GB;
  static constexpr int RA = NSA * ABYTES, RB = NSB * BBYTES;
  static constexpr int NBAR = NK + (WK == 2 ? 2 : 1);
  static_assert(PA % NL == 0 && PB % NL == 0, "pieces");
  static_assert(RA + RB <= LDS_RING, "lds");
  static_assert(NSA <= NK && NSB <= NK && NSB >= NSA, "stages");
  static_assert(KSW >= 1 && KSW <= 2, "ksw");
};

template <class C> struct Frags { bf16x8 a[C::KSW][C::MF]; bf16x8 b[C::KSW][C::NF]; };

template <class C, class Src>
struct Loader {
  LAS char* lds; const Src& src;
  int lw, lane;
  unsigned voffA[C::GA], voffB[C::GB];
  static __device__ __forceinline__ unsigned rowmajor(int p, int ld) {
    const int row = p >> 3, c = (p & 7) ^ ((row >> 1) & 7);
    return row * ld + c * 16;
  }
  __device__ __forceinline__ Loader(LAS char* lds_, const Src& s, int lw_, int lane_) : lds(lds_), src(s), lw(lw_), lane(lane_) {
#pragma unroll
    for (int i = 0; i < C::GA; ++i) {
      const int p = (i * NL + lw) * 64 + lane;
      if constexpr (C::AMODE == 0) { const int r = p % C::BM; voffA[i] = (p - r + (r < src.rows ? r : (r & 31))) * 16; }
      else voffA[i] = rowmajor(p, Src::ldA);
    }
#pragma unroll
    for (int i = 0; i < C::GB; ++i) {
      const int p = (i * NL + lw) * 64 + lane;
      if constexpr (C::BMODE == 0) voffB[i] = p * 16;
      else if constexpr (C::BMODE == 1) voffB[i] = rowmajor(p, Src::ldB);
      else { const int q = p % (C::BK * 8), k = q >> 3, c = (q & 7) ^ ((((k >> 3) & 1) << 2) | (((k >> 1) & 1) << 1)); voffB[i] = k * Src::ldB + c * 16; }
    }
  }
  __device__ __forceinline__ void issueA(int k, unsigned buf) {
    const char* ga = src.baseA(k);
    LAS char* la = lds + buf + lw * 1024;
#pragma unroll
    for (int i = 0; i < C::GA; ++i) glds16<Src::AUXA>(ga + voffA[i], la + i * NL * 1024);
  }
  __device__ __forceinline__ void issueB(int k, unsigned buf) {
    LAS char* lb = lds + C::RA + buf + lw * 1024;
#pragma unroll
    for (int i = 0; i < C::GB; ++i) glds16<Src::AUXB>(src.baseB(k, i >= C::GB / 2) + voffB[i], lb + i * NL * 1024);
  }
  __device__ __forceinline__ void preloadB() {
#pragma unroll 1
    for (int s = 0; s < C::NPRE; ++s) issueB(s, s * C::BBYTES);
  }
  static constexpr int cmin(int a, int b) { return a < b ? a : b; }
  static constexpr int cmax(int a, int b) { return a > b ? a : b; }
  static constexpr int PP(int s) { return cmin(s, C::NSA) * C::GA + cmax(cmin(s, C::NSB) - C::NPRE, 0) * C::GB; }
  static constexpr int T(int i) { return PP(C::NSB) + cmin(i, C::NK - C::NSA) * C::GA + cmin(i, C::NK - C::NSB) * C::GB; }
  static constexpr int need(int s) {
    const int pa = s < C::NSA ? PP(s) + C::GA : T(s - C::NSA) + C::GA;
    const int pb = s < C::NPRE ? 0 : (s < C::NSB ? PP(s + 1) : T(s - C::NSB + 1));
    return cmax(pa, pb);
  }
  static constexpr int cnt(int k) { return cmin(T(k) - need(k + 1), 63); }
  static constexpr int key(int k) { return cnt(k) * 4 + (k + C::NSA < C::NK ? 2 : 0) + (k + C::NSB < C::NK ? 1 : 0); }
  static constexpr int seg_end(int k) { int e = k + 1; while (e < C::NK - 1 && key(e) == key(k)) ++e; return e; }
  unsigned ca, cb;
  template <int K> __device__ __forceinline__ void iter(int k) {
    static_assert(cnt(K) <= 63, "vmcnt");
    wait_vm<cnt(K)>();
    bar();
    if constexpr (K + C::NSA < C::NK) { issueA(k + C::NSA, ca); ca += C::ABYTES; if (ca == C::RA) ca = 0; }
    if constexpr (K + C::NSB < C::NK) { issueB(k + C::NSB, cb); cb += C::BBYTES; if (cb == C::RB) cb = 0; }
  }
  template <int K, class Hook> __device__ __forceinline__ void seg(Hook hook2) {
    if constexpr (K < C::NK - 1) {
      if constexpr (K == C::NK - C::NSB) hook2();
      constexpr int E = seg_end(K);
      if constexpr (E - K == 1) iter<K>(K);
      else {
#pragma unroll 1
        for (int k = K; k < E; ++k) iter<K>(k);
      }
      seg<E>(hook2);
    }
  }
  template <class Hook, class Hook2> __device__ __forceinline__ void run(Hook hook, Hook2 hook2) {
#pragma unroll 1
    for (int s = 0; s < C::NSB; ++s) {
      if (s == NRAMP) { wait_vm<cmin(PP(NRAMP) - need(0), 63)>(); bar(); }
      if (s < C::NSA) issueA(s, s * C::ABYTES);
      if (s >= C::NPRE) issueB(s, s * C::BBYTES);
    }
    hook();
    ca = 0; cb = 0;
    seg<0>(hook2);
  }
};

template <class C>
struct Consumer {
  LAS char* lds;
  int wid, lane, wm, wn, wk;
  unsigned rdA[2], rdB[4];

  __device__ __forceinline__ Consumer(LAS char* lds_, int wid_, int lane_) : lds(lds_), wid(wid_), lane(lane_) {
    wk = wid % C::WK; wn = (wid / C::WK) % C::WN; wm = wid / (C::WK * C::WN);
    const int fr = lane & 15, fq = lane >> 4;
    const int arow = wm * (C::BM / C::WM) + fr;
    const int ss0 = wk * C::KSW;
#pragma unroll
    for (int s = 0; s < 2; ++s) {
      const int ss = ss0 + s;
      if constexpr (C::AMODE == 0) rdA[s] = ((ss0 * 4 + fq) * C::BM + arow) * 16;
      else rdA[s] = arow * 128 + ((((ss & 1) * 4 + fq) ^ ((fr >> 1) & 7)) * 16);
    }
    if constexpr (C::BMODE <= 1) {
      const int brow = wn * (C::BN / C::WN) + fr;
#pragma unroll
      for (int s = 0; s < 2; ++s) {
        const int ss = ss0 + s;
        if constexpr (C::BMODE == 0) rdB[s] = C::RA + ((ss0 * 4 + fq) * C::BN + brow) * 16;
        else rdB[s] = C::RA + brow * 128 + ((((ss & 1) * 4 + fq) ^ ((fr >> 1) & 7)) * 16);
      }
    } else {
      const int g = lane >> 4, q = (lane >> 2) & 3, p = lane & 3;
#pragma unroll
      for (int j = 0; j < 4; ++j) {
        const int n = j;
        const int jj = (C::BMODE == 4) ? j : 2 * wn + (n & 1), mat = (C::BMODE == 4) ? 0 : (n >> 1);
        rdB[j] = C::RA + mat * (C::BK * 128) + (32 * ss0 + 8 * g + q) * 128 + ((((jj ^ (((g & 1) << 1) | (q >> 1))) << 1) | (p >> 1)) * 16) + 8 * (p & 1);
      }
    }
  }

  __device__ __forceinline__ void read(Frags<C>& f, unsigned bufa, unsigned bufb) {
    LAS char* a = lds + bufa; LAS char* b = lds + bufb;
#pragma unroll
    for (int s = 0; s < C::KSW; ++s) {
      if constexpr (C::BMODE == 0) {
#pragma unroll
        for (int n = 0; n < C::NF; ++n) f.b[s][n] = *(const LAS bf16x8*)(b + rdB[0] + s * (C::BN * 64) + n * 256);
      } else if constexpr (C::BMODE == 1) {
#pragma unroll
        for (int n = 0; n < C::NF; ++n) f.b[s][n] = *(const LAS bf16x8*)(b + rdB[s] + n * 2048);
      } else {
#pragma unroll
        for (int n = 0; n < C::NF; ++n) {
          bf16x4 lo = __builtin_amdgcn_ds_read_tr16_b64_v4i16((LAS bf16x4*)(b + rdB[n] + s * 4096));
          bf16x4 hi = __builtin_amdgcn_ds_read_tr16_b64_v4i16((LAS bf16x4*)(b + rdB[n] + s * 4096 + 512));
          f.b[s][n] = __builtin_shufflevector(lo, hi, 0, 1, 2, 3, 4, 5, 6, 7);
        }
      }
      if constexpr (C::AMODE == 0) {
#pragma unroll
        for (int m = 0; m < C::MF; ++m) f.a[s][m] = *(const LAS bf16x8*)(a + rdA[0] + s * (C::BM * 64) + m * 256);
      } else {
#pragma unroll
        for (int m = 0; m < C::MF; ++m) f.a[s][m] = *(const LAS bf16x8*)(a + rdA[s] + m * 2048);
      }
    }
  }

  __device__ __forceinline__ void mma(f32x4 (&acc)[C::MF][C::NF], const Frags<C>& f) {
#pragma unroll
    for (int s = 0; s < C::KSW; ++s)
#pragma unroll
      for (int m = 0; m < C::MF; ++m)
#pragma unroll
        for (int n = 0; n < C::NF; ++n)
          acc[m][n] = C::SWAP ? __builtin_amdgcn_mfma_f32_16x16x32_bf16(f.b[s][n], f.a[s][m], acc[m][n], 0, 0, 0)
                              : __builtin_amdgcn_mfma_f32_16x16x32_bf16(f.a[s][m], f.b[s][n], acc[m][n], 0, 0, 0);
  }
  static constexpr int NRD = C::KSW * (C::MF + (C::BMODE >= 2 ? 2 : 1) * C::NF);
  static constexpr int NMM = C::KSW * C::MF * C::NF;
  __device__ __forceinline__ void interleave() {
    constexpr int RPM = (NRD + NMM - 3) / (NMM - 2);
#pragma unroll
    for (int i = 0; i < NMM; ++i) {
      __builtin_amdgcn_sched_group_barrier(0x100, RPM, 0);
      __builtin_amdgcn_sched_group_barrier(0x008, 1, 0);
    }
  }
  unsigned na, nb;
  __device__ __forceinline__ void step(f32x4 (&acc)[C::MF][C::NF], Frags<C>& cur, Frags<C>& nxt) {
    na += C::ABYTES; if (na == C::RA) na = 0;
    nb += C::BBYTES; if (nb == C::RB) nb = 0;
    wait_lgkm0(); bar();
    __builtin_amdgcn_sched_barrier(0);
    read(nxt, na, nb);
    mma(acc, cur);
    interleave();
    __builtin_amdgcn_sched_barrier(0);
  }

  template <class Hook, class Hook2> __device__ __forceinline__ void run(f32x4 (&acc)[C::MF][C::NF], Hook hook, Hook2 hook2) {
    Frags<C> f0, f1;
#pragma unroll
    for (int m = 0; m < C::MF; ++m)
#pragma unroll
      for (int n = 0; n < C::NF; ++n) { acc[m][n] = (f32x4){0.f, 0.f, 0.f, 0.f}; asm volatile("" : "+v"(acc[m][n])); }
    bar();
    if (wid == 0) { STAMP(C::KID, 1); CSTAMP(C::KID, 8); }
    hook();
    read(f0, 0, 0);
    na = 0; nb = 0;
    if (wid == 0) STAMP(C::KID, 4);
#pragma unroll 1
    for (int k = 0; k + 2 < C::NK; k += 2) {
      step(acc, f0, f1);
      step(acc, f1, f0);
    }
    if (wid == 0) STAMP(C::KID, 5);
    if constexpr (C::NK % 2 == 0) {
      step(acc, f0, f1);
      wait_lgkm0(); bar();
      hook2();
      mma(acc, f1);
    } else {
      wait_lgkm0(); bar();
      mma(acc, f0);
    }
    if (wid == 0) { STAMP(C::KID, 2); CSTAMP(C::KID, 9); }
  }

  static constexpr bool SPLM = (C::MF % 2 == 0);
  static constexpr int RM = SPLM ? C::MF / 2 : C::MF, RN = SPLM ? C::NF : C::NF / 2;
  __device__ __forceinline__ void reduce_k(f32x4 (&acc)[C::MF][C::NF], f32x4 (&red)[RM][RN]) {
    static_assert(C::WK == 2, "");
    static_assert(LDS_X + C::NC * RM * RN * 1024 <= LDS_MISC || RM * RN == 6, "");
    LAS f32x4* x = (LAS f32x4*)(lds + LDS_X - (RM * RN == 6 ? 1024 : 0));
#pragma unroll
    for (int m = 0; m < RM; ++m)
#pragma unroll
      for (int n = 0; n < RN; ++n) {
        const f32x4 lo = acc[m][n], hi = SPLM ? acc[m + RM][n] : acc[m][n + RN];
        x[(wid * RM * RN + m * RN + n) * 64 + lane] = wk ? lo : hi;
        red[m][n] = wk ? hi : lo;
      }
    wait_lgkm0(); bar();
#pragma unroll
    for (int m = 0; m < RM; ++m)
#pragma unroll
      for (int n = 0; n < RN; ++n) red[m][n] += x[((wid ^ 1) * RM * RN + m * RN + n) * 64 + lane];
  }
};

constexpr int FLE = 0;
constexpr int FL0 = 32;
constexpr int FL1 = FL0 + 2048;
constexpr int FL2 = FL1 + 16 * 16;
constexpr int FL3 = FL2 + 8 * 32;
constexpr int FLW = FL3 + 8 * 32;
constexpr int FLC = FLW + 48;

__device__ __forceinline__ void publish(LAS unsigned* arr, unsigned nwaves, u64* flag, u64 tag, int lane, int kid) {
  if (threadIdx.x < 64) STAMP(kid, 7);
  asm volatile("" ::: "memory");
  if (lane == 0) __hip_atomic_fetch_add(arr + 8, 1u, RLX_WG);
  wait_vm<0>();
  if (lane == 0) {
    const unsigned old = __hip_atomic_fetch_add(arr, 1u, RLX_WG);
    if (old == nwaves - 1) {
      __hip_atomic_store(flag, tag, RLX_AGENT);
      STAMP(kid, 6);
    }
  }
}
__device__ __forceinline__ void lds_wait(LAS unsigned* w, unsigned want) {
  const unsigned a = (unsigned)(size_t)w;
#pragma unroll 1
  for (unsigned i = 0; i < (1u << 24); ++i) {
    unsigned v;
    asm volatile("ds_read_b32 %0, %1\n\ts_waitcnt lgkmcnt(0)" : "=v"(v) : "v"(a) : "memory");
    if (__builtin_amdgcn_readfirstlane(v) >= want) break;
    __builtin_amdgcn_s_sleep(1);
  }
}
template <int SLEEP, int NW>
__device__ __forceinline__ void await(const u64* word, bool need, u64 tag, int lw, LAS unsigned* go, unsigned epoch) {
  if (lw == 0) {
    const u64 t0 = wall_clock64();
    for (;;) {
      bool ok = true;
#pragma unroll
      for (int i = 0; i < NW; ++i) ok &= __hip_atomic_load(word + i * 64, RLX_AGENT) == tag;
      if (__all(!need || ok)) break;
      if (wall_clock64() - t0 > 2000000ull) break;
      if (SLEEP > PSLEEP && __any(need && ok)) __builtin_amdgcn_s_sleep(PSLEEP); else __builtin_amdgcn_s_sleep(SLEEP);
    }
    __hip_atomic_store(go, epoch, RLX_WG);
  } else {
    lds_wait(go, epoch);
  }
  __builtin_amdgcn_fence(__ATOMIC_ACQUIRE, "wavefront");
}
#define P1_PRE 1
#define PL1 1
#define PL2 1
#define PL3 1

#define PRE1 2
#define PRE2 6
#define PRE3 5
#define PRE4 6
using C1 = Cfg<1, 80, 128, 64, 1, 4, 1, 4, 6, 32, 0, 1, 0, PRE1>;
#define P2A 4
#define P2B 6
using C2 = Cfg<2, 64, 80, 64, 2, 1, 2, P2A, P2B, 20, 0, 1, 1, PRE2>;
#define P3A 4
#define P3B 5
using C3 = Cfg<3, 256, 128, 32, 2, 2, 1, P3A, P3B, 20, 0, 3, 1, PRE3>;
#define P4A 4
#define P4B 6
using C4 = Cfg<4, 64, 80, 64, 2, 1, 2, P4A, P4B, 27, 0, 0, 1, PRE4>;
#define ROT 15
struct Src1 {
  const char* a; const char* b; int o, c;
  static constexpr int ldA = 0, ldB = S_ * 2, AUXA = AUXH, AUXB = 0, rows = 80;
  __device__ __forceinline__ int kt(int k) const { const int s = (k + o) & 31; return (ROT & 1) ? ((s & 24) | ((s + c) & 7)) : k; }
  __device__ __forceinline__ const char* baseA(int k) const { return a + kt(k) * (80 * 128); }
  __device__ __forceinline__ const char* baseB(int k, int) const { return b + kt(k) * 128; }
};
struct Src2 {
  const char* a; const char* bre; const char* bim; int rot;
  static constexpr int ldA = 0, ldB = D_ * 2, AUXA = AUXH, AUXB = 0, rows = 64;
  __device__ __forceinline__ int kt(int k) const { const int s = k + ((ROT & 2) ? rot : 0); return s >= 20 ? s - 20 : s; }
  __device__ __forceinline__ const char* baseA(int k) const { return a + kt(k) * (64 * 128); }
  __device__ __forceinline__ const char* baseB(int k, int) const { const int t = kt(k); return t < 10 ? bre + t * 128 : bim + (t - 10) * 128; }
};
struct Src3 {
  const char* a; const char* bg; const char* bu; int rot;
  static constexpr int ldA = 0, ldB = F_ * 2, AUXA = AUXH, AUXB = 0, rows = 256;
  __device__ __forceinline__ int kt(int k) const { const int s = k + ((ROT & 4) ? rot : 0); return s >= 20 ? s - 20 : s; }
  __device__ __forceinline__ const char* baseA(int k) const { return a + kt(k) * (256 * 64); }
  __device__ __forceinline__ const char* baseB(int k, int up) const { return (up ? bu : bg) + kt(k) * (32 * F_ * 2); }
};
struct Src4 {
  const char* a; const char* b;
  static constexpr int ldA = 0, ldB = 0, AUXA = AUXH, AUXB = AUXH, rows = 64;
  __device__ __forceinline__ const char* baseA(int k) const { return a + k * (64 * 128); }
  __device__ __forceinline__ const char* baseB(int k, int) const { return b + k * (80 * 128); }
};

struct Params {
  const u16* x; const u16* sre; const u16* sim; const u16* hre; const u16* him; const u16* wg; const u16* wu; const u16* wd;
  u16* xnI; u16* pqI; u16* hI; u16* hhI; u16* wdI; f32x2* stats; float* cs; u64* flags; u16* out;
};

template <int CTRL, int RMASK> __device__ __forceinline__ float dpp_add(float v) {
  return v + __int_as_float(__builtin_amdgcn_update_dpp(0, __float_as_int(v), CTRL, RMASK, 0xf, true));
}
__device__ __forceinline__ float wave_sum(float v) {
  v = dpp_add<0x111, 0xf>(v); v = dpp_add<0x112, 0xf>(v); v = dpp_add<0x114, 0xf>(v); v = dpp_add<0x118, 0xf>(v);
  v = dpp_add<0x142, 0xa>(v); v = dpp_add<0x143, 0xc>(v);
  return __int_as_float(__builtin_amdgcn_readlane(__float_as_int(v), 63));
}
__device__ __forceinline__ float sum_rows(float v) {
  const auto a = __builtin_amdgcn_permlane16_swap(__float_as_uint(v), __float_as_uint(v), false, false);
  v = __uint_as_float(a[0]) + __uint_as_float(a[1]);
  const auto b = __builtin_amdgcn_permlane32_swap(__float_as_uint(v), __float_as_uint(v), false, false);
  return __uint_as_float(b[0]) + __uint_as_float(b[1]);
}
__device__ __forceinline__ u32x4 col8(LAS char* tile, int cb, int lane) {
  LAS char* a = tile + ((lane >> 2) & 3) * 1296 + (cb * 64 + (lane >> 4) * 16 + (lane & 3) * 4) * 2;
  const bf16x4 lo = __builtin_amdgcn_ds_read_tr16_b64_v4i16((LAS bf16x4*)a);
  const bf16x4 hi = __builtin_amdgcn_ds_read_tr16_b64_v4i16((LAS bf16x4*)(a + 4 * 1296));
  return __builtin_bit_cast(u32x4, __builtin_shufflevector(lo, hi, 0, 1, 2, 3, 4, 5, 6, 7));
}
template <int N> __device__ __forceinline__ void bars() {
#pragma unroll 1
  for (int i = 0; i < N; ++i) bar();
}

__global__ __launch_bounds__(NWAVES * 64) void mega(const Params p) {
  __shared__ __attribute__((aligned(1024))) char smem_[LDS_TOTAL];
  LAS char* lds = (LAS char*)smem_;
  const int wid = __builtin_amdgcn_readfirstlane(threadIdx.x >> 6), lane = threadIdx.x & 63;
  const int bid = blockIdx.x, g = bid & 7, idx = bid >> 3;
  LAS unsigned* arr = (LAS unsigned*)(lds + LDS_MISC + 256);
  if (wid == 0) STAMP(0, 0);
  const u16* xr = p.x + (bid * 8 + (wid & 7)) * D_;
  const bf16x8 v0 = *(const bf16x8*)(xr + lane * 8);
  const bf16x8 v1 = *(const bf16x8*)(xr + 512 + (lane & 15) * 8);
  const u64 ep = __hip_atomic_load(p.flags + FLE, RLX_AGENT);
  if (threadIdx.x < 16) arr[threadIdx.x] = 0u;
  wait_lgkm0(); bar();
  const int j1 = idx & 7, mq = idx >> 3, isQ = mq >> 1, t1 = g * 256 + (mq & 1) * 128;
  const int j2 = idx & 7, mt2 = g * 4 + (idx >> 3);
  const int mt3 = (g & 6) + (idx & 1), j3 = (g & 1) * 14 + (idx >> 1);
  const bool on3 = idx < 28 && j3 < 27;
  LAS f32x4* rs = (LAS f32x4*)(lds + LDS_X);
  LAS float* csl = (LAS float*)(lds + LDS_X + 4096);

  {
    float f[16];
#pragma unroll
    for (int i = 0; i < 8; ++i) { f[i] = bf2f((u16)v0[i]); f[8 + i] = bf2f((u16)v1[i]); }
    const float piv = __int_as_float(__builtin_amdgcn_readfirstlane(__float_as_int(f[0])));
    asm volatile("" :: "v"(f[8]) : "memory");
    if (lane == 0) __hip_atomic_fetch_add(arr + 4, 1u, RLX_WG);
    if (wid == 0) STAMP(0, 4);
    float s0 = 0.f, s1 = 0.f, q0 = 0.f, q1 = 0.f;
#pragma unroll
    for (int i = 0; i < 8; ++i) { const float d = f[i] - piv, e = f[8 + i] - piv; s0 += d; q0 += d * d; s1 += e; q1 += e * e; }
    const float sd = wave_sum(s0 + (lane < 16 ? s1 : 0.f)) * (1.0f / D_);
    const float qd = wave_sum(q0 + (lane < 16 ? q1 : 0.f)) * (1.0f / D_);
    const float mb = rbf(piv + sd), vb = rbf(fmaxf(qd - sd * sd, 0.f));
    const float rden = __builtin_amdgcn_rcpf(rbf(__builtin_amdgcn_sqrtf(rbf(vb + 1e-5f))));
    u32x4 w0, w1;
#pragma unroll
    for (int i = 0; i < 4; ++i) {
      w0[i] = pk2(rbf(f[2 * i] - mb) * rden, rbf(f[2 * i + 1] - mb) * rden);
      w1[i] = pk2(rbf(f[8 + 2 * i] - mb) * rden, rbf(f[9 + 2 * i] - mb) * rden);
    }
    LAS char* tb = lds + LDS_X;
    *(LAS u32x4*)(tb + wid * 1296 + lane * 16) = w0;
    if (lane < 16) *(LAS u32x4*)(tb + wid * 1296 + 1024 + lane * 16) = w1;
    wait_lgkm0();
    if (lane == 0) __hip_atomic_fetch_add(arr + 7, 1u, RLX_WG);
  }
  const u64 tag = (((u64)(unsigned)__builtin_amdgcn_readfirstlane((unsigned)(ep >> 32)) << 32) | (unsigned)__builtin_amdgcn_readfirstlane((unsigned)ep)) + 1ull;
  if (wid >= 6) {
    const int lw = wid - 6;
    {
      asm volatile("" :: "s"(tag) : "memory");
      if (lane == 0) __hip_atomic_fetch_add(arr + 5, 1u, RLX_WG);
      Src1 s{(const char*)(p.xnI + (size_t)j1 * 256 * 80 * 8), (const char*)((isQ ? p.sim : p.sre) + (size_t)t1 * S_), (g + 2 * mq) & 7, j1};
      Loader<C1, Src1> L(lds, s, lw, lane);
      lds_wait(arr + 4, 8);
      L.preloadB();
      lds_wait(arr + 5, 8);
      await<PSLEEP, 4>(p.flags + FL0 + g * 256 + lane, true, tag, lw, arr + 3, 1u);
      if (bid == 0 && lw == 0 && lane == 0) __hip_atomic_store(p.flags + FLE, tag, RLX_AGENT);
      if (lw == 0) STAMP(1, 0);
      wait_vm<0>();
      L.run([] {}, [] {});
      bar();
    }
    {
      Src2 s{(const char*)(p.pqI + (size_t)mt2 * 160 * 64 * 8), (const char*)(p.hre + (size_t)j2 * 80 * D_), (const char*)(p.him + (size_t)j2 * 80 * D_), (mt2 * 5) >> 3};
      Loader<C2, Src2> L(lds, s, lw, lane);
      if (PL1 == 1) lds_wait(arr + 0, C1::NC);
      if (PL1 == 2) lds_wait(arr + 8, C1::NC);
      L.preloadB();
      lds_wait(arr + 0, C1::NC);
      await<PSLEEP, 1>(p.flags + FL1 + (mt2 >> 1) * 16 + (lane & 15), true, tag, lw, arr + 3, 2u);
      if (lw == 0) STAMP(2, 0);
      wait_vm<0>();
      L.run([] {}, [] {});
      bar();
    }
    if (on3) {
      Src3 s{(const char*)(p.hI + (size_t)mt3 * 80 * 256 * 8), (const char*)(p.wg + j3 * 64), (const char*)(p.wu + j3 * 64), (mt3 * 5) >> 1};
      Loader<C3, Src3> L(lds, s, lw, lane);
      bar();
      if (PL2 == 1) lds_wait(arr + 1, C2::NC);
      if (PL2 == 2) lds_wait(arr + 9, C2::NC);
      L.preloadB();
      lds_wait(arr + 1, C2::NC);
      await<PSLEEP, 1>(p.flags + FL2 + mt3 * 32 + (lane & 31), true, tag, lw, arr + 3, 3u);
      if (lw == 0) STAMP(3, 0);
      wait_vm<0>();
      L.run([] {}, [] {});
      bar();
      await<PSLEEP, 1>(p.flags + FLW + min(lane, 39), true, tag, lw, arr + 3, 4u);
    } else {
      bar();
    }
    {
      Src4 s{(const char*)(p.hhI + (size_t)mt2 * 216 * 64 * 8), (const char*)(p.wdI + (size_t)j2 * 216 * 80 * 8)};
      Loader<C4, Src4> L(lds, s, lw, lane);
      if (on3) {
        if (PL3 == 1) lds_wait(arr + 2, C3::NC);
        if (PL3 == 2) lds_wait(arr + 10, C3::NC);
        if (PL3 == 3) lds_wait(arr + 10, C3::NC / 2);
        L.preloadB();
        lds_wait(arr + 2, C3::NC);
        await<PSLEEP, 1>(p.flags + FL3 + g * 32 + (lane & 31), (lane & 31) < 27, tag, lw, arr + 3, 5u);
      } else {
        await<ISLEEP, 1>(p.flags + FLW + min(lane, 39), true, tag, lw, arr + 3, 4u);
        L.preloadB();
        await<ISLEEP, 1>(p.flags + FL3 + g * 32 + (lane & 31), (lane & 31) < 27, tag, lw, arr + 3, 5u);
      }
      if (lw == 0) STAMP(4, 0);
      wait_vm<0>();
      L.run([] {}, [] {});
      bar();
      bar();
    }
    return;
  }

#define FRESH_LANE int lane_ = threadIdx.x & 63; asm volatile("" : "+v"(lane_)); const int lane = lane_, fr = lane & 15, fq = lane >> 4; (void)fr; (void)fq;
  {
    LAS char* tb = lds + LDS_X;
    lds_wait(arr + 7, 8);
    auto rsrc = __builtin_amdgcn_make_buffer_rsrc(p.xnI, 0, S_ * D_ * 2, 0x00020000);
#pragma unroll
    for (int h = 0; h < 2; ++h) {
      const int cb = h * 6 + wid;
      if (cb < 10) {
        const int d = cb * 64 + lane, j = d / 80, n = d % 80;
        __builtin_amdgcn_raw_buffer_store_b128(col8(tb, cb, lane), rsrc, ((j * 256 + bid) * 80 + n) * 16, 0, 16);
      }
    }
    if (wid == 0) STAMP(0, 7);
    wait_vm<0>();
    lds_wait(arr + 5, 2);
    unsigned old = 0;
    if (lane == 0) old = __hip_atomic_fetch_add(arr + 5, 1u, RLX_WG);
    if (__builtin_amdgcn_readfirstlane(old) == 7u && lane < 8) __hip_atomic_store(p.flags + FL0 + lane * 256 + bid, tag, RLX_AGENT);
    if (wid == 0) STAMP(0, 3);
    if (wid >= 4) {
      if (!on3) return;
      bars<C1::NBAR + C2::NBAR + 10>();
      const int aw = wid - 4;
      const u64 fc = __hip_atomic_load(p.flags + FLC + j3 * 16, RLX_AGENT);
      bars<2>();
      if (!__all(fc == tag)) await<PSLEEP, 1>(p.flags + FLC + j3 * 16, true, tag, 0, arr + 11, 1u);
      __builtin_amdgcn_fence(__ATOMIC_ACQUIRE, "wavefront");
      const float cv = __hip_atomic_load(p.cs + j3 * 128 + aw * 64 + lane, RLX_AGENT);
      f32x2 pv[2][8];
#pragma unroll
      for (int h = 0; h < 2; ++h)
#pragma unroll
        for (int i = 0; i < 8; ++i) pv[h][i] = __builtin_bit_cast(f32x2, ld8(p.stats + i * S_ + mt3 * 256 + aw * 128 + h * 64 + lane));
      bars<3>();
#pragma unroll
      for (int h = 0; h < 2; ++h) {
        float mean = 0.f, q = 0.f, dd = 0.f;
#pragma unroll
        for (int i = 0; i < 8; ++i) { mean += pv[h][i][0]; q += pv[h][i][1]; }
        mean *= 0.125f;
#pragma unroll
        for (int i = 0; i < 8; ++i) { const float d = pv[h][i][0] - mean; dd += d * d; }
        q += 80.0f * dd;
        const float den = rbf(__builtin_amdgcn_sqrtf(rbf(rbf(q * (1.0f / D_)) + 1e-5f)));
        rs[aw * 128 + h * 64 + lane] = (f32x4){-rbf(mean), den, -1.44269504089f * __builtin_amdgcn_rcpf(den), 0.f};
      }
      csl[aw * 64 + lane] = cv;
      wait_lgkm0();
      return;
    }
  }
  {
    FRESH_LANE
    Consumer<C1> L(lds, wid, lane);
    f32x4 acc[C1::MF][C1::NF];
    L.run(acc, [] {}, [] {});
    const float sg = isQ ? -1.f : 1.f;
#pragma unroll
    for (int n = 0; n < 2; ++n) {
      const int t = t1 + L.wn * 32 + n * 16 + fr, mt = t >> 6, r = t & 63;
#pragma unroll
      for (int m = 0; m < 5; ++m) {
        const int koct = isQ * 80 + j1 * 10 + m * 2 + (fq >> 1);
        u32x2 w; w[0] = pk2(sg * acc[m][n][0], sg * acc[m][n][1]); w[1] = pk2(sg * acc[m][n][2], sg * acc[m][n][3]);
        st8(p.pqI + (((size_t)mt * 160 + koct) * 64 + r) * 8 + (fq & 1) * 4, w);
      }
    }
    publish(arr + 0, C1::NC, p.flags + FL1 + (t1 >> 7) * 16 + isQ * 8 + j1, tag, lane, 1);
    if (wid == 0) STAMP(1, 3);
  }
  {
    FRESH_LANE
    Consumer<C2> L(lds, wid, lane);
    f32x4 acc[C2::MF][C2::NF];
    const int t = mt2 * 64 + L.wm * 32 + L.wk * 16 + fr;
    bf16x4 xv[C2::NF];
#pragma unroll
    for (int n = 0; n < C2::NF; ++n) xv[n] = *(const bf16x4*)(p.x + (size_t)t * D_ + j2 * 80 + n * 16 + fq * 4);
#pragma unroll
    for (int n = 0; n < C2::NF; ++n) asm volatile("" : "+v"(xv[n]));
    L.run(acc, [] {}, [] {});
    f32x4 red[1][5];
    L.reduce_k(acc, red);
    float hv[C2::NF][4];
    float s = 0.f;
#pragma unroll
    for (int n = 0; n < C2::NF; ++n) {
      u32x2 w;
      w[0] = pk2(red[0][n][0] + bf2f((u16)xv[n][0]), red[0][n][1] + bf2f((u16)xv[n][1]));
      w[1] = pk2(red[0][n][2] + bf2f((u16)xv[n][2]), red[0][n][3] + bf2f((u16)xv[n][3]));
      hv[n][0] = __uint_as_float(w[0] << 16); hv[n][1] = __uint_as_float(w[0] & 0xffff0000u);
      hv[n][2] = __uint_as_float(w[1] << 16); hv[n][3] = __uint_as_float(w[1] & 0xffff0000u);
      s += (hv[n][0] + hv[n][1]) + (hv[n][2] + hv[n][3]);
      const int koct = j2 * 10 + n * 2 + (fq >> 1);
      st8(p.hI + (((size_t)(t >> 8) * 80 + koct) * 256 + (t & 255)) * 8 + (fq & 1) * 4, w);
    }
    const float mean = sum_rows(s) * (1.0f / 80.0f);
    float q = 0.f;
#pragma unroll
    for (int n = 0; n < C2::NF; ++n)
#pragma unroll
      for (int r = 0; r < 4; ++r) { const float d = hv[n][r] - mean; q += d * d; }
    q = sum_rows(q);
    if (fq == 0) st8(p.stats + j2 * S_ + t, __builtin_bit_cast(u32x2, (f32x2){mean, q}));
    publish(arr + 1, C2::NC, p.flags + FL2 + g * 32 + idx, tag, lane, 2);
    if (wid == 0) STAMP(2, 3);
  }
  if (on3) {
    FRESH_LANE
    Consumer<C3> L(lds, wid, lane);
    f32x4 acc[C3::MF][C3::NF];
    f32x4 st[C3::MF], cg[2], cu[2];
    L.run(acc, [] {}, [&] {
#pragma unroll
      for (int m = 0; m < C3::MF; ++m) st[m] = rs[L.wm * 128 + m * 16 + fr];
#pragma unroll
      for (int i = 0; i < 2; ++i) {
        const int fl = L.wn * 32 + i * 16 + fq * 4;
        cg[i] = *(const LAS f32x4*)(csl + fl); cu[i] = *(const LAS f32x4*)(csl + 64 + fl);
      }
    });
    u16* hb = p.hhI + (((size_t)(mt3 * 4 + L.wm * 2) * 216 + j3 * 8 + L.wn * 4 + (fq >> 1)) * 64 + fr) * 8 + (fq & 1) * 4;
#pragma unroll
    for (int m = 0; m < C3::MF; ++m)
#pragma unroll
      for (int i = 0; i < 2; ++i) {
        const f32x4 s0 = {st[m][0], st[m][0], st[m][0], st[m][0]}, s1 = {st[m][1], st[m][1], st[m][1], st[m][1]};
        const f32x4 x = __builtin_elementwise_fma(cg[i], s0, acc[m][i]) * st[m][2];
        f32x4 e;
#pragma unroll
        for (int r = 0; r < 4; ++r) e[r] = __builtin_amdgcn_exp2f(x[r]);
        const f32x4 d = __builtin_elementwise_fma(e, s1, s1);
        f32x4 rc;
#pragma unroll
        for (int r = 0; r < 4; ++r) rc[r] = __builtin_amdgcn_rcpf(d[r]);
        const f32x4 o = __builtin_elementwise_fma(cu[i], s0, acc[m][i + 2]) * rc;
        u32x2 w; w[0] = pk2(o[0], o[1]); w[1] = pk2(o[2], o[3]);
        st8(hb + (m >> 2) * (216 * 64 * 8) + i * (2 * 64 * 8) + (m & 3) * 128, w);
      }
    publish(arr + 2, C3::NC, p.flags + FL3 + mt3 * 32 + j3, tag, lane, 3);
    if (wid == 0) STAMP(3, 3);
  } else {
    FRESH_LANE
    const int id = idx >= 28 ? g * 4 + idx - 28 : 32 + (g >> 1) * 2 + idx - 26;
    auto rsrc = __builtin_amdgcn_make_buffer_rsrc(p.wdI, 0, F_ * D_ * 2, 0x00020000);
    lds_wait(arr + 1, C2::NC);
    if (id < 27) {
      const u16* w = ((wid & 2) ? p.wu : p.wg) + (size_t)((wid & 1) * 320 + (lane >> 3)) * F_ + id * 64 + (lane & 7) * 8;
      f32x2 a[4] = {{0.f, 0.f}, {0.f, 0.f}, {0.f, 0.f}, {0.f, 0.f}};
#pragma unroll 1
      for (int b = 0; b < 2; ++b) {
        u32x4 v[20];
#pragma unroll
        for (int i = 0; i < 20; ++i) v[i] = *(const u32x4*)(w + (size_t)(b * 20 + i) * 8 * F_);
#pragma unroll
        for (int i = 0; i < 20; ++i)
#pragma unroll
          for (int e = 0; e < 4; ++e) a[e] += (f32x2){__uint_as_float(v[i][e] << 16), __uint_as_float(v[i][e] & 0xffff0000u)};
      }
      LAS float* sf = (LAS float*)(lds + LDS_X);
#pragma unroll
      for (int e = 0; e < 4; ++e)
#pragma unroll
        for (int c = 0; c < 2; ++c) { float t = sum_rows(a[e][c]); t += __shfl_xor(t, 8); a[e][c] = t; }
      if (lane < 8) {
        *(LAS f32x4*)(sf + wid * 64 + lane * 8) = (f32x4){a[0][0], a[0][1], a[1][0], a[1][1]};
        *(LAS f32x4*)(sf + wid * 64 + lane * 8 + 4) = (f32x4){a[2][0], a[2][1], a[3][0], a[3][1]};
      }
      wait_lgkm0();
      unsigned old = 0;
      if (lane == 0) old = __hip_atomic_fetch_add(arr + 12, 1u, RLX_WG);
      if (__builtin_amdgcn_readfirstlane(old) == 3u) {
        __hip_atomic_store(p.cs + id * 128 + lane, sf[lane] + sf[64 + lane], RLX_AGENT);
        __hip_atomic_store(p.cs + id * 128 + 64 + lane, sf[128 + lane] + sf[192 + lane], RLX_AGENT);
        wait_vm<0>();
        if (lane == 0) __hip_atomic_store(p.flags + FLC + id * 16, tag, RLX_AGENT);
      }
    }
    const int k0 = id < 27 ? id * 4 : 108 + (id - 27) * 9, nk = id < 27 ? 4 : min(9, 216 - k0);
#pragma unroll 1
    for (int i0 = 0; i0 < nk; i0 += 3) {
      bf16x8 u0[6], u1[6];
#pragma unroll
      for (int i = 0; i < 6; ++i) {
        const int koct = k0 + min(i0 + (i >> 1), nk - 1);
        const u16* wr = p.wd + (size_t)(koct * 8 + (i & 1) * 4 + wid) * D_;
        u0[i] = *(const bf16x8*)(wr + lane * 8);
        u1[i] = *(const bf16x8*)(wr + 512 + (lane & 15) * 8);
      }
#pragma unroll
      for (int i = 0; i < 6; ++i) {
        LAS char* tb = lds + (i0 + (i >> 1)) * 10368 + ((i & 1) * 4 + wid) * 1296;
        *(LAS bf16x8*)(tb + lane * 16) = u0[i];
        if (lane < 16) *(LAS bf16x8*)(tb + 1024 + lane * 16) = u1[i];
      }
    }
    wait_lgkm0();
    if (lane == 0) __hip_atomic_fetch_add(arr + 6, 1u, RLX_WG);
    lds_wait(arr + 6, 4);
#pragma unroll 1
    for (int i = 0; i < nk; ++i) {
#pragma unroll
      for (int h = 0; h < 3; ++h) {
        const int cb = h * 4 + wid;
        if (cb < 10) {
          const int d = cb * 64 + lane, j = d / 80, n = d % 80;
          __builtin_amdgcn_raw_buffer_store_b128(col8(lds + i * 10368, cb, lane), rsrc, ((j * 216 + k0 + i) * 80 + n) * 16, 0, 16);
        }
      }
    }
    publish(arr + 2, 4, p.flags + FLW + id, tag, lane, 0);
  }
  {
    FRESH_LANE
    Consumer<C4> L(lds, wid, lane);
    f32x4 acc[C4::MF][C4::NF];
    const int t = mt2 * 64 + L.wm * 32 + L.wk * 16 + fr;
    u32x2 hv[5];
    L.run(acc, [&] {
#pragma unroll
      for (int n = 0; n < 5; ++n)
        hv[n] = ld8(p.hI + (((size_t)(t >> 8) * 80 + j2 * 10 + n * 2 + (fq >> 1)) * 256 + (t & 255)) * 8 + (fq & 1) * 4);
    }, [] {});
    f32x4 red[1][5];
    L.reduce_k(acc, red);
#pragma unroll
    for (int n = 0; n < 5; ++n) {
      u32x2 w;
      w[0] = pk2(red[0][n][0] + __uint_as_float(hv[n][0] << 16), red[0][n][1] + __uint_as_float(hv[n][0] & 0xffff0000u));
      w[1] = pk2(red[0][n][2] + __uint_as_float(hv[n][1] << 16), red[0][n][3] + __uint_as_float(hv[n][1] & 0xffff0000u));
      *(u32x2*)(p.out + (size_t)t * D_ + j2 * 80 + n * 16 + fq * 4) = w;
    }
    if (wid == 0) { wait_vm<0>(); STAMP(4, 3); }
  }
}

extern "C" void kernel_launch(void* const* d_in, const int* in_sizes, int n_in, void* d_out, int out_size,
                              void* d_ws, size_t ws_size, hipStream_t stream) {
  char* ws = (char*)d_ws;
  Params p;
  p.x = (const u16*)d_in[0]; p.sre = (const u16*)d_in[1]; p.sim = (const u16*)d_in[2];
  p.hre = (const u16*)d_in[3]; p.him = (const u16*)d_in[4];
  p.wg = (const u16*)d_in[6]; p.wu = (const u16*)d_in[7]; p.wd = (const u16*)d_in[8];
  p.flags = (u64*)ws;
  p.cs = (float*)(ws + 65536);
  p.stats = (f32x2*)(ws + 131072);
  p.xnI = (u16*)(ws + (1u << 20));
  p.pqI = (u16*)(ws + (4u << 20));
  p.hI = (u16*)(ws + (10u << 20));
  p.hhI = (u16*)(ws + (13u << 20));
  p.wdI = (u16*)(ws + (21u << 20));
  p.out = (u16*)d_out;
  mega<<<256, NWAVES * 64, 0, stream>>>(p);
}
```

```cpp
#include <hip/hip_runtime.h>
#include <hip/hip_bf16.h>
#include <type_traits>
#define PSLEEP 4
#define ISLEEP 16
#define NRAMP 3
#define TW 2
#define AUXH 16

typedef unsigned short u16;
typedef unsigned long long u64;
typedef short bf16x8 __attribute__((ext_vector_type(8)));
typedef short bf16x4 __attribute__((ext_vector_type(4)));
typedef float f32x4 __attribute__((ext_vector_type(4)));
typedef float f32x2 __attribute__((ext_vector_type(2)));
typedef unsigned u32x2 __attribute__((ext_vector_type(2)));
typedef unsigned u32x4 __attribute__((ext_vector_type(4)));
#define LAS __attribute__((address_space(3)))
#define GAS __attribute__((address_space(1)))
#define RLX_AGENT __ATOMIC_RELAXED, __HIP_MEMORY_SCOPE_AGENT
#define RLX_WG __ATOMIC_RELAXED, __HIP_MEMORY_SCOPE_WORKGROUP

constexpr int S_ = 2048, D_ = 640, F_ = 1728;

__device__ __forceinline__ float bf2f(u16 v) { return __uint_as_float(((unsigned)v) << 16); }
typedef __bf16 bf16x2n __attribute__((ext_vector_type(2)));
__device__ __forceinline__ unsigned pk2(float lo, float hi) {
  return __builtin_bit_cast(unsigned, __builtin_convertvector((f32x2){lo, hi}, bf16x2n));
}
__device__ __forceinline__ float rbf(float v) { return __uint_as_float(pk2(v, v) << 16); }

#define CSTAMP(kid, i)
#define STAMP(kid, i)
template <int N> __device__ __forceinline__ void wait_vm() { asm volatile("s_waitcnt vmcnt(%0)" ::"n"(N) : "memory"); }
__device__ __forceinline__ void wait_lgkm0() { asm volatile("" ::: "memory"); __builtin_amdgcn_s_waitcnt(0xC07F); asm volatile("" ::: "memory"); }
__device__ __forceinline__ void bar() { asm volatile("" ::: "memory"); __builtin_amdgcn_s_barrier(); asm volatile("" ::: "memory"); }
template <int N, int STEP> __device__ __forceinline__ void wait_dyn(int n) {
  if constexpr (N <= 0) wait_vm<0>();
  else { if (n >= N) wait_vm<N>(); else wait_dyn<N - STEP, STEP>(n); }
}

template <int AUX> __device__ __forceinline__ void glds16(const char* g, LAS char* l) {
  __builtin_amdgcn_global_load_lds((const GAS void*)g, (LAS void*)l, 16, 0, AUX);
}
template <int AUX> __device__ __forceinline__ void touch(const char* g, LAS char* scratch) {
  __builtin_amdgcn_global_load_lds((const GAS void*)g, (LAS void*)scratch, 4, 0, AUX);
}
__device__ __forceinline__ void st8(void* p, u32x2 v) { __hip_atomic_store((u64*)p, __builtin_bit_cast(u64, v), RLX_AGENT); }
__device__ __forceinline__ u32x2 ld8(const void* p) { return __builtin_bit_cast(u32x2, __hip_atomic_load((u64*)p, RLX_AGENT)); }

constexpr int NL = 2;
constexpr int NWAVES = 8;
constexpr int LDS_RING = 136 * 1024;
constexpr int LDS_X = LDS_RING;
constexpr int LDS_TOTAL = 160 * 1024;
constexpr int LDS_MISC = LDS_TOTAL - 512;

template <int KID_, int BM_, int BN_, int BK_, int WM_, int WN_, int WK_, int NSA_, int NSB_, int NK_, int AMODE_, int BMODE_, int SWAP_, int NPRE_>
struct Cfg {
  static constexpr int KID = KID_, BM = BM_, BN = BN_, BK = BK_, WM = WM_, WN = WN_, WK = WK_, NSA = NSA_, NSB = NSB_, NK = NK_;
  static constexpr int AMODE = AMODE_, BMODE = BMODE_, SWAP = SWAP_, NPRE = NPRE_;
  static constexpr int NC = WM * WN * WK;
  static constexpr int MF = BM / WM / 16, NF = BN / WN / 16, KSW = BK / 32 / WK;
  static constexpr int ABYTES = BM * BK * 2, BBYTES = BN * BK * 2;
  static constexpr int PA = ABYTES / 1024, PB = BBYTES / 1024;
  static constexpr int GA = PA / NL, GB = PB / NL, G = GA + GB;
  static constexpr int RA = NSA * ABYTES, RB = NSB * BBYTES;
  static constexpr int NBAR = NK + (WK == 2 ? 2 : 1);
  static_assert(PA % NL == 0 && PB % NL == 0, "pieces");
  static_assert(RA + RB <= LDS_RING, "lds");
  static_assert(NSA <= NK && NSB <= NK && NSB >= NSA, "stages");
  static_assert(KSW >= 1 && KSW <= 2, "ksw");
};

template <class C> struct Frags { bf16x8 a[C::KSW][C::MF]; bf16x8 b[C::KSW][C::NF]; };

template <class C, class Src>
struct Loader {
  LAS char* lds; const Src& src;
  int lw, lane;
  unsigned voffA[C::GA], voffB[C::GB];
  static __device__ __forceinline__ unsigned rowmajor(int p, int ld) {
    const int row = p >> 3, c = (p & 7) ^ ((row >> 1) & 7);
    return row * ld + c * 16;
  }
  __device__ __forceinline__ Loader(LAS char* lds_, const Src& s, int lw_, int lane_) : lds(lds_), src(s), lw(lw_), lane(lane_) {
#pragma unroll
    for (int i = 0; i < C::GA; ++i) {
      const int p = (i * NL + lw) * 64 + lane;
      if constexpr (C::AMODE == 0) { const int r = p % C::BM; voffA[i] = (p - r + (r < src.rows ? r : (r & 31))) * 16; }
      else voffA[i] = rowmajor(p, Src::ldA);
    }
#pragma unroll
    for (int i = 0; i < C::GB; ++i) {
      const int p = (i * NL + lw) * 64 + lane;
      if constexpr (C::BMODE == 0) voffB[i] = p * 16;
      else if constexpr (C::BMODE == 1) voffB[i] = rowmajor(p, Src::ldB);
      else { const int q = p % (C::BK * 8), k = q >> 3, c = (q & 7) ^ ((((k >> 3) & 1) << 2) | (((k >> 1) & 1) << 1)); voffB[i] = k * Src::ldB + c * 16; }
    }
  }
  __device__ __forceinline__ void issueA(int k, unsigned buf) {
    const char* ga = src.baseA(k);
    LAS char* la = lds + buf + lw * 1024;
#pragma unroll
    for (int i = 0; i < C::GA; ++i) glds16<Src::AUXA>(ga + voffA[i], la + i * NL * 1024);
  }
  __device__ __forceinline__ void issueB(int k, unsigned buf) {
    LAS char* lb = lds + C::RA + buf + lw * 1024;
#pragma unroll
    for (int i = 0; i < C::GB; ++i) glds16<Src::AUXB>(src.baseB(k, i >= C::GB / 2) + voffB[i], lb + i * NL * 1024);
  }
  __device__ __forceinline__ void preloadB() {
#pragma unroll 1
    for (int s = 0; s < C::NPRE; ++s) issueB(s, s * C::BBYTES);
  }
  static constexpr int cmin(int a, int b) { return a < b ? a : b; }
  static constexpr int cmax(int a, int b) { return a > b ? a : b; }
  static constexpr int PP(int s) { return cmin(s, C::NSA) * C::GA + cmax(cmin(s, C::NSB) - C::NPRE, 0) * C::GB; }
  static constexpr int T(int i) { return PP(C::NSB) + cmin(i, C::NK - C::NSA) * C::GA + cmin(i, C::NK - C::NSB) * C::GB; }
  static constexpr int need(int s) {
    const int pa = s < C::NSA ? PP(s) + C::GA : T(s - C::NSA) + C::GA;
    const int pb = s < C::NPRE ? 0 : (s < C::NSB ? PP(s + 1) : T(s - C::NSB + 1));
    return cmax(pa, pb);
  }
  static constexpr int cnt(int k) { return cmin(T(k) - need(k + 1), 63); }
  static constexpr int key(int k) { return cnt(k) * 4 + (k + C::NSA < C::NK ? 2 : 0) + (k + C::NSB < C::NK ? 1 : 0); }
  static constexpr int seg_end(int k) { int e = k + 1; while (e < C::NK - 1 && key(e) == key(k)) ++e; return e; }
  unsigned ca, cb;
  template <int K> __device__ __forceinline__ void iter(int k) {
    static_assert(cnt(K) <= 63, "vmcnt");
    wait_vm<cnt(K)>();
    bar();
    if constexpr (K + C::NSA < C::NK) { issueA(k + C::NSA, ca); ca += C::ABYTES; if (ca == C::RA) ca = 0; }
    if constexpr (K + C::NSB < C::NK) { issueB(k + C::NSB, cb); cb += C::BBYTES; if (cb == C::RB) cb = 0; }
  }
  template <int K, class Hook> __device__ __forceinline__ void seg(Hook hook2) {
    if constexpr (K < C::NK - 1) {
      if constexpr (K == C::NK - C::NSB) hook2();
      constexpr int E = seg_end(K);
      if constexpr (E - K == 1) iter<K>(K);
      else {
#pragma unroll 1
        for (int k = K; k < E; ++k) iter<K>(k);
      }
      seg<E>(hook2);
    }
  }
  template <class Hook, class Hook2> __device__ __forceinline__ void run(Hook hook, Hook2 hook2) {
#pragma unroll 1
    for (int s = 0; s < C::NSB; ++s) {
      if (s == NRAMP) { wait_vm<cmin(PP(NRAMP) - need(0), 63)>(); bar(); }
      if (s < C::NSA) issueA(s, s * C::ABYTES);
      if (s >= C::NPRE) issueB(s, s * C::BBYTES);
    }
    hook();
    ca = 0; cb = 0;
    seg<0>(hook2);
  }
};

template <class C>
struct Consumer {
  LAS char* lds;
  int wid, lane, wm, wn, wk;
  unsigned rdA[2], rdB[4];

  __device__ __forceinline__ Consumer(LAS char* lds_, int wid_, int lane_) : lds(lds_), wid(wid_), lane(lane_) {
    wk = wid % C::WK; wn = (wid / C::WK) % C::WN; wm = wid / (C::WK * C::WN);
    const int fr = lane & 15, fq = lane >> 4;
    const int arow = wm * (C::BM / C::WM) + fr;
    const int ss0 = wk * C::KSW;
#pragma unroll
    for (int s = 0; s < 2; ++s) {
      const int ss = ss0 + s;
      if constexpr (C::AMODE == 0) rdA[s] = ((ss0 * 4 + fq) * C::BM + arow) * 16;
      else rdA[s] = arow * 128 + ((((ss & 1) * 4 + fq) ^ ((fr >> 1) & 7)) * 16);
    }
    if constexpr (C::BMODE <= 1) {
      const int brow = wn * (C::BN / C::WN) + fr;
#pragma unroll
      for (int s = 0; s < 2; ++s) {
        const int ss = ss0 + s;
        if constexpr (C::BMODE == 0) rdB[s] = C::RA + ((ss0 * 4 + fq) * C::BN + brow) * 16;
        else rdB[s] = C::RA + brow * 128 + ((((ss & 1) * 4 + fq) ^ ((fr >> 1) & 7)) * 16);
      }
    } else {
      const int g = lane >> 4, q = (lane >> 2) & 3, p = lane & 3;
#pragma unroll
      for (int j = 0; j < 4; ++j) {
        const int n = j;
        const int jj = (C::BMODE == 4) ? j : 2 * wn + (n & 1), mat = (C::BMODE == 4) ? 0 : (n >> 1);
        rdB[j] = C::RA + mat * (C::BK * 128) + (32 * ss0 + 8 * g + q) * 128 + ((((jj ^ (((g & 1) << 1) | (q >> 1))) << 1) | (p >> 1)) * 16) + 8 * (p & 1);
      }
    }
  }

  __device__ __forceinline__ void read(Frags<C>& f, unsigned bufa, unsigned bufb) {
    LAS char* a = lds + bufa; LAS char* b = lds + bufb;
#pragma unroll
    for (int s = 0; s < C::KSW; ++s) {
      if constexpr (C::BMODE == 0) {
#pragma unroll
        for (int n = 0; n < C::NF; ++n) f.b[s][n] = *(const LAS bf16x8*)(b + rdB[0] + s * (C::BN * 64) + n * 256);
      } else if constexpr (C::BMODE == 1) {
#pragma unroll
        for (int n = 0; n < C::NF; ++n) f.b[s][n] = *(const LAS bf16x8*)(b + rdB[s] + n * 2048);
      } else {
#pragma unroll
        for (int n = 0; n < C::NF; ++n) {
          bf16x4 lo = __builtin_amdgcn_ds_read_tr16_b64_v4i16((LAS bf16x4*)(b + rdB[n] + s * 4096));
          bf16x4 hi = __builtin_amdgcn_ds_read_tr16_b64_v4i16((LAS bf16x4*)(b + rdB[n] + s * 4096 + 512));
          f.b[s][n] = __builtin_shufflevector(lo, hi, 0, 1, 2, 3, 4, 5, 6, 7);
        }
      }
      if constexpr (C::AMODE == 0) {
#pragma unroll
        for (int m = 0; m < C::MF; ++m) f.a[s][m] = *(const LAS bf16x8*)(a + rdA[0] + s * (C::BM * 64) + m * 256);
      } else {
#pragma unroll
        for (int m = 0; m < C::MF; ++m) f.a[s][m] = *(const LAS bf16x8*)(a + rdA[s] + m * 2048);
      }
    }
  }

  __device__ __forceinline__ void mma(f32x4 (&acc)[C::MF][C::NF], const Frags<C>& f) {
#pragma unroll
    for (int s = 0; s < C::KSW; ++s)
#pragma unroll
      for (int m = 0; m < C::MF; ++m)
#pragma unroll
        for (int n = 0; n < C::NF; ++n)
          acc[m][n] = C::SWAP ? __builtin_amdgcn_mfma_f32_16x16x32_bf16(f.b[s][n], f.a[s][m], acc[m][n], 0, 0, 0)
                              : __builtin_amdgcn_mfma_f32_16x16x32_bf16(f.a[s][m], f.b[s][n], acc[m][n], 0, 0, 0);
  }
  static constexpr int NRD = C::KSW * (C::MF + (C::BMODE >= 2 ? 2 : 1) * C::NF);
  static constexpr int NMM = C::KSW * C::MF * C::NF;
  __device__ __forceinline__ void interleave() {
    constexpr int RPM = (NRD + NMM - 3) / (NMM - 2);
#pragma unroll
    for (int i = 0; i < NMM; ++i) {
      __builtin_amdgcn_sched_group_barrier(0x100, RPM, 0);
      __builtin_amdgcn_sched_group_barrier(0x008, 1, 0);
    }
  }
  unsigned na, nb;
  __device__ __forceinline__ void step(f32x4 (&acc)[C::MF][C::NF], Frags<C>& cur, Frags<C>& nxt) {
    na += C::ABYTES; if (na == C::RA) na = 0;
    nb += C::BBYTES; if (nb == C::RB) nb = 0;
    wait_lgkm0(); bar();
    __builtin_amdgcn_sched_barrier(0);
    read(nxt, na, nb);
    mma(acc, cur);
    interleave();
    __builtin_amdgcn_sched_barrier(0);
  }

  template <class Hook, class Hook2> __device__ __forceinline__ void run(f32x4 (&acc)[C::MF][C::NF], Hook hook, Hook2 hook2) {
    Frags<C> f0, f1;
#pragma unroll
    for (int m = 0; m < C::MF; ++m)
#pragma unroll
      for (int n = 0; n < C::NF; ++n) { acc[m][n] = (f32x4){0.f, 0.f, 0.f, 0.f}; asm volatile("" : "+v"(acc[m][n])); }
    bar();
    if (wid == 0) { STAMP(C::KID, 1); CSTAMP(C::KID, 8); }
    hook();
    read(f0, 0, 0);
    na = 0; nb = 0;
    if (wid == 0) STAMP(C::KID, 4);
#pragma unroll 1
    for (int k = 0; k + 2 < C::NK; k += 2) {
      step(acc, f0, f1);
      step(acc, f1, f0);
    }
    if (wid == 0) STAMP(C::KID, 5);
    if constexpr (C::NK % 2 == 0) {
      step(acc, f0, f1);
      wait_lgkm0(); bar();
      hook2();
      mma(acc, f1);
    } else {
      wait_lgkm0(); bar();
      mma(acc, f0);
    }
    if (wid == 0) { STAMP(C::KID, 2); CSTAMP(C::KID, 9); }
  }

  static constexpr bool SPLM = (C::MF % 2 == 0);
  static constexpr int RM = SPLM ? C::MF / 2 : C::MF, RN = SPLM ? C::NF : C::NF / 2;
  __device__ __forceinline__ void reduce_k(f32x4 (&acc)[C::MF][C::NF], f32x4 (&red)[RM][RN]) {
    static_assert(C::WK == 2, "");
    static_assert(LDS_X + C::NC * RM * RN * 1024 <= LDS_MISC || RM * RN == 6, "");
    LAS f32x4* x = (LAS f32x4*)(lds + LDS_X - (RM * RN == 6 ? 1024 : 0));
#pragma unroll
    for (int m = 0; m < RM; ++m)
#pragma unroll
      for (int n = 0; n < RN; ++n) {
        const f32x4 lo = acc[m][n], hi = SPLM ? acc[m + RM][n] : acc[m][n + RN];
        x[(wid * RM * RN + m * RN + n) * 64 + lane] = wk ? lo : hi;
        red[m][n] = wk ? hi : lo;
      }
    wait_lgkm0(); bar();
#pragma unroll
    for (int m = 0; m < RM; ++m)
#pragma unroll
      for (int n = 0; n < RN; ++n) red[m][n] += x[((wid ^ 1) * RM * RN + m * RN + n) * 64 + lane];
  }
};

constexpr int FLE = 0;
constexpr int FL0 = 32;
constexpr int FL1 = FL0 + 2048;
constexpr int FL2 = FL1 + 16 * 16;
constexpr int FL3 = FL2 + 8 * 32;
constexpr int FLW = FL3 + 8 * 32;
constexpr int FLC = FLW + 48;

__device__ __forceinline__ void publish(LAS unsigned* arr, unsigned nwaves, u64* flag, u64 tag, int lane, int kid) {
  if (threadIdx.x < 64) STAMP(kid, 7);
  asm volatile("" ::: "memory");
  if (lane == 0) __hip_atomic_fetch_add(arr + 8, 1u, RLX_WG);
  wait_vm<0>();
  if (lane == 0) {
    const unsigned old = __hip_atomic_fetch_add(arr, 1u, RLX_WG);
    if (old == nwaves - 1) {
      __hip_atomic_store(flag, tag, RLX_AGENT);
      STAMP(kid, 6);
    }
  }
}
__device__ __forceinline__ void lds_wait(LAS unsigned* w, unsigned want) {
  const unsigned a = (unsigned)(size_t)w;
#pragma unroll 1
  for (unsigned i = 0; i < (1u << 24); ++i) {
    unsigned v;
    asm volatile("ds_read_b32 %0, %1\n\ts_waitcnt lgkmcnt(0)" : "=v"(v) : "v"(a) : "memory");
    if (__builtin_amdgcn_readfirstlane(v) >= want) break;
    __builtin_amdgcn_s_sleep(1);
  }
}
template <int SLEEP, int NW>
__device__ __forceinline__ void await(const u64* word, bool need, u64 tag, int lw, LAS unsigned* go, unsigned epoch) {
  if (lw == 0) {
    const u64 t0 = wall_clock64();
    for (;;) {
      bool ok = true;
#pragma unroll
      for (int i = 0; i < NW; ++i) ok &= __hip_atomic_load(word + i * 64, RLX_AGENT) == tag;
      if (__all(!need || ok)) break;
      if (wall_clock64() - t0 > 2000000ull) break;
      if (SLEEP > PSLEEP && __any(need && ok)) __builtin_amdgcn_s_sleep(PSLEEP); else __builtin_amdgcn_s_sleep(SLEEP);
    }
    __hip_atomic_store(go, epoch, RLX_WG);
  } else {
    lds_wait(go, epoch);
  }
  __builtin_amdgcn_fence(__ATOMIC_ACQUIRE, "wavefront");
}
#define P1_PRE 1
#define PL1 1
#define PL2 1
#define PL3 1

#define PRE1 2
#define PRE2 6
#define PRE3 5
#define PRE4 6
using C1 = Cfg<1, 80, 128, 64, 1, 4, 1, 4, 6, 32, 0, 1, 0, PRE1>;
#define P2A 4
#define P2B 6
using C2 = Cfg<2, 64, 80, 64, 2, 1, 2, P2A, P2B, 20, 0, 1, 1, PRE2>;
#define P3A 4
#define P3B 5
using C3 = Cfg<3, 256, 128, 32, 2, 2, 1, P3A, P3B, 20, 0, 3, 1, PRE3>;
#define P4A 4
#define P4B 6
using C4 = Cfg<4, 64, 80, 64, 2, 1, 2, P4A, P4B, 27, 0, 0, 1, PRE4>;
#define ROT 15
struct Src1 {
  const char* a; const char* b; int o, c;
  static constexpr int ldA = 0, ldB = S_ * 2, AUXA = AUXH, AUXB = 0, rows = 80;
  __device__ __forceinline__ int kt(int k) const { const int s = (k + o) & 31; return (ROT & 1) ? ((s & 24) | ((s + c) & 7)) : k; }
  __device__ __forceinline__ const char* baseA(int k) const { return a + kt(k) * (80 * 128); }
  __device__ __forceinline__ const char* baseB(int k, int) const { return b + kt(k) * 128; }
};
struct Src2 {
  const char* a; const char* bre; const char* bim; int rot;
  static constexpr int ldA = 0, ldB = D_ * 2, AUXA = AUXH, AUXB = 0, rows = 64;
  __device__ __forceinline__ int kt(int k) const { const int s = k + ((ROT & 2) ? rot : 0); return s >= 20 ? s - 20 : s; }
  __device__ __forceinline__ const char* baseA(int k) const { return a + kt(k) * (64 * 128); }
  __device__ __forceinline__ const char* baseB(int k, int) const { const int t = kt(k); return t < 10 ? bre + t * 128 : bim + (t - 10) * 128; }
};
struct Src3 {
  const char* a; const char* bg; const char* bu; int rot;
  static constexpr int ldA = 0, ldB = F_ * 2, AUXA = AUXH, AUXB = 0, rows = 256;
  __device__ __forceinline__ int kt(int k) const { const int s = k + ((ROT & 4) ? rot : 0); return s >= 20 ? s - 20 : s; }
  __device__ __forceinline__ const char* baseA(int k) const { return a + kt(k) * (256 * 64); }
  __device__ __forceinline__ const char* baseB(int k, int up) const { return (up ? bu : bg) + kt(k) * (32 * F_ * 2); }
};
struct Src4 {
  const char* a; const char* b;
  static constexpr int ldA = 0, ldB = 0, AUXA = AUXH, AUXB = AUXH, rows = 64;
  __device__ __forceinline__ const char* baseA(int k) const { return a + k * (64 * 128); }
  __device__ __forceinline__ const char* baseB(int k, int) const { return b + k * (80 * 128); }
};

struct Params {
  const u16* x; const u16* sre; const u16* sim; const u16* hre; const u16* him; const u16* wg; const u16* wu; const u16* wd;
  u16* xnI; u16* pqI; u16* hI; u16* hhI; u16* wdI; f32x2* stats; float* cs; u64* flags; u16* out;
};

template <int CTRL, int RMASK> __device__ __forceinline__ float dpp_add(float v) {
  return v + __int_as_float(__builtin_amdgcn_update_dpp(0, __float_as_int(v), CTRL, RMASK, 0xf, true));
}
__device__ __forceinline__ float wave_sum(float v) {
  v = dpp_add<0x111, 0xf>(v); v = dpp_add<0x112, 0xf>(v); v = dpp_add<0x114, 0xf>(v); v = dpp_add<0x118, 0xf>(v);
  v = dpp_add<0x142, 0xa>(v); v = dpp_add<0x143, 0xc>(v);
  return __int_as_float(__builtin_amdgcn_readlane(__float_as_int(v), 63));
}
__device__ __forceinline__ float sum_rows(float v) {
  const auto a = __builtin_amdgcn_permlane16_swap(__float_as_uint(v), __float_as_uint(v), false, false);
  v = __uint_as_float(a[0]) + __uint_as_float(a[1]);
  const auto b = __builtin_amdgcn_permlane32_swap(__float_as_uint(v), __float_as_uint(v), false, false);
  return __uint_as_float(b[0]) + __uint_as_float(b[1]);
}
__device__ __forceinline__ u32x4 col8(LAS char* tile, int cb, int lane) {
  LAS char* a = tile + ((lane >> 2) & 3) * 1296 + (cb * 64 + (lane >> 4) * 16 + (lane & 3) * 4) * 2;
  const bf16x4 lo = __builtin_amdgcn_ds_read_tr16_b64_v4i16((LAS bf16x4*)a);
  const bf16x4 hi = __builtin_amdgcn_ds_read_tr16_b64_v4i16((LAS bf16x4*)(a + 4 * 1296));
  return __builtin_bit_cast(u32x4, __builtin_shufflevector(lo, hi, 0, 1, 2, 3, 4, 5, 6, 7));
}
template <int N> __device__ __forceinline__ void bars() {
#pragma unroll 1
  for (int i = 0; i < N; ++i) bar();
}

__global__ __launch_bounds__(NWAVES * 64) void mega(const Params p) {
  __shared__ __attribute__((aligned(1024))) char smem_[LDS_TOTAL];
  LAS char* lds = (LAS char*)smem_;
  const int wid = __builtin_amdgcn_readfirstlane(threadIdx.x >> 6), lane = threadIdx.x & 63;
  const int bid = blockIdx.x, g = bid & 7, idx = bid >> 3;
  LAS unsigned* arr = (LAS unsigned*)(lds + LDS_MISC + 256);
  if (wid == 0) STAMP(0, 0);
  const u16* xr = p.x + (bid * 8 + (wid & 7)) * D_;
  const bf16x8 v0 = *(const bf16x8*)(xr + lane * 8);
  const bf16x8 v1 = *(const bf16x8*)(xr + 512 + (lane & 15) * 8);
  const u64 ep = __hip_atomic_load(p.flags + FLE, RLX_AGENT);
  if (threadIdx.x < 16) arr[threadIdx.x] = 0u;
  wait_lgkm0(); bar();
  const int j1 = idx & 7, mq = idx >> 3, isQ = mq >> 1, t1 = g * 256 + (mq & 1) * 128;
  const int j2 = idx & 7, mt2 = g * 4 + (idx >> 3);
  const int mt3 = (g & 6) + (idx & 1), j3 = (g & 1) * 14 + (idx >> 1);
  const bool on3 = idx < 28 && j3 < 27;
  LAS f32x4* rs = (LAS f32x4*)(lds + LDS_X);
  LAS float* csl = (LAS float*)(lds + LDS_X + 4096);

  {
    float f[16];
#pragma unroll
    for (int i = 0; i < 8; ++i) { f[i] = bf2f((u16)v0[i]); f[8 + i] = bf2f((u16)v1[i]); }
    const float piv = __int_as_float(__builtin_amdgcn_readfirstlane(__float_as_int(f[0])));
    asm volatile("" :: "v"(f[8]) : "memory");
    if (lane == 0) __hip_atomic_fetch_add(arr + 4, 1u, RLX_WG);
    if (wid == 0) STAMP(0, 4);
    float s0 = 0.f, s1 = 0.f, q0 = 0.f, q1 = 0.f;
#pragma unroll
    for (int i = 0; i < 8; ++i) { const float d = f[i] - piv, e = f[8 + i] - piv; s0 += d; q0 += d * d; s1 += e; q1 += e * e; }
    const float sd = wave_sum(s0 + (lane < 16 ? s1 : 0.f)) * (1.0f / D_);
    const float qd = wave_sum(q0 + (lane < 16 ? q1 : 0.f)) * (1.0f / D_);
    const float mb = rbf(piv + sd), vb = rbf(fmaxf(qd - sd * sd, 0.f));
    const float rden = __builtin_amdgcn_rcpf(rbf(__builtin_amdgcn_sqrtf(rbf(vb + 1e-5f))));
    u32x4 w0, w1;
#pragma unroll
    for (int i = 0; i < 4; ++i) {
      w0[i] = pk2(rbf(f[2 * i] - mb) * rden, rbf(f[2 * i + 1] - mb) * rden);
      w1[i] = pk2(rbf(f[8 + 2 * i] - mb) * rden, rbf(f[9 + 2 * i] - mb) * rden);
    }
    LAS char* tb = lds + LDS_X;
    *(LAS u32x4*)(tb + wid * 1296 + lane * 16) = w0;
    if (lane < 16) *(LAS u32x4*)(tb + wid * 1296 + 1024 + lane * 16) = w1;
    wait_lgkm0();
    if (lane == 0) __hip_atomic_fetch_add(arr + 7, 1u, RLX_WG);
  }
  const u64 tag = (((u64)(unsigned)__builtin_amdgcn_readfirstlane((unsigned)(ep >> 32)) << 32) | (unsigned)__builtin_amdgcn_readfirstlane((unsigned)ep)) + 1ull;
  if (wid >= 6) {
    const int lw = wid - 6;
    {
      asm volatile("" :: "s"(tag) : "memory");
      if (lane == 0) __hip_atomic_fetch_add(arr + 5, 1u, RLX_WG);
      Src1 s{(const char*)(p.xnI + (size_t)j1 * 256 * 80 * 8), (const char*)((isQ ? p.sim : p.sre) + (size_t)t1 * S_), (g + 2 * mq) & 7, j1};
      Loader<C1, Src1> L(lds, s, lw, lane);
      lds_wait(arr + 4, 8);
      L.preloadB();
      lds_wait(arr + 5, 8);
      await<PSLEEP, 4>(p.flags + FL0 + g * 256 + lane, true, tag, lw, arr + 3, 1u);
      if (bid == 0 && lw == 0 && lane == 0) __hip_atomic_store(p.flags + FLE, tag, RLX_AGENT);
      if (lw == 0) STAMP(1, 0);
      wait_vm<0>();
      L.run([] {}, [] {});
      bar();
    }
    {
      Src2 s{(const char*)(p.pqI + (size_t)mt2 * 160 * 64 * 8), (const char*)(p.hre + (size_t)j2 * 80 * D_), (const char*)(p.him + (size_t)j2 * 80 * D_), (mt2 * 5) >> 3};
      Loader<C2, Src2> L(lds, s, lw, lane);
      if (PL1 == 1) lds_wait(arr + 0, C1::NC);
      if (PL1 == 2) lds_wait(arr + 8, C1::NC);
      L.preloadB();
      lds_wait(arr + 0, C1::NC);
      await<PSLEEP, 1>(p.flags + FL1 + (mt2 >> 1) * 16 + (lane & 15), true, tag, lw, arr + 3, 2u);
      if (lw == 0) STAMP(2, 0);
      wait_vm<0>();
      L.run([] {}, [] {});
      bar();
    }
    if (on3) {
      Src3 s{(const char*)(p.hI + (size_t)mt3 * 80 * 256 * 8), (const char*)(p.wg + j3 * 64), (const char*)(p.wu + j3 * 64), (mt3 * 5) >> 1};
      Loader<C3, Src3> L(lds, s, lw, lane);
      bar();
      if (PL2 == 1) lds_wait(arr + 1, C2::NC);
      if (PL2 == 2) lds_wait(arr + 9, C2::NC);
      L.preloadB();
      lds_wait(arr + 1, C2::NC);
      await<PSLEEP, 1>(p.flags + FL2 + mt3 * 32 + (lane & 31), true, tag, lw, arr + 3, 3u);
      if (lw == 0) STAMP(3, 0);
      wait_vm<0>();
      L.run([] {}, [] {});
      bar();
      await<PSLEEP, 1>(p.flags + FLW + min(lane, 39), true, tag, lw, arr + 3, 4u);
    } else {
      bar();
    }
    {
      Src4 s{(const char*)(p.hhI + (size_t)mt2 * 216 * 64 * 8), (const char*)(p.wdI + (size_t)j2 * 216 * 80 * 8)};
      Loader<C4, Src4> L(lds, s, lw, lane);
      if (on3) {
        if (PL3 == 1) lds_wait(arr + 2, C3::NC);
        if (PL3 == 2) lds_wait(arr + 10, C3::NC);
        if (PL3 == 3) lds_wait(arr + 10, C3::NC / 2);
        L.preloadB();
        lds_wait(arr + 2, C3::NC);
        await<PSLEEP, 1>(p.flags + FL3 + g * 32 + (lane & 31), (lane & 31) < 27, tag, lw, arr + 3, 5u);
      } else {
        await<ISLEEP, 1>(p.flags + FLW + min(lane, 39), true, tag, lw, arr + 3, 4u);
        L.preloadB();
        await<ISLEEP, 1>(p.flags + FL3 + g * 32 + (lane & 31), (lane & 31) < 27, tag, lw, arr + 3, 5u);
      }
      if (lw == 0) STAMP(4, 0);
      wait_vm<0>();
      L.run([] {}, [] {});
      bar();
      bar();
    }
    return;
  }

#define FRESH_LANE int lane_ = threadIdx.x & 63; asm volatile("" : "+v"(lane_)); const int lane = lane_, fr = lane & 15, fq = lane >> 4; (void)fr; (void)fq;
  {
    LAS char* tb = lds + LDS_X;
    lds_wait(arr + 7, 8);
    auto rsrc = __builtin_amdgcn_make_buffer_rsrc(p.xnI, 0, S_ * D_ * 2, 0x00020000);
#pragma unroll
    for (int h = 0; h < 2; ++h) {
      const int cb = h * 6 + wid;
      if (cb < 10) {
        const int d = cb * 64 + lane, j = d / 80, n = d % 80;
        __builtin_amdgcn_raw_buffer_store_b128(col8(tb, cb, lane), rsrc, ((j * 256 + bid) * 80 + n) * 16, 0, 16);
      }
    }
    if (wid == 0) STAMP(0, 7);
    wait_vm<0>();
    lds_wait(arr + 5, 2);
    unsigned old = 0;
    if (lane == 0) old = __hip_atomic_fetch_add(arr + 5, 1u, RLX_WG);
    if (__builtin_amdgcn_readfirstlane(old) == 7u && lane < 8) __hip_atomic_store(p.flags + FL0 + lane * 256 + bid, tag, RLX_AGENT);
    if (wid == 0) STAMP(0, 3);
    if (wid >= 4) {
      {
        constexpr int PG = TW == 1 ? 65536 : (TW == 2 ? 4096 : 16384);
        constexpr int n0 = (655360 + PG - 1) / PG, n1 = n0 + (655360 + PG - 1) / PG, n2 = n1 + (1769472 + PG - 1) / PG, n3 = n2 + (2211840 + PG - 1) / PG,
                      n4 = n3 + (327680 + PG - 1) / PG, n5 = n4 + (819200 + PG - 1) / PG, n6 = n5 + (819200 + PG - 1) / PG, n7 = n6 + (2211840 + PG - 1) / PG,
                      n8 = n7 + (2211840 + PG - 1) / PG;
        const int q = ((wid - 4) * 64 + lane) * 32 + idx;
        const char* a = (const char*)p.pqI + (size_t)g * 655360 + (size_t)q * PG;
        if (q >= n0) a = (const char*)p.hI + (size_t)(g & 6) * 327680 + (size_t)(q - n0) * PG;
        if (q >= n1) a = (const char*)p.hhI + (size_t)(g & 6) * 884736 + (size_t)(q - n1) * PG;
        if (q >= n2) a = (const char*)p.wdI + (size_t)(q - n2) * PG;
        if (q >= n3) a = (const char*)p.out + (size_t)g * 327680 + (size_t)(q - n3) * PG;
        if (q >= n4) a = (const char*)p.hre + (size_t)(q - n4) * PG;
        if (q >= n5) a = (const char*)p.him + (size_t)(q - n5) * PG;
        if (q >= n6) a = (const char*)p.wg + (size_t)(q - n6) * PG;
        if (q >= n7) a = (const char*)p.wu + (size_t)(q - n7) * PG;
        if (q < n8) touch<0>(a, lds + LDS_MISC);
      }
      if (!on3) return;
      bars<C1::NBAR + C2::NBAR + 10>();
      const int aw = wid - 4;
      const u64 fc = __hip_atomic_load(p.flags + FLC + j3 * 16, RLX_AGENT);
      bars<2>();
      if (!__all(fc == tag)) await<PSLEEP, 1>(p.flags + FLC + j3 * 16, true, tag, 0, arr + 11, 1u);
      __builtin_amdgcn_fence(__ATOMIC_ACQUIRE, "wavefront");
      const float cv = __hip_atomic_load(p.cs + j3 * 128 + aw * 64 + lane, RLX_AGENT);
      f32x2 pv[2][8];
#pragma unroll
      for (int h = 0; h < 2; ++h)
#pragma unroll
        for (int i = 0; i < 8; ++i) pv[h][i] = __builtin_bit_cast(f32x2, ld8(p.stats + i * S_ + mt3 * 256 + aw * 128 + h * 64 + lane));
      bars<3>();
#pragma unroll
      for (int h = 0; h < 2; ++h) {
        float mean = 0.f, q = 0.f, dd = 0.f;
#pragma unroll
        for (int i = 0; i < 8; ++i) { mean += pv[h][i][0]; q += pv[h][i][1]; }
        mean *= 0.125f;
#pragma unroll
        for (int i = 0; i < 8; ++i) { const float d = pv[h][i][0] - mean; dd += d * d; }
        q += 80.0f * dd;
        const float den = rbf(__builtin_amdgcn_sqrtf(rbf(rbf(q * (1.0f / D_)) + 1e-5f)));
        rs[aw * 128 + h * 64 + lane] = (f32x4){-rbf(mean), den, -1.44269504089f * __builtin_amdgcn_rcpf(den), 0.f};
      }
      csl[aw * 64 + lane] = cv;
      wait_lgkm0();
      return;
    }
  }
  {
    FRESH_LANE
    Consumer<C1> L(lds, wid, lane);
    f32x4 acc[C1::MF][C1::NF];
    L.run(acc, [] {}, [] {});
    const float sg = isQ ? -1.f : 1.f;
#pragma unroll
    for (int n = 0; n < 2; ++n) {
      const int t = t1 + L.wn * 32 + n * 16 + fr, mt = t >> 6, r = t & 63;
#pragma unroll
      for (int m = 0; m < 5; ++m) {
        const int koct = isQ * 80 + j1 * 10 + m * 2 + (fq >> 1);
        u32x2 w; w[0] = pk2(sg * acc[m][n][0], sg * acc[m][n][1]); w[1] = pk2(sg * acc[m][n][2], sg * acc[m][n][3]);
        st8(p.pqI + (((size_t)mt * 160 + koct) * 64 + r) * 8 + (fq & 1) * 4, w);
      }
    }
    publish(arr + 0, C1::NC, p.flags + FL1 + (t1 >> 7) * 16 + isQ * 8 + j1, tag, lane, 1);
    if (wid == 0) STAMP(1, 3);
  }
  {
    FRESH_LANE
    Consumer<C2> L(lds, wid, lane);
    f32x4 acc[C2::MF][C2::NF];
    const int t = mt2 * 64 + L.wm * 32 + L.wk * 16 + fr;
    bf16x4 xv[C2::NF];
#pragma unroll
    for (int n = 0; n < C2::NF; ++n) xv[n] = *(const bf16x4*)(p.x + (size_t)t * D_ + j2 * 80 + n * 16 + fq * 4);
#pragma unroll
    for (int n = 0; n < C2::NF; ++n) asm volatile("" : "+v"(xv[n]));
    L.run(acc, [] {}, [] {});
    f32x4 red[1][5];
    L.reduce_k(acc, red);
    float hv[C2::NF][4];
    float s = 0.f;
#pragma unroll
    for (int n = 0; n < C2::NF; ++n) {
      u32x2 w;
      w[0] = pk2(red[0][n][0] + bf2f((u16)xv[n][0]), red[0][n][1] + bf2f((u16)xv[n][1]));
      w[1] = pk2(red[0][n][2] + bf2f((u16)xv[n][2]), red[0][n][3] + bf2f((u16)xv[n][3]));
      hv[n][0] = __uint_as_float(w[0] << 16); hv[n][1] = __uint_as_float(w[0] & 0xffff0000u);
      hv[n][2] = __uint_as_float(w[1] << 16); hv[n][3] = __uint_as_float(w[1] & 0xffff0000u);
      s += (hv[n][0] + hv[n][1]) + (hv[n][2] + hv[n][3]);
      const int koct = j2 * 10 + n * 2 + (fq >> 1);
      st8(p.hI + (((size_t)(t >> 8) * 80 + koct) * 256 + (t & 255)) * 8 + (fq & 1) * 4, w);
    }
    const float mean = sum_rows(s) * (1.0f / 80.0f);
    float q = 0.f;
#pragma unroll
    for (int n = 0; n < C2::NF; ++n)
#pragma unroll
      for (int r = 0; r < 4; ++r) { const float d = hv[n][r] - mean; q += d * d; }
    q = sum_rows(q);
    if (fq == 0) st8(p.stats + j2 * S_ + t, __builtin_bit_cast(u32x2, (f32x2){mean, q}));
    publish(arr + 1, C2::NC, p.flags + FL2 + g * 32 + idx, tag, lane, 2);
    if (wid == 0) STAMP(2, 3);
  }
  if (on3) {
    FRESH_LANE
    Consumer<C3> L(lds, wid, lane);
    f32x4 acc[C3::MF][C3::NF];
    f32x4 st[C3::MF], cg[2], cu[2];
    L.run(acc, [] {}, [&] {
#pragma unroll
      for (int m = 0; m < C3::MF; ++m) st[m] = rs[L.wm * 128 + m * 16 + fr];
#pragma unroll
      for (int i = 0; i < 2; ++i) {
        const int fl = L.wn * 32 + i * 16 + fq * 4;
        cg[i] = *(const LAS f32x4*)(csl + fl); cu[i] = *(const LAS f32x4*)(csl + 64 + fl);
      }
    });
    u16* hb = p.hhI + (((size_t)(mt3 * 4 + L.wm * 2) * 216 + j3 * 8 + L.wn * 4 + (fq >> 1)) * 64 + fr) * 8 + (fq & 1) * 4;
#pragma unroll
    for (int m = 0; m < C3::MF; ++m)
#pragma unroll
      for (int i = 0; i < 2; ++i) {
        const f32x4 s0 = {st[m][0], st[m][0], st[m][0], st[m][0]}, s1 = {st[m][1], st[m][1], st[m][1], st[m][1]};
        const f32x4 x = __builtin_elementwise_fma(cg[i], s0, acc[m][i]) * st[m][2];
        f32x4 e;
#pragma unroll
        for (int r = 0; r < 4; ++r) e[r] = __builtin_amdgcn_exp2f(x[r]);
        const f32x4 d = __builtin_elementwise_fma(e, s1, s1);
        f32x4 rc;
#pragma unroll
        for (int r = 0; r < 4; ++r) rc[r] = __builtin_amdgcn_rcpf(d[r]);
        const f32x4 o = __builtin_elementwise_fma(cu[i], s0, acc[m][i + 2]) * rc;
        u32x2 w; w[0] = pk2(o[0], o[1]); w[1] = pk2(o[2], o[3]);
        st8(hb + (m >> 2) * (216 * 64 * 8) + i * (2 * 64 * 8) + (m & 3) * 128, w);
      }
    publish(arr + 2, C3::NC, p.flags + FL3 + mt3 * 32 + j3, tag, lane, 3);
    if (wid == 0) STAMP(3, 3);
  } else {
    FRESH_LANE
    const int id = idx >= 28 ? g * 4 + idx - 28 : 32 + (g >> 1) * 2 + idx - 26;
    auto rsrc = __builtin_amdgcn_make_buffer_rsrc(p.wdI, 0, F_ * D_ * 2, 0x00020000);
    lds_wait(arr + 1, C2::NC);
    if (id < 27) {
      const u16* w = ((wid & 2) ? p.wu : p.wg) + (size_t)((wid & 1) * 320 + (lane >> 3)) * F_ + id * 64 + (lane & 7) * 8;
      f32x2 a[4] = {{0.f, 0.f}, {0.f, 0.f}, {0.f, 0.f}, {0.f, 0.f}};
#pragma unroll 1
      for (int b = 0; b < 2; ++b) {
        u32x4 v[20];
#pragma unroll
        for (int i = 0; i < 20; ++i) v[i] = *(const u32x4*)(w + (size_t)(b * 20 + i) * 8 * F_);
#pragma unroll
        for (int i = 0; i < 20; ++i)
#pragma unroll
          for (int e = 0; e < 4; ++e) a[e] += (f32x2){__uint_as_float(v[i][e] << 16), __uint_as_float(v[i][e] & 0xffff0000u)};
      }
      LAS float* sf = (LAS float*)(lds + LDS_X);
#pragma unroll
      for (int e = 0; e < 4; ++e)
#pragma unroll
        for (int c = 0; c < 2; ++c) { float t = sum_rows(a[e][c]); t += __shfl_xor(t, 8); a[e][c] = t; }
      if (lane < 8) {
        *(LAS f32x4*)(sf + wid * 64 + lane * 8) = (f32x4){a[0][0], a[0][1], a[1][0], a[1][1]};
        *(LAS f32x4*)(sf + wid * 64 + lane * 8 + 4) = (f32x4){a[2][0], a[2][1], a[3][0], a[3][1]};
      }
      wait_lgkm0();
      unsigned old = 0;
      if (lane == 0) old = __hip_atomic_fetch_add(arr + 12, 1u, RLX_WG);
      if (__builtin_amdgcn_readfirstlane(old) == 3u) {
        __hip_atomic_store(p.cs + id * 128 + lane, sf[lane] + sf[64 + lane], RLX_AGENT);
        __hip_atomic_store(p.cs + id * 128 + 64 + lane, sf[128 + lane] + sf[192 + lane], RLX_AGENT);
        wait_vm<0>();
        if (lane == 0) __hip_atomic_store(p.flags + FLC + id * 16, tag, RLX_AGENT);
      }
    }
    const int k0 = id < 27 ? id * 4 : 108 + (id - 27) * 9, nk = id < 27 ? 4 : min(9, 216 - k0);
#pragma unroll 1
    for (int i0 = 0; i0 < nk; i0 += 3) {
      bf16x8 u0[6], u1[6];
#pragma unroll
      for (int i = 0; i < 6; ++i) {
        const int koct = k0 + min(i0 + (i >> 1), nk - 1);
        const u16* wr = p.wd + (size_t)(koct * 8 + (i & 1) * 4 + wid) * D_;
        u0[i] = *(const bf16x8*)(wr + lane * 8);
        u1[i] = *(const bf16x8*)(wr + 512 + (lane & 15) * 8);
      }
#pragma unroll
      for (int i = 0; i < 6; ++i) {
        LAS char* tb = lds + (i0 + (i >> 1)) * 10368 + ((i & 1) * 4 + wid) * 1296;
        *(LAS bf16x8*)(tb + lane * 16) = u0[i];
        if (lane < 16) *(LAS bf16x8*)(tb + 1024 + lane * 16) = u1[i];
      }
    }
    wait_lgkm0();
    if (lane == 0) __hip_atomic_fetch_add(arr + 6, 1u, RLX_WG);
    lds_wait(arr + 6, 4);
#pragma unroll 1
    for (int i = 0; i < nk; ++i) {
#pragma unroll
      for (int h = 0; h < 3; ++h) {
        const int cb = h * 4 + wid;
        if (cb < 10) {
          const int d = cb * 64 + lane, j = d / 80, n = d % 80;
          __builtin_amdgcn_raw_buffer_store_b128(col8(lds + i * 10368, cb, lane), rsrc, ((j * 216 + k0 + i) * 80 + n) * 16, 0, 16);
        }
      }
    }
    publish(arr + 2, 4, p.flags + FLW + id, tag, lane, 0);
  }
  {
    FRESH_LANE
    Consumer<C4> L(lds, wid, lane);
    f32x4 acc[C4::MF][C4::NF];
    const int t = mt2 * 64 + L.wm * 32 + L.wk * 16 + fr;
    u32x2 hv[5];
    L.run(acc, [&] {
#pragma unroll
      for (int n = 0; n < 5; ++n)
        hv[n] = ld8(p.hI + (((size_t)(t >> 8) * 80 + j2 * 10 + n * 2 + (fq >> 1)) * 256 + (t & 255)) * 8 + (fq & 1) * 4);
    }, [] {});
    f32x4 red[1][5];
    L.reduce_k(acc, red);
#pragma unroll
    for (int n = 0; n < 5; ++n) {
      u32x2 w;
      w[0] = pk2(red[0][n][0] + __uint_as_float(hv[n][0] << 16), red[0][n][1] + __uint_as_float(hv[n][0] & 0xffff0000u));
      w[1] = pk2(red[0][n][2] + __uint_as_float(hv[n][1] << 16), red[0][n][3] + __uint_as_float(hv[n][1] & 0xffff0000u));
      *(u32x2*)(p.out + (size_t)t * D_ + j2 * 80 + n * 16 + fq * 4) = w;
    }
    if (wid == 0) { wait_vm<0>(); STAMP(4, 3); }
  }
}

extern "C" void kernel_launch(void* const* d_in, const int* in_sizes, int n_in, void* d_out, int out_size,
                              void* d_ws, size_t ws_size, hipStream_t stream) {
  char* ws = (char*)d_ws;
  Params p;
  p.x = (const u16*)d_in[0]; p.sre = (const u16*)d_in[1]; p.sim = (const u16*)d_in[2];
  p.hre = (const u16*)d_in[3]; p.him = (const u16*)d_in[4];
  p.wg = (const u16*)d_in[6]; p.wu = (const u16*)d_in[7]; p.wd = (const u16*)d_in[8];
  p.flags = (u64*)ws;
  p.cs = (float*)(ws + 65536);
  p.stats = (f32x2*)(ws + 131072);
  p.xnI = (u16*)(ws + (1u << 20));
  p.pqI = (u16*)(ws + (4u << 20));
  p.hI = (u16*)(ws + (10u << 20));
  p.hhI = (u16*)(ws + (13u << 20));
  p.wdI = (u16*)(ws + (21u << 20));
  p.out = (u16*)d_out;
  mega<<<256, NWAVES * 64, 0, stream>>>(p);
}
```

```cpp
#include <hip/hip_runtime.h>
#include <hip/hip_bf16.h>
#include <type_traits>
#define PSLEEP 4
#define ISLEEP 16
#define NRAMP 3
#define TW 0
#define XNT 0
#define PL0 1
#define AUXH 16

typedef unsigned short u16;
typedef unsigned long long u64;
typedef short bf16x8 __attribute__((ext_vector_type(8)));
typedef short bf16x4 __attribute__((ext_vector_type(4)));
typedef float f32x4 __attribute__((ext_vector_type(4)));
typedef float f32x2 __attribute__((ext_vector_type(2)));
typedef unsigned u32x2 __attribute__((ext_vector_type(2)));
typedef unsigned u32x4 __attribute__((ext_vector_type(4)));
#define LAS __attribute__((address_space(3)))
#define GAS __attribute__((address_space(1)))
#define RLX_AGENT __ATOMIC_RELAXED, __HIP_MEMORY_SCOPE_AGENT
#define RLX_WG __ATOMIC_RELAXED, __HIP_MEMORY_SCOPE_WORKGROUP

constexpr int S_ = 2048, D_ = 640, F_ = 1728;

__device__ __forceinline__ float bf2f(u16 v) { return __uint_as_float(((unsigned)v) << 16); }
typedef __bf16 bf16x2n __attribute__((ext_vector_type(2)));
__device__ __forceinline__ unsigned pk2(float lo, float hi) {
  return __builtin_bit_cast(unsigned, __builtin_convertvector((f32x2){lo, hi}, bf16x2n));
}
__device__ __forceinline__ float rbf(float v) { return __uint_as_float(pk2(v, v) << 16); }

#define CSTAMP(kid, i)
#define STAMP(kid, i)
template <int N> __device__ __forceinline__ void wait_vm() { asm volatile("s_waitcnt vmcnt(%0)" ::"n"(N) : "memory"); }
__device__ __forceinline__ void wait_lgkm0() { asm volatile("" ::: "memory"); __builtin_amdgcn_s_waitcnt(0xC07F); asm volatile("" ::: "memory"); }
__device__ __forceinline__ void bar() { asm volatile("" ::: "memory"); __builtin_amdgcn_s_barrier(); asm volatile("" ::: "memory"); }
template <int N, int STEP> __device__ __forceinline__ void wait_dyn(int n) {
  if constexpr (N <= 0) wait_vm<0>();
  else { if (n >= N) wait_vm<N>(); else wait_dyn<N - STEP, STEP>(n); }
}

template <int AUX> __device__ __forceinline__ void glds16(const char* g, LAS char* l) {
  __builtin_amdgcn_global_load_lds((const GAS void*)g, (LAS void*)l, 16, 0, AUX);
}
template <int AUX> __device__ __forceinline__ void touch(const char* g, LAS char* scratch) {
  __builtin_amdgcn_global_load_lds((const GAS void*)g, (LAS void*)scratch, 4, 0, AUX);
}
__device__ __forceinline__ void st8(void* p, u32x2 v) { __hip_atomic_store((u64*)p, __builtin_bit_cast(u64, v), RLX_AGENT); }
__device__ __forceinline__ u32x2 ld8(const void* p) { return __builtin_bit_cast(u32x2, __hip_atomic_load((u64*)p, RLX_AGENT)); }

constexpr int NL = 2;
constexpr int NWAVES = 8;
constexpr int LDS_RING = 136 * 1024;
constexpr int LDS_X = LDS_RING;
constexpr int LDS_TOTAL = 160 * 1024;
constexpr int LDS_MISC = LDS_TOTAL - 512;

template <int KID_, int BM_, int BN_, int BK_, int WM_, int WN_, int WK_, int NSA_, int NSB_, int NK_, int AMODE_, int BMODE_, int SWAP_, int NPRE_>
struct Cfg {
  static constexpr int KID = KID_, BM = BM_, BN = BN_, BK = BK_, WM = WM_, WN = WN_, WK = WK_, NSA = NSA_, NSB = NSB_, NK = NK_;
  static constexpr int AMODE = AMODE_, BMODE = BMODE_, SWAP = SWAP_, NPRE = NPRE_;
  static constexpr int NC = WM * WN * WK;
  static constexpr int MF = BM / WM / 16, NF = BN / WN / 16, KSW = BK / 32 / WK;
  static constexpr int ABYTES = BM * BK * 2, BBYTES = BN * BK * 2;
  static constexpr int PA = ABYTES / 1024, PB = BBYTES / 1024;
  static constexpr int GA = PA / NL, GB = PB / NL, G = GA + GB;
  static constexpr int RA = NSA * ABYTES, RB = NSB * BBYTES;
  static constexpr int NBAR = NK + (WK == 2 ? 2 : 1);
  static_assert(PA % NL == 0 && PB % NL == 0, "pieces");
  static_assert(RA + RB <= LDS_RING, "lds");
  static_assert(NSA <= NK && NSB <= NK && NSB >= NSA, "stages");
  static_assert(KSW >= 1 && KSW <= 2, "ksw");
};

template <class C> struct Frags { bf16x8 a[C::KSW][C::MF]; bf16x8 b[C::KSW][C::NF]; };

template <class C, class Src>
struct Loader {
  LAS char* lds; const Src& src;
  int lw, lane;
  unsigned voffA[C::GA], voffB[C::GB];
  static __device__ __forceinline__ unsigned rowmajor(int p, int ld) {
    const int row = p >> 3, c = (p & 7) ^ ((row >> 1) & 7);
    return row * ld + c * 16;
  }
  __device__ __forceinline__ Loader(LAS char* lds_, const Src& s, int lw_, int lane_) : lds(lds_), src(s), lw(lw_), lane(lane_) {
#pragma unroll
    for (int i = 0; i < C::GA; ++i) {
      const int p = (i * NL + lw) * 64 + lane;
      if constexpr (C::AMODE == 0) { const int r = p % C::BM; voffA[i] = (p - r + (r < src.rows ? r : (r & 31))) * 16; }
      else voffA[i] = rowmajor(p, Src::ldA);
    }
#pragma unroll
    for (int i = 0; i < C::GB; ++i) {
      const int p = (i * NL + lw) * 64 + lane;
      if constexpr (C::BMODE == 0) voffB[i] = p * 16;
      else if constexpr (C::BMODE == 1) voffB[i] = rowmajor(p, Src::ldB);
      else { const int q = p % (C::BK * 8), k = q >> 3, c = (q & 7) ^ ((((k >> 3) & 1) << 2) | (((k >> 1) & 1) << 1)); voffB[i] = k * Src::ldB + c * 16; }
    }
  }
  __device__ __forceinline__ void issueA(int k, unsigned buf) {
    const char* ga = src.baseA(k);
    LAS char* la = lds + buf + lw * 1024;
#pragma unroll
    for (int i = 0; i < C::GA; ++i) glds16<Src::AUXA>(ga + voffA[i], la + i * NL * 1024);
  }
  __device__ __forceinline__ void issueB(int k, unsigned buf) {
    LAS char* lb = lds + C::RA + buf + lw * 1024;
#pragma unroll
    for (int i = 0; i < C::GB; ++i) glds16<Src::AUXB>(src.baseB(k, i >= C::GB / 2) + voffB[i], lb + i * NL * 1024);
  }
  __device__ __forceinline__ void preloadB() {
#pragma unroll 1
    for (int s = 0; s < C::NPRE; ++s) issueB(s, s * C::BBYTES);
  }
  static constexpr int cmin(int a, int b) { return a < b ? a : b; }
  static constexpr int cmax(int a, int b) { return a > b ? a : b; }
  static constexpr int PP(int s) { return cmin(s, C::NSA) * C::GA + cmax(cmin(s, C::NSB) - C::NPRE, 0) * C::GB; }
  static constexpr int T(int i) { return PP(C::NSB) + cmin(i, C::NK - C::NSA) * C::GA + cmin(i, C::NK - C::NSB) * C::GB; }
  static constexpr int need(int s) {
    const int pa = s < C::NSA ? PP(s) + C::GA : T(s - C::NSA) + C::GA;
    const int pb = s < C::NPRE ? 0 : (s < C::NSB ? PP(s + 1) : T(s - C::NSB + 1));
    return cmax(pa, pb);
  }
  static constexpr int cnt(int k) { return cmin(T(k) - need(k + 1), 63); }
  static constexpr int key(int k) { return cnt(k) * 4 + (k + C::NSA < C::NK ? 2 : 0) + (k + C::NSB < C::NK ? 1 : 0); }
  static constexpr int seg_end(int k) { int e = k + 1; while (e < C::NK - 1 && key(e) == key(k)) ++e; return e; }
  unsigned ca, cb;
  template <int K> __device__ __forceinline__ void iter(int k) {
    static_assert(cnt(K) <= 63, "vmcnt");
    wait_vm<cnt(K)>();
    bar();
    if constexpr (K + C::NSA < C::NK) { issueA(k + C::NSA, ca); ca += C::ABYTES; if (ca == C::RA) ca = 0; }
    if constexpr (K + C::NSB < C::NK) { issueB(k + C::NSB, cb); cb += C::BBYTES; if (cb == C::RB) cb = 0; }
  }
  template <int K, class Hook> __device__ __forceinline__ void seg(Hook hook2) {
    if constexpr (K < C::NK - 1) {
      if constexpr (K == C::NK - C::NSB) hook2();
      constexpr int E = seg_end(K);
      if constexpr (E - K == 1) iter<K>(K);
      else {
#pragma unroll 1
        for (int k = K; k < E; ++k) iter<K>(k);
      }
      seg<E>(hook2);
    }
  }
  template <class Hook, class Hook2> __device__ __forceinline__ void run(Hook hook, Hook2 hook2) {
#pragma unroll 1
    for (int s = 0; s < C::NSB; ++s) {
      if (s == NRAMP) { wait_vm<cmin(PP(NRAMP) - need(0), 63)>(); bar(); }
      if (s < C::NSA) issueA(s, s * C::ABYTES);
      if (s >= C::NPRE) issueB(s, s * C::BBYTES);
    }
    hook();
    ca = 0; cb = 0;
    seg<0>(hook2);
  }
};

template <class C>
struct Consumer {
  LAS char* lds;
  int wid, lane, wm, wn, wk;
  unsigned rdA[2], rdB[4];

  __device__ __forceinline__ Consumer(LAS char* lds_, int wid_, int lane_) : lds(lds_), wid(wid_), lane(lane_) {
    wk = wid % C::WK; wn = (wid / C::WK) % C::WN; wm = wid / (C::WK * C::WN);
    const int fr = lane & 15, fq = lane >> 4;
    const int arow = wm * (C::BM / C::WM) + fr;
    const int ss0 = wk * C::KSW;
#pragma unroll
    for (int s = 0; s < 2; ++s) {
      const int ss = ss0 + s;
      if constexpr (C::AMODE == 0) rdA[s] = ((ss0 * 4 + fq) * C::BM + arow) * 16;
      else rdA[s] = arow * 128 + ((((ss & 1) * 4 + fq) ^ ((fr >> 1) & 7)) * 16);
    }
    if constexpr (C::BMODE <= 1) {
      const int brow = wn * (C::BN / C::WN) + fr;
#pragma unroll
      for (int s = 0; s < 2; ++s) {
        const int ss = ss0 + s;
        if constexpr (C::BMODE == 0) rdB[s] = C::RA + ((ss0 * 4 + fq) * C::BN + brow) * 16;
        else rdB[s] = C::RA + brow * 128 + ((((ss & 1) * 4 + fq) ^ ((fr >> 1) & 7)) * 16);
      }
    } else {
      const int g = lane >> 4, q = (lane >> 2) & 3, p = lane & 3;
#pragma unroll
      for (int j = 0; j < 4; ++j) {
        const int n = j;
        const int jj = (C::BMODE == 4) ? j : 2 * wn + (n & 1), mat = (C::BMODE == 4) ? 0 : (n >> 1);
        rdB[j] = C::RA + mat * (C::BK * 128) + (32 * ss0 + 8 * g + q) * 128 + ((((jj ^ (((g & 1) << 1) | (q >> 1))) << 1) | (p >> 1)) * 16) + 8 * (p & 1);
      }
    }
  }

  __device__ __forceinline__ void read(Frags<C>& f, unsigned bufa, unsigned bufb) {
    LAS char* a = lds + bufa; LAS char* b = lds + bufb;
#pragma unroll
    for (int s = 0; s < C::KSW; ++s) {
      if constexpr (C::BMODE == 0) {
#pragma unroll
        for (int n = 0; n < C::NF; ++n) f.b[s][n] = *(const LAS bf16x8*)(b + rdB[0] + s * (C::BN * 64) + n * 256);
      } else if constexpr (C::BMODE == 1) {
#pragma unroll
        for (int n = 0; n < C::NF; ++n) f.b[s][n] = *(const LAS bf16x8*)(b + rdB[s] + n * 2048);
      } else {
#pragma unroll
        for (int n = 0; n < C::NF; ++n) {
          bf16x4 lo = __builtin_amdgcn_ds_read_tr16_b64_v4i16((LAS bf16x4*)(b + rdB[n] + s * 4096));
          bf16x4 hi = __builtin_amdgcn_ds_read_tr16_b64_v4i16((LAS bf16x4*)(b + rdB[n] + s * 4096 + 512));
          f.b[s][n] = __builtin_shufflevector(lo, hi, 0, 1, 2, 3, 4, 5, 6, 7);
        }
      }
      if constexpr (C::AMODE == 0) {
#pragma unroll
        for (int m = 0; m < C::MF; ++m) f.a[s][m] = *(const LAS bf16x8*)(a + rdA[0] + s * (C::BM * 64) + m * 256);
      } else {
#pragma unroll
        for (int m = 0; m < C::MF; ++m) f.a[s][m] = *(const LAS bf16x8*)(a + rdA[s] + m * 2048);
      }
    }
  }

  __device__ __forceinline__ void mma(f32x4 (&acc)[C::MF][C::NF], const Frags<C>& f) {
#pragma unroll
    for (int s = 0; s < C::KSW; ++s)
#pragma unroll
      for (int m = 0; m < C::MF; ++m)
#pragma unroll
        for (int n = 0; n < C::NF; ++n)
          acc[m][n] = C::SWAP ? __builtin_amdgcn_mfma_f32_16x16x32_bf16(f.b[s][n], f.a[s][m], acc[m][n], 0, 0, 0)
                              : __builtin_amdgcn_mfma_f32_16x16x32_bf16(f.a[s][m], f.b[s][n], acc[m][n], 0, 0, 0);
  }
  static constexpr int NRD = C::KSW * (C::MF + (C::BMODE >= 2 ? 2 : 1) * C::NF);
  static constexpr int NMM = C::KSW * C::MF * C::NF;
  __device__ __forceinline__ void interleave() {
    constexpr int RPM = (NRD + NMM - 3) / (NMM - 2);
#pragma unroll
    for (int i = 0; i < NMM; ++i) {
      __builtin_amdgcn_sched_group_barrier(0x100, RPM, 0);
      __builtin_amdgcn_sched_group_barrier(0x008, 1, 0);
    }
  }
  unsigned na, nb;
  __device__ __forceinline__ void step(f32x4 (&acc)[C::MF][C::NF], Frags<C>& cur, Frags<C>& nxt) {
    na += C::ABYTES; if (na == C::RA) na = 0;
    nb += C::BBYTES; if (nb == C::RB) nb = 0;
    wait_lgkm0(); bar();
    __builtin_amdgcn_sched_barrier(0);
    read(nxt, na, nb);
    mma(acc, cur);
    interleave();
    __builtin_amdgcn_sched_barrier(0);
  }

  template <class Hook, class Hook2> __device__ __forceinline__ void run(f32x4 (&acc)[C::MF][C::NF], Hook hook, Hook2 hook2) {
    Frags<C> f0, f1;
#pragma unroll
    for (int m = 0; m < C::MF; ++m)
#pragma unroll
      for (int n = 0; n < C::NF; ++n) { acc[m][n] = (f32x4){0.f, 0.f, 0.f, 0.f}; asm volatile("" : "+v"(acc[m][n])); }
    bar();
    if (wid == 0) { STAMP(C::KID, 1); CSTAMP(C::KID, 8); }
    hook();
    read(f0, 0, 0);
    na = 0; nb = 0;
    if (wid == 0) STAMP(C::KID, 4);
#pragma unroll 1
    for (int k = 0; k + 2 < C::NK; k += 2) {
      step(acc, f0, f1);
      step(acc, f1, f0);
    }
    if (wid == 0) STAMP(C::KID, 5);
    if constexpr (C::NK % 2 == 0) {
      step(acc, f0, f1);
      wait_lgkm0(); bar();
      hook2();
      mma(acc, f1);
    } else {
      wait_lgkm0(); bar();
      mma(acc, f0);
    }
    if (wid == 0) { STAMP(C::KID, 2); CSTAMP(C::KID, 9); }
  }

  static constexpr bool SPLM = (C::MF % 2 == 0);
  static constexpr int RM = SPLM ? C::MF / 2 : C::MF, RN = SPLM ? C::NF : C::NF / 2;
  __device__ __forceinline__ void reduce_k(f32x4 (&acc)[C::MF][C::NF], f32x4 (&red)[RM][RN]) {
    static_assert(C::WK == 2, "");
    static_assert(LDS_X + C::NC * RM * RN * 1024 <= LDS_MISC || RM * RN == 6, "");
    LAS f32x4* x = (LAS f32x4*)(lds + LDS_X - (RM * RN == 6 ? 1024 : 0));
#pragma unroll
    for (int m = 0; m < RM; ++m)
#pragma unroll
      for (int n = 0; n < RN; ++n) {
        const f32x4 lo = acc[m][n], hi = SPLM ? acc[m + RM][n] : acc[m][n + RN];
        x[(wid * RM * RN + m * RN + n) * 64 + lane] = wk ? lo : hi;
        red[m][n] = wk ? hi : lo;
      }
    wait_lgkm0(); bar();
#pragma unroll
    for (int m = 0; m < RM; ++m)
#pragma unroll
      for (int n = 0; n < RN; ++n) red[m][n] += x[((wid ^ 1) * RM * RN + m * RN + n) * 64 + lane];
  }
};

constexpr int FLE = 0;
constexpr int FL0 = 32;
constexpr int FL1 = FL0 + 2048;
constexpr int FL2 = FL1 + 16 * 16;
constexpr int FL3 = FL2 + 8 * 32;
constexpr int FLW = FL3 + 8 * 32;
constexpr int FLC = FLW + 48;

__device__ __forceinline__ void publish(LAS unsigned* arr, unsigned nwaves, u64* flag, u64 tag, int lane, int kid) {
  if (threadIdx.x < 64) STAMP(kid, 7);
  asm volatile("" ::: "memory");
  if (lane == 0) __hip_atomic_fetch_add(arr + 8, 1u, RLX_WG);
  wait_vm<0>();
  if (lane == 0) {
    const unsigned old = __hip_atomic_fetch_add(arr, 1u, RLX_WG);
    if (old == nwaves - 1) {
      __hip_atomic_store(flag, tag, RLX_AGENT);
      STAMP(kid, 6);
    }
  }
}
__device__ __forceinline__ void lds_wait(LAS unsigned* w, unsigned want) {
  const unsigned a = (unsigned)(size_t)w;
#pragma unroll 1
  for (unsigned i = 0; i < (1u << 24); ++i) {
    unsigned v;
    asm volatile("ds_read_b32 %0, %1\n\ts_waitcnt lgkmcnt(0)" : "=v"(v) : "v"(a) : "memory");
    if (__builtin_amdgcn_readfirstlane(v) >= want) break;
    __builtin_amdgcn_s_sleep(1);
  }
}
template <int SLEEP, int NW>
__device__ __forceinline__ void await(const u64* word, bool need, u64 tag, int lw, LAS unsigned* go, unsigned epoch) {
  if (lw == 0) {
    const u64 t0 = wall_clock64();
    for (;;) {
      bool ok = true;
#pragma unroll
      for (int i = 0; i < NW; ++i) ok &= __hip_atomic_load(word + i * 64, RLX_AGENT) == tag;
      if (__all(!need || ok)) break;
      if (wall_clock64() - t0 > 2000000ull) break;
      if (SLEEP > PSLEEP && __any(need && ok)) __builtin_amdgcn_s_sleep(PSLEEP); else __builtin_amdgcn_s_sleep(SLEEP);
    }
    __hip_atomic_store(go, epoch, RLX_WG);
  } else {
    lds_wait(go, epoch);
  }
  __builtin_amdgcn_fence(__ATOMIC_ACQUIRE, "wavefront");
}
#define P1_PRE 1
#define PL1 1
#define PL2 1
#define PL3 1

#define PRE1 2
#define PRE2 6
#define PRE3 5
#define PRE4 6
using C1 = Cfg<1, 80, 128, 64, 1, 4, 1, 4, 6, 32, 0, 1, 0, PRE1>;
#define P2A 4
#define P2B 6
using C2 = Cfg<2, 64, 80, 64, 2, 1, 2, P2A, P2B, 20, 0, 1, 1, PRE2>;
#define P3A 4
#define P3B 5
using C3 = Cfg<3, 256, 128, 32, 2, 2, 1, P3A, P3B, 20, 0, 3, 1, PRE3>;
#define P4A 4
#define P4B 6
using C4 = Cfg<4, 64, 80, 64, 2, 1, 2, P4A, P4B, 27, 0, 0, 1, PRE4>;
#define ROT 15
struct Src1 {
  const char* a; const char* b; int o, c;
  static constexpr int ldA = 0, ldB = S_ * 2, AUXA = AUXH, AUXB = 0, rows = 80;
  __device__ __forceinline__ int kt(int k) const { const int s = (k + o) & 31; return (ROT & 1) ? ((s & 24) | ((s + c) & 7)) : k; }
  __device__ __forceinline__ const char* baseA(int k) const { return a + kt(k) * (80 * 128); }
  __device__ __forceinline__ const char* baseB(int k, int) const { return b + kt(k) * 128; }
};
struct Src2 {
  const char* a; const char* bre; const char* bim; int rot;
  static constexpr int ldA = 0, ldB = D_ * 2, AUXA = AUXH, AUXB = 0, rows = 64;
  __device__ __forceinline__ int kt(int k) const { const int s = k + ((ROT & 2) ? rot : 0); return s >= 20 ? s - 20 : s; }
  __device__ __forceinline__ const char* baseA(int k) const { return a + kt(k) * (64 * 128); }
  __device__ __forceinline__ const char* baseB(int k, int) const { const int t = kt(k); return t < 10 ? bre + t * 128 : bim + (t - 10) * 128; }
};
struct Src3 {
  const char* a; const char* bg; const char* bu; int rot;
  static constexpr int ldA = 0, ldB = F_ * 2, AUXA = AUXH, AUXB = 0, rows = 256;
  __device__ __forceinline__ int kt(int k) const { const int s = k + ((ROT & 4) ? rot : 0); return s >= 20 ? s - 20 : s; }
  __device__ __forceinline__ const char* baseA(int k) const { return a + kt(k) * (256 * 64); }
  __device__ __forceinline__ const char* baseB(int k, int up) const { return (up ? bu : bg) + kt(k) * (32 * F_ * 2); }
};
struct Src4 {
  const char* a; const char* b;
  static constexpr int ldA = 0, ldB = 0, AUXA = AUXH, AUXB = AUXH, rows = 64;
  __device__ __forceinline__ const char* baseA(int k) const { return a + k * (64 * 128); }
  __device__ __forceinline__ const char* baseB(int k, int) const { return b + k * (80 * 128); }
};

struct Params {
  const u16* x; const u16* sre; const u16* sim; const u16* hre; const u16* him; const u16* wg; const u16* wu; const u16* wd;
  u16* xnI; u16* pqI; u16* hI; u16* hhI; u16* wdI; f32x2* stats; float* cs; u64* flags; u16* out;
};

template <int CTRL, int RMASK> __device__ __forceinline__ float dpp_add(float v) {
  return v + __int_as_float(__builtin_amdgcn_update_dpp(0, __float_as_int(v), CTRL, RMASK, 0xf, true));
}
__device__ __forceinline__ float wave_sum(float v) {
  v = dpp_add<0x111, 0xf>(v); v = dpp_add<0x112, 0xf>(v); v = dpp_add<0x114, 0xf>(v); v = dpp_add<0x118, 0xf>(v);
  v = dpp_add<0x142, 0xa>(v); v = dpp_add<0x143, 0xc>(v);
  return __int_as_float(__builtin_amdgcn_readlane(__float_as_int(v), 63));
}
__device__ __forceinline__ float sum_rows(float v) {
  const auto a = __builtin_amdgcn_permlane16_swap(__float_as_uint(v), __float_as_uint(v), false, false);
  v = __uint_as_float(a[0]) + __uint_as_float(a[1]);
  const auto b = __builtin_amdgcn_permlane32_swap(__float_as_uint(v), __float_as_uint(v), false, false);
  return __uint_as_float(b[0]) + __uint_as_float(b[1]);
}
__device__ __forceinline__ u32x4 col8(LAS char* tile, int cb, int lane) {
  LAS char* a = tile + ((lane >> 2) & 3) * 1296 + (cb * 64 + (lane >> 4) * 16 + (lane & 3) * 4) * 2;
  const bf16x4 lo = __builtin_amdgcn_ds_read_tr16_b64_v4i16((LAS bf16x4*)a);
  const bf16x4 hi = __builtin_amdgcn_ds_read_tr16_b64_v4i16((LAS bf16x4*)(a + 4 * 1296));
  return __builtin_bit_cast(u32x4, __builtin_shufflevector(lo, hi, 0, 1, 2, 3, 4, 5, 6, 7));
}
template <int N> __device__ __forceinline__ void bars() {
#pragma unroll 1
  for (int i = 0; i < N; ++i) bar();
}

__global__ __launch_bounds__(NWAVES * 64) void mega(const Params p) {
  __shared__ __attribute__((aligned(1024))) char smem_[LDS_TOTAL];
  LAS char* lds = (LAS char*)smem_;
  const int wid = __builtin_amdgcn_readfirstlane(threadIdx.x >> 6), lane = threadIdx.x & 63;
  const int bid = blockIdx.x, g = bid & 7, idx = bid >> 3;
  LAS unsigned* arr = (LAS unsigned*)(lds + LDS_MISC + 256);
  if (wid == 0) STAMP(0, 0);
  const u16* xr = p.x + (bid * 8 + (wid & 7)) * D_;
  const bf16x8 v0 = *(const bf16x8*)(xr + lane * 8);
  const bf16x8 v1 = *(const bf16x8*)(xr + 512 + (lane & 15) * 8);
  const u64 ep = __hip_atomic_load(p.flags + FLE, RLX_AGENT);
  if (threadIdx.x < 16) arr[threadIdx.x] = 0u;
  wait_lgkm0(); bar();
  const int j1 = idx & 7, mq = idx >> 3, isQ = mq >> 1, t1 = g * 256 + (mq & 1) * 128;
  const int j2 = idx & 7, mt2 = g * 4 + (idx >> 3);
  const int mt3 = (g & 6) + (idx & 1), j3 = (g & 1) * 14 + (idx >> 1);
  const bool on3 = idx < 28 && j3 < 27;
  LAS f32x4* rs = (LAS f32x4*)(lds + LDS_X);
  LAS float* csl = (LAS float*)(lds + LDS_X + 4096);

  {
    float f[16];
#pragma unroll
    for (int i = 0; i < 8; ++i) { f[i] = bf2f((u16)v0[i]); f[8 + i] = bf2f((u16)v1[i]); }
    const float piv = __int_as_float(__builtin_amdgcn_readfirstlane(__float_as_int(f[0])));
    asm volatile("" :: "v"(f[8]) : "memory");
    if (lane == 0) __hip_atomic_fetch_add(arr + 4, 1u, RLX_WG);
    if (wid == 0) STAMP(0, 4);
    float s0 = 0.f, s1 = 0.f, q0 = 0.f, q1 = 0.f;
#pragma unroll
    for (int i = 0; i < 8; ++i) { const float d = f[i] - piv, e = f[8 + i] - piv; s0 += d; q0 += d * d; s1 += e; q1 += e * e; }
    const float sd = wave_sum(s0 + (lane < 16 ? s1 : 0.f)) * (1.0f / D_);
    const float qd = wave_sum(q0 + (lane < 16 ? q1 : 0.f)) * (1.0f / D_);
    const float mb = rbf(piv + sd), vb = rbf(fmaxf(qd - sd * sd, 0.f));
    const float rden = __builtin_amdgcn_rcpf(rbf(__builtin_amdgcn_sqrtf(rbf(vb + 1e-5f))));
    u32x4 w0, w1;
#pragma unroll
    for (int i = 0; i < 4; ++i) {
      w0[i] = pk2(rbf(f[2 * i] - mb) * rden, rbf(f[2 * i + 1] - mb) * rden);
      w1[i] = pk2(rbf(f[8 + 2 * i] - mb) * rden, rbf(f[9 + 2 * i] - mb) * rden);
    }
    LAS char* tb = lds + LDS_X;
    *(LAS u32x4*)(tb + wid * 1296 + lane * 16) = w0;
    if (lane < 16) *(LAS u32x4*)(tb + wid * 1296 + 1024 + lane * 16) = w1;
    wait_lgkm0();
    if (lane == 0) __hip_atomic_fetch_add(arr + 7, 1u, RLX_WG);
  }
  const u64 tag = (((u64)(unsigned)__builtin_amdgcn_readfirstlane((unsigned)(ep >> 32)) << 32) | (unsigned)__builtin_amdgcn_readfirstlane((unsigned)ep)) + 1ull;
  if (wid >= 6) {
    const int lw = wid - 6;
    {
      asm volatile("" :: "s"(tag) : "memory");
      if (lane == 0) __hip_atomic_fetch_add(arr + 5, 1u, RLX_WG);
      Src1 s{(const char*)(p.xnI + (size_t)j1 * 256 * 80 * 8), (const char*)((isQ ? p.sim : p.sre) + (size_t)t1 * S_), (g + 2 * mq) & 7, j1};
      Loader<C1, Src1> L(lds, s, lw, lane);
      if (PL0 == 0) lds_wait(arr + 4, 8);
      if (PL0 == 1) lds_wait(arr + 5, 8);
      L.preloadB();
      lds_wait(arr + 5, 8);
      await<PSLEEP, 4>(p.flags + FL0 + g * 256 + lane, true, tag, lw, arr + 3, 1u);
      if (bid == 0 && lw == 0 && lane == 0) __hip_atomic_store(p.flags + FLE, tag, RLX_AGENT);
      if (lw == 0) STAMP(1, 0);
      wait_vm<0>();
      L.run([] {}, [] {});
      bar();
    }
    {
      Src2 s{(const char*)(p.pqI + (size_t)mt2 * 160 * 64 * 8), (const char*)(p.hre + (size_t)j2 * 80 * D_), (const char*)(p.him + (size_t)j2 * 80 * D_), (mt2 * 5) >> 3};
      Loader<C2, Src2> L(lds, s, lw, lane);
      if (PL1 == 1) lds_wait(arr + 0, C1::NC);
      if (PL1 == 2) lds_wait(arr + 8, C1::NC);
      L.preloadB();
      lds_wait(arr + 0, C1::NC);
      await<PSLEEP, 1>(p.flags + FL1 + (mt2 >> 1) * 16 + (lane & 15), true, tag, lw, arr + 3, 2u);
      if (lw == 0) STAMP(2, 0);
      wait_vm<0>();
      L.run([] {}, [] {});
      bar();
    }
    if (on3) {
      Src3 s{(const char*)(p.hI + (size_t)mt3 * 80 * 256 * 8), (const char*)(p.wg + j3 * 64), (const char*)(p.wu + j3 * 64), (mt3 * 5) >> 1};
      Loader<C3, Src3> L(lds, s, lw, lane);
      bar();
      if (PL2 == 1) lds_wait(arr + 1, C2::NC);
      if (PL2 == 2) lds_wait(arr + 9, C2::NC);
      L.preloadB();
      lds_wait(arr + 1, C2::NC);
      await<PSLEEP, 1>(p.flags + FL2 + mt3 * 32 + (lane & 31), true, tag, lw, arr + 3, 3u);
      if (lw == 0) STAMP(3, 0);
      wait_vm<0>();
      L.run([] {}, [] {});
      bar();
      await<PSLEEP, 1>(p.flags + FLW + min(lane, 39), true, tag, lw, arr + 3, 4u);
    } else {
      bar();
    }
    {
      Src4 s{(const char*)(p.hhI + (size_t)mt2 * 216 * 64 * 8), (const char*)(p.wdI + (size_t)j2 * 216 * 80 * 8)};
      Loader<C4, Src4> L(lds, s, lw, lane);
      if (on3) {
        if (PL3 == 1) lds_wait(arr + 2, C3::NC);
        if (PL3 == 2) lds_wait(arr + 10, C3::NC);
        if (PL3 == 3) lds_wait(arr + 10, C3::NC / 2);
        L.preloadB();
        lds_wait(arr + 2, C3::NC);
        await<PSLEEP, 1>(p.flags + FL3 + g * 32 + (lane & 31), (lane & 31) < 27, tag, lw, arr + 3, 5u);
      } else {
        await<ISLEEP, 1>(p.flags + FLW + min(lane, 39), true, tag, lw, arr + 3, 4u);
        L.preloadB();
        await<ISLEEP, 1>(p.flags + FL3 + g * 32 + (lane & 31), (lane & 31) < 27, tag, lw, arr + 3, 5u);
      }
      if (lw == 0) STAMP(4, 0);
      wait_vm<0>();
      L.run([] {}, [] {});
      bar();
      bar();
    }
    return;
  }

#define FRESH_LANE int lane_ = threadIdx.x & 63; asm volatile("" : "+v"(lane_)); const int lane = lane_, fr = lane & 15, fq = lane >> 4; (void)fr; (void)fq;
  {
    LAS char* tb = lds + LDS_X;
    lds_wait(arr + 7, 8);
    auto rsrc = __builtin_amdgcn_make_buffer_rsrc(p.xnI, 0, S_ * D_ * 2, 0x00020000);
#pragma unroll
    for (int h = 0; h < 2; ++h) {
      const int cb = h * 6 + wid;
      if (cb < 10) {
        const int d = cb * 64 + lane, j = d / 80, n = d % 80;
        __builtin_amdgcn_raw_buffer_store_b128(col8(tb, cb, lane), rsrc, ((j * 256 + bid) * 80 + n) * 16, 0, 16);
      }
    }
    if (wid == 0) STAMP(0, 7);
    wait_vm<0>();
    lds_wait(arr + 5, 2);
    unsigned old = 0;
    if (lane == 0) old = __hip_atomic_fetch_add(arr + 5, 1u, RLX_WG);
    if (__builtin_amdgcn_readfirstlane(old) == 7u && lane < 8) __hip_atomic_store(p.flags + FL0 + lane * 256 + bid, tag, RLX_AGENT);
    if (wid == 0) STAMP(0, 3);
    if (wid >= 4) {
      if (!on3) return;
      bars<C1::NBAR + C2::NBAR + 13>();
      const int aw = wid - 4;
      const u64 fc = __hip_atomic_load(p.flags + FLC + j3 * 16, RLX_AGENT);
      bars<2>();
      if (!__all(fc == tag)) await<PSLEEP, 1>(p.flags + FLC + j3 * 16, true, tag, 0, arr + 11, 1u);
      __builtin_amdgcn_fence(__ATOMIC_ACQUIRE, "wavefront");
      const float cv = __hip_atomic_load(p.cs + j3 * 128 + aw * 64 + lane, RLX_AGENT);
      f32x2 pv[2][8];
#pragma unroll
      for (int h = 0; h < 2; ++h)
#pragma unroll
        for (int i = 0; i < 8; ++i) pv[h][i] = __builtin_bit_cast(f32x2, ld8(p.stats + i * S_ + mt3 * 256 + aw * 128 + h * 64 + lane));
      bars<3>();
#pragma unroll
      for (int h = 0; h < 2; ++h) {
        float mean = 0.f, q = 0.f, dd = 0.f;
#pragma unroll
        for (int i = 0; i < 8; ++i) { mean += pv[h][i][0]; q += pv[h][i][1]; }
        mean *= 0.125f;
#pragma unroll
        for (int i = 0; i < 8; ++i) { const float d = pv[h][i][0] - mean; dd += d * d; }
        q += 80.0f * dd;
        const float den = rbf(__builtin_amdgcn_sqrtf(rbf(rbf(q * (1.0f / D_)) + 1e-5f)));
        rs[aw * 128 + h * 64 + lane] = (f32x4){-rbf(mean), den, -1.44269504089f * __builtin_amdgcn_rcpf(den), 0.f};
      }
      csl[aw * 64 + lane] = cv;
      wait_lgkm0();
      return;
    }
  }
  {
    FRESH_LANE
    Consumer<C1> L(lds, wid, lane);
    f32x4 acc[C1::MF][C1::NF];
    L.run(acc, [] {}, [] {});
    const float sg = isQ ? -1.f : 1.f;
#pragma unroll
    for (int n = 0; n < 2; ++n) {
      const int t = t1 + L.wn * 32 + n * 16 + fr, mt = t >> 6, r = t & 63;
#pragma unroll
      for (int m = 0; m < 5; ++m) {
        const int koct = isQ * 80 + j1 * 10 + m * 2 + (fq >> 1);
        u32x2 w; w[0] = pk2(sg * acc[m][n][0], sg * acc[m][n][1]); w[1] = pk2(sg * acc[m][n][2], sg * acc[m][n][3]);
        st8(p.pqI + (((size_t)mt * 160 + koct) * 64 + r) * 8 + (fq & 1) * 4, w);
      }
    }
    publish(arr + 0, C1::NC, p.flags + FL1 + (t1 >> 7) * 16 + isQ * 8 + j1, tag, lane, 1);
    if (wid == 0) STAMP(1, 3);
  }
  {
    FRESH_LANE
    Consumer<C2> L(lds, wid, lane);
    f32x4 acc[C2::MF][C2::NF];
    const int t = mt2 * 64 + L.wm * 32 + L.wk * 16 + fr;
    bf16x4 xv[C2::NF];
#pragma unroll
    for (int n = 0; n < C2::NF; ++n) xv[n] = *(const bf16x4*)(p.x + (size_t)t * D_ + j2 * 80 + n * 16 + fq * 4);
#pragma unroll
    for (int n = 0; n < C2::NF; ++n) asm volatile("" : "+v"(xv[n]));
    L.run(acc, [] {}, [] {});
    f32x4 red[1][5];
    L.reduce_k(acc, red);
    float hv[C2::NF][4];
    float s = 0.f;
#pragma unroll
    for (int n = 0; n < C2::NF; ++n) {
      u32x2 w;
      w[0] = pk2(red[0][n][0] + bf2f((u16)xv[n][0]), red[0][n][1] + bf2f((u16)xv[n][1]));
      w[1] = pk2(red[0][n][2] + bf2f((u16)xv[n][2]), red[0][n][3] + bf2f((u16)xv[n][3]));
      hv[n][0] = __uint_as_float(w[0] << 16); hv[n][1] = __uint_as_float(w[0] & 0xffff0000u);
      hv[n][2] = __uint_as_float(w[1] << 16); hv[n][3] = __uint_as_float(w[1] & 0xffff0000u);
      s += (hv[n][0] + hv[n][1]) + (hv[n][2] + hv[n][3]);
      const int koct = j2 * 10 + n * 2 + (fq >> 1);
      st8(p.hI + (((size_t)(t >> 8) * 80 + koct) * 256 + (t & 255)) * 8 + (fq & 1) * 4, w);
    }
    const float mean = sum_rows(s) * (1.0f / 80.0f);
    float q = 0.f;
#pragma unroll
    for (int n = 0; n < C2::NF; ++n)
#pragma unroll
      for (int r = 0; r < 4; ++r) { const float d = hv[n][r] - mean; q += d * d; }
    q = sum_rows(q);
    if (fq == 0) st8(p.stats + j2 * S_ + t, __builtin_bit_cast(u32x2, (f32x2){mean, q}));
    publish(arr + 1, C2::NC, p.flags + FL2 + g * 32 + idx, tag, lane, 2);
    if (wid == 0) STAMP(2, 3);
  }
  if (on3) {
    FRESH_LANE
    Consumer<C3> L(lds, wid, lane);
    f32x4 acc[C3::MF][C3::NF];
    f32x4 st[C3::MF], cg[2], cu[2];
    L.run(acc, [] {}, [&] {
#pragma unroll
      for (int m = 0; m < C3::MF; ++m) st[m] = rs[L.wm * 128 + m * 16 + fr];
#pragma unroll
      for (int i = 0; i < 2; ++i) {
        const int fl = L.wn * 32 + i * 16 + fq * 4;
        cg[i] = *(const LAS f32x4*)(csl + fl); cu[i] = *(const LAS f32x4*)(csl + 64 + fl);
      }
    });
    u16* hb = p.hhI + (((size_t)(mt3 * 4 + L.wm * 2) * 216 + j3 * 8 + L.wn * 4 + (fq >> 1)) * 64 + fr) * 8 + (fq & 1) * 4;
#pragma unroll
    for (int m = 0; m < C3::MF; ++m)
#pragma unroll
      for (int i = 0; i < 2; ++i) {
        const f32x4 s0 = {st[m][0], st[m][0], st[m][0], st[m][0]}, s1 = {st[m][1], st[m][1], st[m][1], st[m][1]};
        const f32x4 x = __builtin_elementwise_fma(cg[i], s0, acc[m][i]) * st[m][2];
        f32x4 e;
#pragma unroll
        for (int r = 0; r < 4; ++r) e[r] = __builtin_amdgcn_exp2f(x[r]);
        const f32x4 d = __builtin_elementwise_fma(e, s1, s1);
        f32x4 rc;
#pragma unroll
        for (int r = 0; r < 4; ++r) rc[r] = __builtin_amdgcn_rcpf(d[r]);
        const f32x4 o = __builtin_elementwise_fma(cu[i], s0, acc[m][i + 2]) * rc;
        u32x2 w; w[0] = pk2(o[0], o[1]); w[1] = pk2(o[2], o[3]);
        st8(hb + (m >> 2) * (216 * 64 * 8) + i * (2 * 64 * 8) + (m & 3) * 128, w);
      }
    publish(arr + 2, C3::NC, p.flags + FL3 + mt3 * 32 + j3, tag, lane, 3);
    if (wid == 0) STAMP(3, 3);
  } else {
    FRESH_LANE
    const int id = idx >= 28 ? g * 4 + idx - 28 : 32 + (g >> 1) * 2 + idx - 26;
    auto rsrc = __builtin_amdgcn_make_buffer_rsrc(p.wdI, 0, F_ * D_ * 2, 0x00020000);
    lds_wait(arr + 1, C2::NC);
    if (id < 27) {
      const u16* w = ((wid & 2) ? p.wu : p.wg) + (size_t)((wid & 1) * 320 + (lane >> 3)) * F_ + id * 64 + (lane & 7) * 8;
      f32x2 a[4] = {{0.f, 0.f}, {0.f, 0.f}, {0.f, 0.f}, {0.f, 0.f}};
#pragma unroll 1
      for (int b = 0; b < 2; ++b) {
        u32x4 v[20];
#pragma unroll
        for (int i = 0; i < 20; ++i) v[i] = *(const u32x4*)(w + (size_t)(b * 20 + i) * 8 * F_);
#pragma unroll
        for (int i = 0; i < 20; ++i)
#pragma unroll
          for (int e = 0; e < 4; ++e) a[e] += (f32x2){__uint_as_float(v[i][e] << 16), __uint_as_float(v[i][e] & 0xffff0000u)};
      }
      LAS float* sf = (LAS float*)(lds + LDS_X);
#pragma unroll
      for (int e = 0; e < 4; ++e)
#pragma unroll
        for (int c = 0; c < 2; ++c) { float t = sum_rows(a[e][c]); t += __shfl_xor(t, 8); a[e][c] = t; }
      if (lane < 8) {
        *(LAS f32x4*)(sf + wid * 64 + lane * 8) = (f32x4){a[0][0], a[0][1], a[1][0], a[1][1]};
        *(LAS f32x4*)(sf + wid * 64 + lane * 8 + 4) = (f32x4){a[2][0], a[2][1], a[3][0], a[3][1]};
      }
      wait_lgkm0();
      unsigned old = 0;
      if (lane == 0) old = __hip_atomic_fetch_add(arr + 12, 1u, RLX_WG);
      if (__builtin_amdgcn_readfirstlane(old) == 3u) {
        __hip_atomic_store(p.cs + id * 128 + lane, sf[lane] + sf[64 + lane], RLX_AGENT);
        __hip_atomic_store(p.cs + id * 128 + 64 + lane, sf[128 + lane] + sf[192 + lane], RLX_AGENT);
        wait_vm<0>();
        if (lane == 0) __hip_atomic_store(p.flags + FLC + id * 16, tag, RLX_AGENT);
        STAMP(0, 5);
      }
    }
    const int k0 = id < 27 ? id * 3 : 81 + (id - 27) * 11, nk = id < 27 ? 3 : min(11, 216 - k0);
#pragma unroll 1
    for (int i0 = 0; i0 < nk; i0 += 3) {
      bf16x8 u0[6], u1[6];
#pragma unroll
      for (int i = 0; i < 6; ++i) {
        const int koct = k0 + min(i0 + (i >> 1), nk - 1);
        const u16* wr = p.wd + (size_t)(koct * 8 + (i & 1) * 4 + wid) * D_;
        u0[i] = *(const bf16x8*)(wr + lane * 8);
        u1[i] = *(const bf16x8*)(wr + 512 + (lane & 15) * 8);
      }
#pragma unroll
      for (int i = 0; i < 6; ++i) {
        LAS char* tb = lds + (i0 + (i >> 1)) * 10368 + ((i & 1) * 4 + wid) * 1296;
        *(LAS bf16x8*)(tb + lane * 16) = u0[i];
        if (lane < 16) *(LAS bf16x8*)(tb + 1024 + lane * 16) = u1[i];
      }
    }
    wait_lgkm0();
    if (lane == 0) __hip_atomic_fetch_add(arr + 6, 1u, RLX_WG);
    lds_wait(arr + 6, 4);
#pragma unroll 1
    for (int i = 0; i < nk; ++i) {
#pragma unroll
      for (int h = 0; h < 3; ++h) {
        const int cb = h * 4 + wid;
        if (cb < 10) {
          const int d = cb * 64 + lane, j = d / 80, n = d % 80;
          __builtin_amdgcn_raw_buffer_store_b128(col8(lds + i * 10368, cb, lane), rsrc, ((j * 216 + k0 + i) * 80 + n) * 16, 0, 16);
        }
      }
    }
    publish(arr + 2, 4, p.flags + FLW + id, tag, lane, 0);
  }
  {
    FRESH_LANE
    Consumer<C4> L(lds, wid, lane);
    f32x4 acc[C4::MF][C4::NF];
    const int t = mt2 * 64 + L.wm * 32 + L.wk * 16 + fr;
    u32x2 hv[5];
    L.run(acc, [&] {
#pragma unroll
      for (int n = 0; n < 5; ++n)
        hv[n] = ld8(p.hI + (((size_t)(t >> 8) * 80 + j2 * 10 + n * 2 + (fq >> 1)) * 256 + (t & 255)) * 8 + (fq & 1) * 4);
    }, [] {});
    f32x4 red[1][5];
    L.reduce_k(acc, red);
#pragma unroll
    for (int n = 0; n < 5; ++n) {
      u32x2 w;
      w[0] = pk2(red[0][n][0] + __uint_as_float(hv[n][0] << 16), red[0][n][1] + __uint_as_float(hv[n][0] & 0xffff0000u));
      w[1] = pk2(red[0][n][2] + __uint_as_float(hv[n][1] << 16), red[0][n][3] + __uint_as_float(hv[n][1] & 0xffff0000u));
      *(u32x2*)(p.out + (size_t)t * D_ + j2 * 80 + n * 16 + fq * 4) = w;
    }
    if (wid == 0) { wait_vm<0>(); STAMP(4, 3); }
  }
}

extern "C" void kernel_launch(void* const* d_in, const int* in_sizes, int n_in, void* d_out, int out_size,
                              void* d_ws, size_t ws_size, hipStream_t stream) {
  char* ws = (char*)d_ws;
  Params p;
  p.x = (const u16*)d_in[0]; p.sre = (const u16*)d_in[1]; p.sim = (const u16*)d_in[2];
  p.hre = (const u16*)d_in[3]; p.him = (const u16*)d_in[4];
  p.wg = (const u16*)d_in[6]; p.wu = (const u16*)d_in[7]; p.wd = (const u16*)d_in[8];
  p.flags = (u64*)ws;
  p.cs = (float*)(ws + 65536);
  p.stats = (f32x2*)(ws + 131072);
  p.xnI = (u16*)(ws + (1u << 20));
  p.pqI = (u16*)(ws + (4u << 20));
  p.hI = (u16*)(ws + (10u << 20));
  p.hhI = (u16*)(ws + (13u << 20));
  p.wdI = (u16*)(ws + (21u << 20));
  p.out = (u16*)d_out;
  mega<<<256, NWAVES * 64, 0, stream>>>(p);
}
```

```cpp
#include <hip/hip_runtime.h>
#include <hip/hip_bf16.h>
#include <type_traits>
#define PSLEEP 4
#define ISLEEP 16
#define NRAMP 3
#define TW 0
#define XNT 0
#define PL0 1
#define PDELAY 40
#define AUXH 16

typedef unsigned short u16;
typedef unsigned long long u64;
typedef short bf16x8 __attribute__((ext_vector_type(8)));
typedef short bf16x4 __attribute__((ext_vector_type(4)));
typedef float f32x4 __attribute__((ext_vector_type(4)));
typedef float f32x2 __attribute__((ext_vector_type(2)));
typedef unsigned u32x2 __attribute__((ext_vector_type(2)));
typedef unsigned u32x4 __attribute__((ext_vector_type(4)));
#define LAS __attribute__((address_space(3)))
#define GAS __attribute__((address_space(1)))
#define RLX_AGENT __ATOMIC_RELAXED, __HIP_MEMORY_SCOPE_AGENT
#define RLX_WG __ATOMIC_RELAXED, __HIP_MEMORY_SCOPE_WORKGROUP

constexpr int S_ = 2048, D_ = 640, F_ = 1728;

__device__ __forceinline__ float bf2f(u16 v) { return __uint_as_float(((unsigned)v) << 16); }
typedef __bf16 bf16x2n __attribute__((ext_vector_type(2)));
__device__ __forceinline__ unsigned pk2(float lo, float hi) {
  return __builtin_bit_cast(unsigned, __builtin_convertvector((f32x2){lo, hi}, bf16x2n));
}
__device__ __forceinline__ float rbf(float v) { return __uint_as_float(pk2(v, v) << 16); }

#define CSTAMP(kid, i)
#define STAMP(kid, i)
template <int N> __device__ __forceinline__ void wait_vm() { asm volatile("s_waitcnt vmcnt(%0)" ::"n"(N) : "memory"); }
__device__ __forceinline__ void wait_lgkm0() { asm volatile("" ::: "memory"); __builtin_amdgcn_s_waitcnt(0xC07F); asm volatile("" ::: "memory"); }
__device__ __forceinline__ void bar() { asm volatile("" ::: "memory"); __builtin_amdgcn_s_barrier(); asm volatile("" ::: "memory"); }
template <int N, int STEP> __device__ __forceinline__ void wait_dyn(int n) {
  if constexpr (N <= 0) wait_vm<0>();
  else { if (n >= N) wait_vm<N>(); else wait_dyn<N - STEP, STEP>(n); }
}

template <int AUX> __device__ __forceinline__ void glds16(const char* g, LAS char* l) {
  __builtin_amdgcn_global_load_lds((const GAS void*)g, (LAS void*)l, 16, 0, AUX);
}
template <int AUX, int OFF> __device__ __forceinline__ void dma16(const char* sbase, unsigned voff) {
  if constexpr (AUX == 16) asm volatile("global_load_lds_dwordx4 %0, %1 offset:%2 sc1" :: "v"(voff), "s"(sbase), "n"(OFF) : "memory");
  else asm volatile("global_load_lds_dwordx4 %0, %1 offset:%2" :: "v"(voff), "s"(sbase), "n"(OFF) : "memory");
}
__device__ __forceinline__ void set_m0(unsigned v) { asm volatile("s_mov_b32 m0, %0\n\ts_nop 0" :: "s"(v) : "memory"); }
template <int AUX> __device__ __forceinline__ void touch(const char* g, LAS char* scratch) {
  __builtin_amdgcn_global_load_lds((const GAS void*)g, (LAS void*)scratch, 4, 0, AUX);
}
__device__ __forceinline__ void st8(void* p, u32x2 v) { __hip_atomic_store((u64*)p, __builtin_bit_cast(u64, v), RLX_AGENT); }
__device__ __forceinline__ u32x2 ld8(const void* p) { return __builtin_bit_cast(u32x2, __hip_atomic_load((u64*)p, RLX_AGENT)); }

constexpr int NL = 2;
constexpr int NWAVES = 8;
constexpr int LDS_RING = 136 * 1024;
constexpr int LDS_X = LDS_RING;
constexpr int LDS_TOTAL = 160 * 1024;
constexpr int LDS_MISC = LDS_TOTAL - 512;

template <int KID_, int BM_, int BN_, int BK_, int WM_, int WN_, int WK_, int NSA_, int NSB_, int NK_, int AMODE_, int BMODE_, int SWAP_, int NPRE_>
struct Cfg {
  static constexpr int KID = KID_, BM = BM_, BN = BN_, BK = BK_, WM = WM_, WN = WN_, WK = WK_, NSA = NSA_, NSB = NSB_, NK = NK_;
  static constexpr int AMODE = AMODE_, BMODE = BMODE_, SWAP = SWAP_, NPRE = NPRE_;
  static constexpr int NC = WM * WN * WK;
  static constexpr int MF = BM / WM / 16, NF = BN / WN / 16, KSW = BK / 32 / WK;
  static constexpr int ABYTES = BM * BK * 2, BBYTES = BN * BK * 2;
  static constexpr int PA = ABYTES / 1024, PB = BBYTES / 1024;
  static constexpr int GA = PA / NL, GB = PB / NL, G = GA + GB;
  static constexpr int RA = NSA * ABYTES, RB = NSB * BBYTES;
  static constexpr int NBAR = NK + (WK == 2 ? 2 : 1);
  static_assert(PA % NL == 0 && PB % NL == 0, "pieces");
  static_assert(RA + RB <= LDS_RING, "lds");
  static_assert(NSA <= NK && NSB <= NK, "stages");
  static_assert(KSW >= 1 && KSW <= 2, "ksw");
};

template <class C> struct Frags { bf16x8 a[C::KSW][C::MF]; bf16x8 b[C::KSW][C::NF]; };

template <class C, class Src, bool ISA>
struct Loader {
  static constexpr int G = ISA ? C::GA : C::GB, NS = ISA ? C::NSA : C::NSB, BYTES = ISA ? C::ABYTES : C::BBYTES;
  static constexpr int RING = NS * BYTES, MODE = ISA ? C::AMODE : C::BMODE, AUX = ISA ? Src::AUXA : Src::AUXB;
  const Src& src;
  int lw;
  unsigned lbase, cur;
  unsigned voff[G];
  __device__ __forceinline__ Loader(LAS char* lds, const Src& s, int lw_, int lane) : src(s), lw(lw_) {
    lbase = (unsigned)(size_t)lds + (ISA ? 0 : C::RA) + lw * (G * 1024);
#pragma unroll
    for (int i = 0; i < G; ++i) {
      const int p = (lw * G + i) * 64 + lane;
      unsigned v;
      if constexpr (MODE == 0) v = p * 16;
      else if constexpr (MODE == 1) { const int row = p >> 3, c = (p & 7) ^ ((row >> 1) & 7); v = row * (ISA ? Src::ldA : Src::ldB) + c * 16; }
      else { const int q = p % (C::BK * 8), k = q >> 3, c = (q & 7) ^ ((((k >> 3) & 1) << 2) | (((k >> 1) & 1) << 1)); v = k * Src::ldB + c * 16; }
      voff[i] = v - (i & 3) * 1024;
    }
  }
  template <int I> __device__ __forceinline__ void pieces(const char* gb, unsigned l) {
    if constexpr (I < G) {
      if constexpr ((I & 3) == 0) set_m0(l + I * 1024);
      dma16<AUX, (I & 3) * 1024>(gb, voff[I]);
      pieces<I + 1>(gb, l);
    }
  }
  __device__ __forceinline__ void issue(int k, unsigned buf) {
    pieces<0>(ISA ? src.baseA(k) : src.baseB(k, lw), lbase + buf);
  }
  __device__ __forceinline__ void fill(int s0, int s1) {
#pragma unroll 1
    for (int s = s0; s < s1; ++s) issue(s, s * BYTES);
  }
  template <int K, class Hook> __device__ __forceinline__ void tail(Hook hook) {
    if constexpr (K < C::NK - 1) {
      wait_vm<(C::NK - 2 - K) * G + Hook::extra(K)>();
      bar();
      hook.template at<K>();
      tail<K + 1>(hook);
    }
  }
  template <int N0, class Hook> __device__ __forceinline__ void run(Hook hook) {
    static_assert(N0 >= 1 && N0 <= NS && (NS - 1) * G <= 63, "ring");
    wait_vm<(N0 - 1) * G>();
    bar();
    fill(N0, NS);
    cur = 0;
#pragma unroll 1
    for (int k = 0; k < C::NK - NS; ++k) {
      wait_vm<(NS - 2) * G>();
      bar();
      issue(k + NS, cur);
      cur += BYTES; if (cur == RING) cur = 0;
    }
    hook.template at<C::NK - NS - 1>();
    tail<C::NK - NS>(hook);
  }
};
struct NoHook {
  static constexpr int extra(int) { return 0; }
  template <int K> __device__ __forceinline__ void at() {}
};

template <class C>
struct Consumer {
  LAS char* lds;
  int wid, lane, wm, wn, wk;
  unsigned rdA[2], rdB[4];

  __device__ __forceinline__ Consumer(LAS char* lds_, int wid_, int lane_) : lds(lds_), wid(wid_), lane(lane_) {
    wk = wid % C::WK; wn = (wid / C::WK) % C::WN; wm = wid / (C::WK * C::WN);
    const int fr = lane & 15, fq = lane >> 4;
    const int arow = wm * (C::BM / C::WM) + fr;
    const int ss0 = wk * C::KSW;
#pragma unroll
    for (int s = 0; s < 2; ++s) {
      const int ss = ss0 + s;
      if constexpr (C::AMODE == 0) rdA[s] = ((ss0 * 4 + fq) * C::BM + arow) * 16;
      else rdA[s] = arow * 128 + ((((ss & 1) * 4 + fq) ^ ((fr >> 1) & 7)) * 16);
    }
    if constexpr (C::BMODE <= 1) {
      const int brow = wn * (C::BN / C::WN) + fr;
#pragma unroll
      for (int s = 0; s < 2; ++s) {
        const int ss = ss0 + s;
        if constexpr (C::BMODE == 0) rdB[s] = C::RA + ((ss0 * 4 + fq) * C::BN + brow) * 16;
        else rdB[s] = C::RA + brow * 128 + ((((ss & 1) * 4 + fq) ^ ((fr >> 1) & 7)) * 16);
      }
    } else {
      const int g = lane >> 4, q = (lane >> 2) & 3, p = lane & 3;
#pragma unroll
      for (int j = 0; j < 4; ++j) {
        const int n = j;
        const int jj = (C::BMODE == 4) ? j : 2 * wn + (n & 1), mat = (C::BMODE == 4) ? 0 : (n >> 1);
        rdB[j] = C::RA + mat * (C::BK * 128) + (32 * ss0 + 8 * g + q) * 128 + ((((jj ^ (((g & 1) << 1) | (q >> 1))) << 1) | (p >> 1)) * 16) + 8 * (p & 1);
      }
    }
  }

  __device__ __forceinline__ void read(Frags<C>& f, unsigned bufa, unsigned bufb) {
    LAS char* a = lds + bufa; LAS char* b = lds + bufb;
#pragma unroll
    for (int s = 0; s < C::KSW; ++s) {
      if constexpr (C::BMODE == 0) {
#pragma unroll
        for (int n = 0; n < C::NF; ++n) f.b[s][n] = *(const LAS bf16x8*)(b + rdB[0] + s * (C::BN * 64) + n * 256);
      } else if constexpr (C::BMODE == 1) {
#pragma unroll
        for (int n = 0; n < C::NF; ++n) f.b[s][n] = *(const LAS bf16x8*)(b + rdB[s] + n * 2048);
      } else {
#pragma unroll
        for (int n = 0; n < C::NF; ++n) {
          bf16x4 lo = __builtin_amdgcn_ds_read_tr16_b64_v4i16((LAS bf16x4*)(b + rdB[n] + s * 4096));
          bf16x4 hi = __builtin_amdgcn_ds_read_tr16_b64_v4i16((LAS bf16x4*)(b + rdB[n] + s * 4096 + 512));
          f.b[s][n] = __builtin_shufflevector(lo, hi, 0, 1, 2, 3, 4, 5, 6, 7);
        }
      }
      if constexpr (C::AMODE == 0) {
#pragma unroll
        for (int m = 0; m < C::MF; ++m) f.a[s][m] = *(const LAS bf16x8*)(a + rdA[0] + s * (C::BM * 64) + m * 256);
      } else {
#pragma unroll
        for (int m = 0; m < C::MF; ++m) f.a[s][m] = *(const LAS bf16x8*)(a + rdA[s] + m * 2048);
      }
    }
  }

  __device__ __forceinline__ void mma(f32x4 (&acc)[C::MF][C::NF], const Frags<C>& f) {
#pragma unroll
    for (int s = 0; s < C::KSW; ++s)
#pragma unroll
      for (int m = 0; m < C::MF; ++m)
#pragma unroll
        for (int n = 0; n < C::NF; ++n)
          acc[m][n] = C::SWAP ? __builtin_amdgcn_mfma_f32_16x16x32_bf16(f.b[s][n], f.a[s][m], acc[m][n], 0, 0, 0)
                              : __builtin_amdgcn_mfma_f32_16x16x32_bf16(f.a[s][m], f.b[s][n], acc[m][n], 0, 0, 0);
  }
  static constexpr int NRD = C::KSW * (C::MF + (C::BMODE >= 2 ? 2 : 1) * C::NF);
  static constexpr int NMM = C::KSW * C::MF * C::NF;
  __device__ __forceinline__ void interleave() {
    constexpr int RPM = (NRD + NMM - 3) / (NMM - 2);
#pragma unroll
    for (int i = 0; i < NMM; ++i) {
      __builtin_amdgcn_sched_group_barrier(0x100, RPM, 0);
      __builtin_amdgcn_sched_group_barrier(0x008, 1, 0);
    }
  }
  unsigned na, nb;
  __device__ __forceinline__ void step(f32x4 (&acc)[C::MF][C::NF], Frags<C>& cur, Frags<C>& nxt) {
    na += C::ABYTES; if (na == C::RA) na = 0;
    nb += C::BBYTES; if (nb == C::RB) nb = 0;
    wait_lgkm0(); bar();
    __builtin_amdgcn_sched_barrier(0);
    read(nxt, na, nb);
    mma(acc, cur);
    interleave();
    __builtin_amdgcn_sched_barrier(0);
  }

  template <class Hook, class Hook2> __device__ __forceinline__ void run(f32x4 (&acc)[C::MF][C::NF], Hook hook, Hook2 hook2) {
    Frags<C> f0, f1;
#pragma unroll
    for (int m = 0; m < C::MF; ++m)
#pragma unroll
      for (int n = 0; n < C::NF; ++n) { acc[m][n] = (f32x4){0.f, 0.f, 0.f, 0.f}; asm volatile("" : "+v"(acc[m][n])); }
    bar();
    if (wid == 0) { STAMP(C::KID, 1); CSTAMP(C::KID, 8); }
    hook();
    read(f0, 0, 0);
    na = 0; nb = 0;
    if (wid == 0) STAMP(C::KID, 4);
#pragma unroll 1
    for (int k = 0; k + 2 < C::NK; k += 2) {
      step(acc, f0, f1);
      step(acc, f1, f0);
    }
    if (wid == 0) STAMP(C::KID, 5);
    if constexpr (C::NK % 2 == 0) {
      step(acc, f0, f1);
      wait_lgkm0(); bar();
      hook2();
      mma(acc, f1);
    } else {
      wait_lgkm0(); bar();
      mma(acc, f0);
    }
    if (wid == 0) { STAMP(C::KID, 2); CSTAMP(C::KID, 9); }
  }

  static constexpr bool SPLM = (C::MF % 2 == 0);
  static constexpr int RM = SPLM ? C::MF / 2 : C::MF, RN = SPLM ? C::NF : C::NF / 2;
  __device__ __forceinline__ void reduce_k(f32x4 (&acc)[C::MF][C::NF], f32x4 (&red)[RM][RN]) {
    static_assert(C::WK == 2, "");
    static_assert(LDS_X + C::NC * RM * RN * 1024 <= LDS_MISC || RM * RN == 6, "");
    LAS f32x4* x = (LAS f32x4*)(lds + LDS_X - (RM * RN == 6 ? 1024 : 0));
#pragma unroll
    for (int m = 0; m < RM; ++m)
#pragma unroll
      for (int n = 0; n < RN; ++n) {
        const f32x4 lo = acc[m][n], hi = SPLM ? acc[m + RM][n] : acc[m][n + RN];
        x[(wid * RM * RN + m * RN + n) * 64 + lane] = wk ? lo : hi;
        red[m][n] = wk ? hi : lo;
      }
    wait_lgkm0(); bar();
#pragma unroll
    for (int m = 0; m < RM; ++m)
#pragma unroll
      for (int n = 0; n < RN; ++n) red[m][n] += x[((wid ^ 1) * RM * RN + m * RN + n) * 64 + lane];
  }
};

constexpr int FLE = 0;
constexpr int FL0 = 32;
constexpr int FL1 = FL0 + 2048;
constexpr int FL2 = FL1 + 16 * 16;
constexpr int FL3 = FL2 + 8 * 32;
constexpr int FLW = FL3 + 8 * 32;
constexpr int FLC = FLW + 48;

__device__ __forceinline__ void publish(LAS unsigned* arr, unsigned nwaves, u64* flag, u64 tag, int lane, int kid) {
  if (threadIdx.x < 64) STAMP(kid, 7);
  asm volatile("" ::: "memory");
  if (lane == 0) __hip_atomic_fetch_add(arr + 8, 1u, RLX_WG);
  wait_vm<0>();
  if (lane == 0) {
    const unsigned old = __hip_atomic_fetch_add(arr, 1u, RLX_WG);
    if (old == nwaves - 1) {
      __hip_atomic_store(flag, tag, RLX_AGENT);
      STAMP(kid, 6);
    }
  }
}
__device__ __forceinline__ void lds_wait(LAS unsigned* w, unsigned want) {
  const unsigned a = (unsigned)(size_t)w;
#pragma unroll 1
  for (unsigned i = 0; i < (1u << 24); ++i) {
    unsigned v;
    asm volatile("ds_read_b32 %0, %1\n\ts_waitcnt lgkmcnt(0)" : "=v"(v) : "v"(a) : "memory");
    if (__builtin_amdgcn_readfirstlane(v) >= want) break;
    __builtin_amdgcn_s_sleep(1);
  }
}
template <int SLEEP, int NW>
__device__ __forceinline__ void await(const u64* word, bool need, u64 tag, int lw, LAS unsigned* go, unsigned epoch) {
  if (lw == 0) {
    const u64 t0 = wall_clock64();
    if (PDELAY) __builtin_amdgcn_s_sleep(PDELAY);
    for (;;) {
      bool ok = true;
#pragma unroll
      for (int i = 0; i < NW; ++i) ok &= __hip_atomic_load(word + i * 64, RLX_AGENT) == tag;
      if (__all(!need || ok)) break;
      if (wall_clock64() - t0 > 2000000ull) break;
      if (SLEEP > PSLEEP && __any(need && ok)) __builtin_amdgcn_s_sleep(PSLEEP); else __builtin_amdgcn_s_sleep(SLEEP);
    }
    __hip_atomic_store(go, epoch, RLX_WG);
  } else {
    lds_wait(go, epoch);
  }
  __builtin_amdgcn_fence(__ATOMIC_ACQUIRE, "wavefront");
}
#define P1_PRE 1
#define PL1 1
#define PL2 1
#define PL3 1

#define PRE1 2
#define PRE2 6
#define PRE3 5
#define PRE4 6
using C1 = Cfg<1, 80, 128, 64, 1, 4, 1, 4, 6, 32, 0, 1, 0, PRE1>;
#define P2A 4
#define P2B 6
using C2 = Cfg<2, 64, 80, 64, 2, 1, 2, P2A, P2B, 20, 0, 1, 1, PRE2>;
#define P3A 4
#define P3B 5
using C3 = Cfg<3, 256, 128, 32, 2, 2, 1, P3A, P3B, 20, 0, 3, 1, PRE3>;
#define P4A 4
#define P4B 6
using C4 = Cfg<4, 64, 80, 64, 2, 1, 2, P4A, P4B, 27, 0, 0, 1, PRE4>;
#define ROT 15
struct Src1 {
  const char* a; const char* b; int o, c;
  static constexpr int ldA = 0, ldB = S_ * 2, AUXA = AUXH, AUXB = 0, rows = 80;
  __device__ __forceinline__ int kt(int k) const { const int s = (k + o) & 31; return (ROT & 1) ? ((s & 24) | ((s + c) & 7)) : k; }
  __device__ __forceinline__ const char* baseA(int k) const { return a + kt(k) * (80 * 128); }
  __device__ __forceinline__ const char* baseB(int k, int) const { return b + kt(k) * 128; }
};
struct Src2 {
  const char* a; const char* bre; const char* bim; int rot;
  static constexpr int ldA = 0, ldB = D_ * 2, AUXA = AUXH, AUXB = 0, rows = 64;
  __device__ __forceinline__ int kt(int k) const { const int s = k + ((ROT & 2) ? rot : 0); return s >= 20 ? s - 20 : s; }
  __device__ __forceinline__ const char* baseA(int k) const { return a + kt(k) * (64 * 128); }
  __device__ __forceinline__ const char* baseB(int k, int) const { const int t = kt(k); return t < 10 ? bre + t * 128 : bim + (t - 10) * 128; }
};
struct Src3 {
  const char* a; const char* bg; const char* bu; int rot;
  static constexpr int ldA = 0, ldB = F_ * 2, AUXA = AUXH, AUXB = 0, rows = 256;
  __device__ __forceinline__ int kt(int k) const { const int s = k + ((ROT & 4) ? rot : 0); return s >= 20 ? s - 20 : s; }
  __device__ __forceinline__ const char* baseA(int k) const { return a + kt(k) * (256 * 64); }
  __device__ __forceinline__ const char* baseB(int k, int up) const { return (up ? bu : bg) + kt(k) * (32 * F_ * 2); }
};
struct Src4 {
  const char* a; const char* b;
  static constexpr int ldA = 0, ldB = 0, AUXA = AUXH, AUXB = AUXH, rows = 64;
  __device__ __forceinline__ const char* baseA(int k) const { return a + k * (64 * 128); }
  __device__ __forceinline__ const char* baseB(int k, int) const { return b + k * (80 * 128); }
};

struct Params {
  const u16* x; const u16* sre; const u16* sim; const u16* hre; const u16* him; const u16* wg; const u16* wu; const u16* wd;
  u16* xnI; u16* pqI; u16* hI; u16* hhI; u16* wdI; f32x2* stats; float* cs; u64* flags; u16* out;
};

template <int CTRL, int RMASK> __device__ __forceinline__ float dpp_add(float v) {
  return v + __int_as_float(__builtin_amdgcn_update_dpp(0, __float_as_int(v), CTRL, RMASK, 0xf, true));
}
__device__ __forceinline__ float wave_sum(float v) {
  v = dpp_add<0x111, 0xf>(v); v = dpp_add<0x112, 0xf>(v); v = dpp_add<0x114, 0xf>(v); v = dpp_add<0x118, 0xf>(v);
  v = dpp_add<0x142, 0xa>(v); v = dpp_add<0x143, 0xc>(v);
  return __int_as_float(__builtin_amdgcn_readlane(__float_as_int(v), 63));
}
__device__ __forceinline__ float sum_rows(float v) {
  const auto a = __builtin_amdgcn_permlane16_swap(__float_as_uint(v), __float_as_uint(v), false, false);
  v = __uint_as_float(a[0]) + __uint_as_float(a[1]);
  const auto b = __builtin_amdgcn_permlane32_swap(__float_as_uint(v), __float_as_uint(v), false, false);
  return __uint_as_float(b[0]) + __uint_as_float(b[1]);
}
__device__ __forceinline__ u32x4 col8(LAS char* tile, int cb, int lane) {
  LAS char* a = tile + ((lane >> 2) & 3) * 1296 + (cb * 64 + (lane >> 4) * 16 + (lane & 3) * 4) * 2;
  const bf16x4 lo = __builtin_amdgcn_ds_read_tr16_b64_v4i16((LAS bf16x4*)a);
  const bf16x4 hi = __builtin_amdgcn_ds_read_tr16_b64_v4i16((LAS bf16x4*)(a + 4 * 1296));
  return __builtin_bit_cast(u32x4, __builtin_shufflevector(lo, hi, 0, 1, 2, 3, 4, 5, 6, 7));
}
template <int N> __device__ __forceinline__ void bars() {
#pragma unroll 1
  for (int i = 0; i < N; ++i) bar();
}

struct Hook3 {
  const Params& p; LAS f32x4* rs; LAS float* csl; LAS unsigned* arr; u64 tag; int mt3, j3, lw, lane;
  f32x2 (&pv)[2][8]; u64& fc; float& cv;
  static constexpr int K0 = C3::NK - C3::NSB - 1;
  static constexpr int extra(int k) { return k <= K0 + 2 ? 17 : 1; }
  template <int K> __device__ __forceinline__ void at() {
    if constexpr (K == K0) {
      fc = __hip_atomic_load(p.flags + FLC + j3 * 16, RLX_AGENT);
#pragma unroll
      for (int h = 0; h < 2; ++h)
#pragma unroll
        for (int i = 0; i < 8; ++i) pv[h][i] = __builtin_bit_cast(f32x2, ld8(p.stats + i * S_ + mt3 * 256 + lw * 128 + h * 64 + lane));
    } else if constexpr (K == K0 + 2) {
      if (!__all(fc == tag)) await<PSLEEP, 1>(p.flags + FLC + j3 * 16, true, tag, 0, arr + 11, 1u);
      __builtin_amdgcn_fence(__ATOMIC_ACQUIRE, "wavefront");
      cv = __hip_atomic_load(p.cs + j3 * 128 + lw * 64 + lane, RLX_AGENT);
#pragma unroll
      for (int h = 0; h < 2; ++h) {
        float mean = 0.f, q = 0.f, dd = 0.f;
#pragma unroll
        for (int i = 0; i < 8; ++i) { mean += pv[h][i][0]; q += pv[h][i][1]; }
        mean *= 0.125f;
#pragma unroll
        for (int i = 0; i < 8; ++i) { const float d = pv[h][i][0] - mean; dd += d * d; }
        q += 80.0f * dd;
        const float den = rbf(__builtin_amdgcn_sqrtf(rbf(rbf(q * (1.0f / D_)) + 1e-5f)));
        rs[lw * 128 + h * 64 + lane] = (f32x4){-rbf(mean), den, -1.44269504089f * __builtin_amdgcn_rcpf(den), 0.f};
      }
    } else if constexpr (K == C3::NK - 2) {
      csl[lw * 64 + lane] = cv;
      wait_lgkm0();
    }
  }
};

__global__ __launch_bounds__(NWAVES * 64) void mega(const Params p) {
  __shared__ __attribute__((aligned(1024))) char smem_[LDS_TOTAL];
  LAS char* lds = (LAS char*)smem_;
  const int wid = __builtin_amdgcn_readfirstlane(threadIdx.x >> 6), lane = threadIdx.x & 63;
  const int bid = blockIdx.x, g = bid & 7, idx = bid >> 3;
  LAS unsigned* arr = (LAS unsigned*)(lds + LDS_MISC + 256);
  if (wid == 0) STAMP(0, 0);
  const u16* xr = p.x + (bid * 8 + (wid & 7)) * D_;
  const bf16x8 v0 = *(const bf16x8*)(xr + lane * 8);
  const bf16x8 v1 = *(const bf16x8*)(xr + 512 + (lane & 15) * 8);
  const u64 ep = __hip_atomic_load(p.flags + FLE, RLX_AGENT);
  if (threadIdx.x < 16) arr[threadIdx.x] = 0u;
  wait_lgkm0(); bar();
  const int j1 = idx & 7, mq = idx >> 3, isQ = mq >> 1, t1 = g * 256 + (mq & 1) * 128;
  const int j2 = idx & 7, mt2 = g * 4 + (idx >> 3);
  const int mt3 = (g & 6) + (idx & 1), j3 = (g & 1) * 14 + (idx >> 1);
  const bool on3 = idx < 28 && j3 < 27;
  LAS f32x4* rs = (LAS f32x4*)(lds + LDS_X);
  LAS float* csl = (LAS float*)(lds + LDS_X + 4096);

  {
    float f[16];
#pragma unroll
    for (int i = 0; i < 8; ++i) { f[i] = bf2f((u16)v0[i]); f[8 + i] = bf2f((u16)v1[i]); }
    const float piv = __int_as_float(__builtin_amdgcn_readfirstlane(__float_as_int(f[0])));
    asm volatile("" :: "v"(f[8]) : "memory");
    if (lane == 0) __hip_atomic_fetch_add(arr + 4, 1u, RLX_WG);
    if (wid == 0) STAMP(0, 4);
    float s0 = 0.f, s1 = 0.f, q0 = 0.f, q1 = 0.f;
#pragma unroll
    for (int i = 0; i < 8; ++i) { const float d = f[i] - piv, e = f[8 + i] - piv; s0 += d; q0 += d * d; s1 += e; q1 += e * e; }
    const float sd = wave_sum(s0 + (lane < 16 ? s1 : 0.f)) * (1.0f / D_);
    const float qd = wave_sum(q0 + (lane < 16 ? q1 : 0.f)) * (1.0f / D_);
    const float mb = rbf(piv + sd), vb = rbf(fmaxf(qd - sd * sd, 0.f));
    const float rden = __builtin_amdgcn_rcpf(rbf(__builtin_amdgcn_sqrtf(rbf(vb + 1e-5f))));
    u32x4 w0, w1;
#pragma unroll
    for (int i = 0; i < 4; ++i) {
      w0[i] = pk2(rbf(f[2 * i] - mb) * rden, rbf(f[2 * i + 1] - mb) * rden);
      w1[i] = pk2(rbf(f[8 + 2 * i] - mb) * rden, rbf(f[9 + 2 * i] - mb) * rden);
    }
    LAS char* tb = lds + LDS_X;
    *(LAS u32x4*)(tb + wid * 1296 + lane * 16) = w0;
    if (lane < 16) *(LAS u32x4*)(tb + wid * 1296 + 1024 + lane * 16) = w1;
    wait_lgkm0();
    if (lane == 0) __hip_atomic_fetch_add(arr + 7, 1u, RLX_WG);
  }
  const u64 tag = (((u64)(unsigned)__builtin_amdgcn_readfirstlane((unsigned)(ep >> 32)) << 32) | (unsigned)__builtin_amdgcn_readfirstlane((unsigned)ep)) + 1ull;
  {
    LAS char* tb = lds + LDS_X;
    lds_wait(arr + 7, 8);
    auto rsrc = __builtin_amdgcn_make_buffer_rsrc(p.xnI, 0, S_ * D_ * 2, 0x00020000);
#pragma unroll
    for (int h = 0; h < 2; ++h) {
      const int cb = h * 8 + wid;
      if (cb < 10) {
        const int d = cb * 64 + lane, j = d / 80, n = d % 80;
        __builtin_amdgcn_raw_buffer_store_b128(col8(tb, cb, lane), rsrc, ((j * 256 + bid) * 80 + n) * 16, 0, 16);
      }
    }
    if (wid == 0) STAMP(0, 7);
    wait_vm<0>();
    unsigned old = 0;
    if (lane == 0) old = __hip_atomic_fetch_add(arr + 5, 1u, RLX_WG);
    if (__builtin_amdgcn_readfirstlane(old) == 7u && lane < 8) __hip_atomic_store(p.flags + FL0 + lane * 256 + bid, tag, RLX_AGENT);
    if (wid == 0) STAMP(0, 3);
  }
  const Src1 s1{(const char*)(p.xnI + (size_t)j1 * 256 * 80 * 8), (const char*)((isQ ? p.sim : p.sre) + (size_t)t1 * S_), (g + 2 * mq) & 7, j1};
  const Src2 s2{(const char*)(p.pqI + (size_t)mt2 * 160 * 64 * 8), (const char*)(p.hre + (size_t)j2 * 80 * D_), (const char*)(p.him + (size_t)j2 * 80 * D_), (mt2 * 5) >> 3};
  const Src3 s3{(const char*)(p.hI + (size_t)mt3 * 80 * 256 * 8), (const char*)(p.wg + j3 * 64), (const char*)(p.wu + j3 * 64), (mt3 * 5) >> 1};
  const Src4 s4{(const char*)(p.hhI + (size_t)mt2 * 216 * 64 * 8), (const char*)(p.wdI + (size_t)j2 * 216 * 80 * 8)};
  if (wid >= 6) {
    const int lw = wid - 6;
    {
      Loader<C1, Src1, true> L(lds, s1, lw, lane);
      lds_wait(arr + 5, 8);
      await<PSLEEP, 4>(p.flags + FL0 + g * 256 + lane, true, tag, lw, arr + 3, 1u);
      if (bid == 0 && lw == 0 && lane == 0) __hip_atomic_store(p.flags + FLE, tag, RLX_AGENT);
      if (lw == 0) STAMP(1, 0);
      wait_vm<0>();
      L.fill(0, NRAMP); L.run<NRAMP>(NoHook{});
      bar();
    }
    {
      Loader<C2, Src2, true> L(lds, s2, lw, lane);
      lds_wait(arr + 0, C1::NC);
      await<PSLEEP, 1>(p.flags + FL1 + (mt2 >> 1) * 16 + (lane & 15), true, tag, lw, arr + 3, 2u);
      if (lw == 0) STAMP(2, 0);
      wait_vm<0>();
      L.fill(0, NRAMP); L.run<NRAMP>(NoHook{});
      bar(); bar();
    }
    if (on3) {
      Loader<C3, Src3, true> L(lds, s3, lw, lane);
      lds_wait(arr + 1, C2::NC);
      await<PSLEEP, 1>(p.flags + FL2 + mt3 * 32 + (lane & 31), true, tag, lw, arr + 3, 3u);
      if (lw == 0) STAMP(3, 0);
      wait_vm<0>();
      L.fill(0, NRAMP); L.run<NRAMP>(NoHook{});
      bar();
    }
    {
      Loader<C4, Src4, true> L(lds, s4, lw, lane);
      lds_wait(arr + 2, 4);
      if (on3) await<PSLEEP, 1>(p.flags + FL3 + g * 32 + (lane & 31), (lane & 31) < 27, tag, lw, arr + 3, 4u);
      else await<ISLEEP, 1>(p.flags + FL3 + g * 32 + (lane & 31), (lane & 31) < 27, tag, lw, arr + 3, 4u);
      if (lw == 0) STAMP(4, 0);
      wait_vm<0>();
      L.fill(0, NRAMP); L.run<NRAMP>(NoHook{});
      bar(); bar();
    }
    return;
  }
  if (wid >= 4) {
    const int lw = wid - 4;
    {
      Loader<C1, Src1, false> L(lds, s1, lw, lane);
      lds_wait(arr + 5, 8);
      L.fill(0, C1::NPRE); L.run<C1::NPRE>(NoHook{});
      bar();
    }
    {
      Loader<C2, Src2, false> L(lds, s2, lw, lane);
      lds_wait(arr + 0, C1::NC);
      L.fill(0, C2::NPRE); L.run<C2::NPRE>(NoHook{});
      bar(); bar();
    }
    if (on3) {
      Loader<C3, Src3, false> L(lds, s3, lw, lane);
      lds_wait(arr + 1, C2::NC);
      static_assert(Hook3::K0 + 2 < C3::NK - 2, "");
      f32x2 pv[2][8]; u64 fc; float cv;
      L.fill(0, C3::NPRE); L.run<C3::NPRE>(Hook3{p, rs, csl, arr, tag, mt3, j3, lw, lane, pv, fc, cv});
      bar();
    }
    {
      Loader<C4, Src4, false> L(lds, s4, lw, lane);
      await<ISLEEP, 1>(p.flags + FLW + min(lane, 39), true, tag, lw, arr + 13, 1u);
      lds_wait(arr + 2, 4);
      wait_vm<0>();
      L.fill(0, C4::NPRE); L.run<C4::NPRE>(NoHook{});
      bar(); bar();
    }
    return;
  }

#define FRESH_LANE int lane_ = threadIdx.x & 63; asm volatile("" : "+v"(lane_)); const int lane = lane_, fr = lane & 15, fq = lane >> 4; (void)fr; (void)fq;
  {
    FRESH_LANE
    Consumer<C1> L(lds, wid, lane);
    f32x4 acc[C1::MF][C1::NF];
    L.run(acc, [] {}, [] {});
    const float sg = isQ ? -1.f : 1.f;
#pragma unroll
    for (int n = 0; n < 2; ++n) {
      const int t = t1 + L.wn * 32 + n * 16 + fr, mt = t >> 6, r = t & 63;
#pragma unroll
      for (int m = 0; m < 5; ++m) {
        const int koct = isQ * 80 + j1 * 10 + m * 2 + (fq >> 1);
        u32x2 w; w[0] = pk2(sg * acc[m][n][0], sg * acc[m][n][1]); w[1] = pk2(sg * acc[m][n][2], sg * acc[m][n][3]);
        st8(p.pqI + (((size_t)mt * 160 + koct) * 64 + r) * 8 + (fq & 1) * 4, w);
      }
    }
    publish(arr + 0, C1::NC, p.flags + FL1 + (t1 >> 7) * 16 + isQ * 8 + j1, tag, lane, 1);
    if (wid == 0) STAMP(1, 3);
  }
  {
    FRESH_LANE
    Consumer<C2> L(lds, wid, lane);
    f32x4 acc[C2::MF][C2::NF];
    const int t = mt2 * 64 + L.wm * 32 + L.wk * 16 + fr;
    bf16x4 xv[C2::NF];
#pragma unroll
    for (int n = 0; n < C2::NF; ++n) xv[n] = *(const bf16x4*)(p.x + (size_t)t * D_ + j2 * 80 + n * 16 + fq * 4);
#pragma unroll
    for (int n = 0; n < C2::NF; ++n) asm volatile("" : "+v"(xv[n]));
    L.run(acc, [] {}, [] {});
    f32x4 red[1][5];
    L.reduce_k(acc, red);
    float hv[C2::NF][4];
    float s = 0.f;
#pragma unroll
    for (int n = 0; n < C2::NF; ++n) {
      u32x2 w;
      w[0] = pk2(red[0][n][0] + bf2f((u16)xv[n][0]), red[0][n][1] + bf2f((u16)xv[n][1]));
      w[1] = pk2(red[0][n][2] + bf2f((u16)xv[n][2]), red[0][n][3] + bf2f((u16)xv[n][3]));
      hv[n][0] = __uint_as_float(w[0] << 16); hv[n][1] = __uint_as_float(w[0] & 0xffff0000u);
      hv[n][2] = __uint_as_float(w[1] << 16); hv[n][3] = __uint_as_float(w[1] & 0xffff0000u);
      s += (hv[n][0] + hv[n][1]) + (hv[n][2] + hv[n][3]);
      const int koct = j2 * 10 + n * 2 + (fq >> 1);
      st8(p.hI + (((size_t)(t >> 8) * 80 + koct) * 256 + (t & 255)) * 8 + (fq & 1) * 4, w);
    }
    const float mean = sum_rows(s) * (1.0f / 80.0f);
    float q = 0.f;
#pragma unroll
    for (int n = 0; n < C2::NF; ++n)
#pragma unroll
      for (int r = 0; r < 4; ++r) { const float d = hv[n][r] - mean; q += d * d; }
    q = sum_rows(q);
    if (fq == 0) st8(p.stats + j2 * S_ + t, __builtin_bit_cast(u32x2, (f32x2){mean, q}));
    publish(arr + 1, C2::NC, p.flags + FL2 + g * 32 + idx, tag, lane, 2);
    if (wid == 0) STAMP(2, 3);
  }
  if (on3) {
    FRESH_LANE
    Consumer<C3> L(lds, wid, lane);
    f32x4 acc[C3::MF][C3::NF];
    f32x4 st[C3::MF], cg[2], cu[2];
    L.run(acc, [] {}, [&] {
#pragma unroll
      for (int m = 0; m < C3::MF; ++m) st[m] = rs[L.wm * 128 + m * 16 + fr];
#pragma unroll
      for (int i = 0; i < 2; ++i) {
        const int fl = L.wn * 32 + i * 16 + fq * 4;
        cg[i] = *(const LAS f32x4*)(csl + fl); cu[i] = *(const LAS f32x4*)(csl + 64 + fl);
      }
    });
    u16* hb = p.hhI + (((size_t)(mt3 * 4 + L.wm * 2) * 216 + j3 * 8 + L.wn * 4 + (fq >> 1)) * 64 + fr) * 8 + (fq & 1) * 4;
#pragma unroll
    for (int m = 0; m < C3::MF; ++m)
#pragma unroll
      for (int i = 0; i < 2; ++i) {
        const f32x4 s0 = {st[m][0], st[m][0], st[m][0], st[m][0]}, s1 = {st[m][1], st[m][1], st[m][1], st[m][1]};
        const f32x4 x = __builtin_elementwise_fma(cg[i], s0, acc[m][i]) * st[m][2];
        f32x4 e;
#pragma unroll
        for (int r = 0; r < 4; ++r) e[r] = __builtin_amdgcn_exp2f(x[r]);
        const f32x4 d = __builtin_elementwise_fma(e, s1, s1);
        f32x4 rc;
#pragma unroll
        for (int r = 0; r < 4; ++r) rc[r] = __builtin_amdgcn_rcpf(d[r]);
        const f32x4 o = __builtin_elementwise_fma(cu[i], s0, acc[m][i + 2]) * rc;
        u32x2 w; w[0] = pk2(o[0], o[1]); w[1] = pk2(o[2], o[3]);
        st8(hb + (m >> 2) * (216 * 64 * 8) + i * (2 * 64 * 8) + (m & 3) * 128, w);
      }
    publish(arr + 2, C3::NC, p.flags + FL3 + mt3 * 32 + j3, tag, lane, 3);
    if (wid == 0) STAMP(3, 3);
  } else {
    FRESH_LANE
    const int id = idx >= 28 ? g * 4 + idx - 28 : 32 + (g >> 1) * 2 + idx - 26;
    auto rsrc = __builtin_amdgcn_make_buffer_rsrc(p.wdI, 0, F_ * D_ * 2, 0x00020000);
    lds_wait(arr + 1, C2::NC);
    if (id < 27) {
      const u16* w = ((wid & 2) ? p.wu : p.wg) + (size_t)((wid & 1) * 320 + (lane >> 3)) * F_ + id * 64 + (lane & 7) * 8;
      f32x2 a[4] = {{0.f, 0.f}, {0.f, 0.f}, {0.f, 0.f}, {0.f, 0.f}};
#pragma unroll 1
      for (int b = 0; b < 2; ++b) {
        u32x4 v[20];
#pragma unroll
        for (int i = 0; i < 20; ++i) v[i] = *(const u32x4*)(w + (size_t)(b * 20 + i) * 8 * F_);
#pragma unroll
        for (int i = 0; i < 20; ++i)
#pragma unroll
          for (int e = 0; e < 4; ++e) a[e] += (f32x2){__uint_as_float(v[i][e] << 16), __uint_as_float(v[i][e] & 0xffff0000u)};
      }
      LAS float* sf = (LAS float*)(lds + LDS_X);
#pragma unroll
      for (int e = 0; e < 4; ++e)
#pragma unroll
        for (int c = 0; c < 2; ++c) { float t = sum_rows(a[e][c]); t += __shfl_xor(t, 8); a[e][c] = t; }
      if (lane < 8) {
        *(LAS f32x4*)(sf + wid * 64 + lane * 8) = (f32x4){a[0][0], a[0][1], a[1][0], a[1][1]};
        *(LAS f32x4*)(sf + wid * 64 + lane * 8 + 4) = (f32x4){a[2][0], a[2][1], a[3][0], a[3][1]};
      }
      wait_lgkm0();
      unsigned old = 0;
      if (lane == 0) old = __hip_atomic_fetch_add(arr + 12, 1u, RLX_WG);
      if (__builtin_amdgcn_readfirstlane(old) == 3u) {
        __hip_atomic_store(p.cs + id * 128 + lane, sf[lane] + sf[64 + lane], RLX_AGENT);
        __hip_atomic_store(p.cs + id * 128 + 64 + lane, sf[128 + lane] + sf[192 + lane], RLX_AGENT);
        wait_vm<0>();
        if (lane == 0) __hip_atomic_store(p.flags + FLC + id * 16, tag, RLX_AGENT);
        STAMP(0, 5);
      }
    }
    const int k0 = id < 27 ? id * 3 : 81 + (id - 27) * 11, nk = id < 27 ? 3 : min(11, 216 - k0);
#pragma unroll 1
    for (int i = 0; i < 2 * nk; ++i) {
      const int row = (i & 1) * 4 + wid;
      const char* wr = (const char*)(p.wd + (size_t)((k0 + (i >> 1)) * 8 + row) * D_);
      set_m0((unsigned)(size_t)lds + (i >> 1) * 10368 + row * 1296);
      dma16<0, 0>(wr, lane * 16);
      if (lane < 16) dma16<0, 1024>(wr, lane * 16);
    }
    wait_vm<0>();
    if (lane == 0) __hip_atomic_fetch_add(arr + 6, 1u, RLX_WG);
    lds_wait(arr + 6, 4);
#pragma unroll 1
    for (int i = 0; i < nk; ++i) {
#pragma unroll
      for (int h = 0; h < 3; ++h) {
        const int cb = h * 4 + wid;
        if (cb < 10) {
          const int d = cb * 64 + lane, j = d / 80, n = d % 80;
          __builtin_amdgcn_raw_buffer_store_b128(col8(lds + i * 10368, cb, lane), rsrc, ((j * 216 + k0 + i) * 80 + n) * 16, 0, 16);
        }
      }
    }
    publish(arr + 2, 4, p.flags + FLW + id, tag, lane, 0);
  }
  {
    FRESH_LANE
    Consumer<C4> L(lds, wid, lane);
    f32x4 acc[C4::MF][C4::NF];
    const int t = mt2 * 64 + L.wm * 32 + L.wk * 16 + fr;
    u32x2 hv[5];
    L.run(acc, [&] {
#pragma unroll
      for (int n = 0; n < 5; ++n)
        hv[n] = ld8(p.hI + (((size_t)(t >> 8) * 80 + j2 * 10 + n * 2 + (fq >> 1)) * 256 + (t & 255)) * 8 + (fq & 1) * 4);
    }, [] {});
    f32x4 red[1][5];
    L.reduce_k(acc, red);
#pragma unroll
    for (int n = 0; n < 5; ++n) {
      u32x2 w;
      w[0] = pk2(red[0][n][0] + __uint_as_float(hv[n][0] << 16), red[0][n][1] + __uint_as_float(hv[n][0] & 0xffff0000u));
      w[1] = pk2(red[0][n][2] + __uint_as_float(hv[n][1] << 16), red[0][n][3] + __uint_as_float(hv[n][1] & 0xffff0000u));
      *(u32x2*)(p.out + (size_t)t * D_ + j2 * 80 + n * 16 + fq * 4) = w;
    }
    if (wid == 0) { wait_vm<0>(); STAMP(4, 3); }
  }
}

extern "C" void kernel_launch(void* const* d_in, const int* in_sizes, int n_in, void* d_out, int out_size,
                              void* d_ws, size_t ws_size, hipStream_t stream) {
  char* ws = (char*)d_ws;
  Params p;
  p.x = (const u16*)d_in[0]; p.sre = (const u16*)d_in[1]; p.sim = (const u16*)d_in[2];
  p.hre = (const u16*)d_in[3]; p.him = (const u16*)d_in[4];
  p.wg = (const u16*)d_in[6]; p.wu = (const u16*)d_in[7]; p.wd = (const u16*)d_in[8];
  p.flags = (u64*)ws;
  p.cs = (float*)(ws + 65536);
  p.stats = (f32x2*)(ws + 131072);
  p.xnI = (u16*)(ws + (1u << 20));
  p.pqI = (u16*)(ws + (4u << 20));
  p.hI = (u16*)(ws + (10u << 20));
  p.hhI = (u16*)(ws + (13u << 20));
  p.wdI = (u16*)(ws + (21u << 20));
  p.out = (u16*)d_out;
  mega<<<256, NWAVES * 64, 0, stream>>>(p);
}
```

```cpp
#include <hip/hip_runtime.h>
#include <hip/hip_bf16.h>
#include <type_traits>
#define PSLEEP 4
#define ISLEEP 16
#define NRAMP 3
#define TW 0
#define XNT 0
#define PL0 1
#define PDELAY 40
#define ALIAS 0
#define AUXH 16

typedef unsigned short u16;
typedef unsigned long long u64;
typedef short bf16x8 __attribute__((ext_vector_type(8)));
typedef short bf16x4 __attribute__((ext_vector_type(4)));
typedef float f32x4 __attribute__((ext_vector_type(4)));
typedef float f32x2 __attribute__((ext_vector_type(2)));
typedef unsigned u32x2 __attribute__((ext_vector_type(2)));
typedef unsigned u32x4 __attribute__((ext_vector_type(4)));
#define LAS __attribute__((address_space(3)))
#define GAS __attribute__((address_space(1)))
#define RLX_AGENT __ATOMIC_RELAXED, __HIP_MEMORY_SCOPE_AGENT
#define RLX_WG __ATOMIC_RELAXED, __HIP_MEMORY_SCOPE_WORKGROUP

constexpr int S_ = 2048, D_ = 640, F_ = 1728;

__device__ __forceinline__ float bf2f(u16 v) { return __uint_as_float(((unsigned)v) << 16); }
typedef __bf16 bf16x2n __attribute__((ext_vector_type(2)));
__device__ __forceinline__ unsigned pk2(float lo, float hi) {
  return __builtin_bit_cast(unsigned, __builtin_convertvector((f32x2){lo, hi}, bf16x2n));
}
__device__ __forceinline__ float rbf(float v) { return __uint_as_float(pk2(v, v) << 16); }

#define CSTAMP(kid, i)
#define STAMP(kid, i)
template <int N> __device__ __forceinline__ void wait_vm() { asm volatile("s_waitcnt vmcnt(%0)" ::"n"(N) : "memory"); }
__device__ __forceinline__ void wait_lgkm0() { asm volatile("" ::: "memory"); __builtin_amdgcn_s_waitcnt(0xC07F); asm volatile("" ::: "memory"); }
__device__ __forceinline__ void bar() { asm volatile("" ::: "memory"); __builtin_amdgcn_s_barrier(); asm volatile("" ::: "memory"); }
template <int N, int STEP> __device__ __forceinline__ void wait_dyn(int n) {
  if constexpr (N <= 0) wait_vm<0>();
  else { if (n >= N) wait_vm<N>(); else wait_dyn<N - STEP, STEP>(n); }
}

template <int AUX> __device__ __forceinline__ void glds16(const char* g, LAS char* l) {
  __builtin_amdgcn_global_load_lds((const GAS void*)g, (LAS void*)l, 16, 0, AUX);
}
template <int AUX, int OFF> __device__ __forceinline__ void dma16(const char* sbase, unsigned voff) {
  if constexpr (AUX == 16) asm volatile("global_load_lds_dwordx4 %0, %1 offset:%2 sc1" :: "v"(voff), "s"(sbase), "n"(OFF) : "memory");
  else asm volatile("global_load_lds_dwordx4 %0, %1 offset:%2" :: "v"(voff), "s"(sbase), "n"(OFF) : "memory");
}
__device__ __forceinline__ void set_m0(unsigned v) { asm volatile("s_mov_b32 m0, %0\n\ts_nop 0" :: "s"(v) : "memory"); }
template <int AUX> __device__ __forceinline__ void touch(const char* g, LAS char* scratch) {
  __builtin_amdgcn_global_load_lds((const GAS void*)g, (LAS void*)scratch, 4, 0, AUX);
}
__device__ __forceinline__ void st8(void* p, u32x2 v) { __hip_atomic_store((u64*)p, __builtin_bit_cast(u64, v), RLX_AGENT); }
__device__ __forceinline__ u32x2 ld8(const void* p) { return __builtin_bit_cast(u32x2, __hip_atomic_load((u64*)p, RLX_AGENT)); }

constexpr int NL = 2;
constexpr int NWAVES = 8;
constexpr int PQG = ALIAS ? 4 * 216 * 64 * 8 : 4 * 160 * 64 * 8;
constexpr int LDS_H = 120 * 1024;
constexpr int LDS_RING = 136 * 1024;
constexpr int LDS_X = LDS_RING;
constexpr int LDS_TOTAL = 160 * 1024;
constexpr int LDS_MISC = LDS_TOTAL - 512;

template <int KID_, int BM_, int BN_, int BK_, int WM_, int WN_, int WK_, int NSA_, int NSB_, int NK_, int AMODE_, int BMODE_, int SWAP_, int NPRE_>
struct Cfg {
  static constexpr int KID = KID_, BM = BM_, BN = BN_, BK = BK_, WM = WM_, WN = WN_, WK = WK_, NSA = NSA_, NSB = NSB_, NK = NK_;
  static constexpr int AMODE = AMODE_, BMODE = BMODE_, SWAP = SWAP_, NPRE = NPRE_;
  static constexpr int NC = WM * WN * WK;
  static constexpr int MF = BM / WM / 16, NF = BN / WN / 16, KSW = BK / 32 / WK;
  static constexpr int ABYTES = BM * BK * 2, BBYTES = BN * BK * 2;
  static constexpr int PA = ABYTES / 1024, PB = BBYTES / 1024;
  static constexpr int GA = PA / NL, GB = PB / NL, G = GA + GB;
  static constexpr int RA = NSA * ABYTES, RB = NSB * BBYTES;
  static constexpr int NBAR = NK + (WK == 2 ? 2 : 1);
  static_assert(PA % NL == 0 && PB % NL == 0, "pieces");
  static_assert(RA + RB <= LDS_RING, "lds");
  static_assert(NSA <= NK && NSB <= NK, "stages");
  static_assert(KSW >= 1 && KSW <= 2, "ksw");
};

template <class C> struct Frags { bf16x8 a[C::KSW][C::MF]; bf16x8 b[C::KSW][C::NF]; };

template <class C, class Src, bool ISA>
struct Loader {
  static constexpr int G = ISA ? C::GA : C::GB, NS = ISA ? C::NSA : C::NSB, BYTES = ISA ? C::ABYTES : C::BBYTES;
  static constexpr int RING = NS * BYTES, MODE = ISA ? C::AMODE : C::BMODE, AUX = ISA ? Src::AUXA : Src::AUXB;
  const Src& src;
  int lw;
  unsigned lbase, cur;
  unsigned voff[G];
  __device__ __forceinline__ Loader(LAS char* lds, const Src& s, int lw_, int lane) : src(s), lw(lw_) {
    lbase = (unsigned)(size_t)lds + (ISA ? 0 : C::RA) + lw * (G * 1024);
#pragma unroll
    for (int i = 0; i < G; ++i) {
      const int p = (lw * G + i) * 64 + lane;
      unsigned v;
      if constexpr (MODE == 0) v = p * 16;
      else if constexpr (MODE == 1) { const int row = p >> 3, c = (p & 7) ^ ((row >> 1) & 7); v = row * (ISA ? Src::ldA : Src::ldB) + c * 16; }
      else { const int q = p % (C::BK * 8), k = q >> 3, c = (q & 7) ^ ((((k >> 3) & 1) << 2) | (((k >> 1) & 1) << 1)); v = k * Src::ldB + c * 16; }
      voff[i] = v - (i & 3) * 1024;
    }
  }
  template <int I> __device__ __forceinline__ void pieces(const char* gb, unsigned l) {
    if constexpr (I < G) {
      if constexpr ((I & 3) == 0) set_m0(l + I * 1024);
      dma16<AUX, (I & 3) * 1024>(gb, voff[I]);
      pieces<I + 1>(gb, l);
    }
  }
  __device__ __forceinline__ void issue(int k, unsigned buf) {
    pieces<0>(ISA ? src.baseA(k) : src.baseB(k, lw), lbase + buf);
  }
  __device__ __forceinline__ void fill(int s0, int s1) {
#pragma unroll 1
    for (int s = s0; s < s1; ++s) issue(s, s * BYTES);
  }
  template <int K, class Hook> __device__ __forceinline__ void tail(Hook hook) {
    if constexpr (K < C::NK - 1) {
      wait_vm<(C::NK - 2 - K) * G + Hook::extra(K)>();
      bar();
      hook.template at<K>();
      tail<K + 1>(hook);
    }
  }
  template <int N0, class Hook> __device__ __forceinline__ void run(Hook hook) {
    static_assert(N0 >= 1 && N0 <= NS && (NS - 1) * G <= 63, "ring");
    wait_vm<(N0 - 1) * G>();
    bar();
    fill(N0, NS);
    cur = 0;
#pragma unroll 1
    for (int k = 0; k < C::NK - NS; ++k) {
      wait_vm<(NS - 2) * G>();
      bar();
      issue(k + NS, cur);
      cur += BYTES; if (cur == RING) cur = 0;
    }
    hook.template at<C::NK - NS - 1>();
    tail<C::NK - NS>(hook);
  }
};
struct NoHook {
  static constexpr int extra(int) { return 0; }
  template <int K> __device__ __forceinline__ void at() {}
};

template <class C>
struct Consumer {
  LAS char* lds;
  int wid, lane, wm, wn, wk;
  unsigned rdA[2], rdB[4];

  __device__ __forceinline__ Consumer(LAS char* lds_, int wid_, int lane_) : lds(lds_), wid(wid_), lane(lane_) {
    wk = wid % C::WK; wn = (wid / C::WK) % C::WN; wm = wid / (C::WK * C::WN);
    const int fr = lane & 15, fq = lane >> 4;
    const int arow = wm * (C::BM / C::WM) + fr;
    const int ss0 = wk * C::KSW;
#pragma unroll
    for (int s = 0; s < 2; ++s) {
      const int ss = ss0 + s;
      if constexpr (C::AMODE == 0) rdA[s] = ((ss0 * 4 + fq) * C::BM + arow) * 16;
      else rdA[s] = arow * 128 + ((((ss & 1) * 4 + fq) ^ ((fr >> 1) & 7)) * 16);
    }
    if constexpr (C::BMODE <= 1) {
      const int brow = wn * (C::BN / C::WN) + fr;
#pragma unroll
      for (int s = 0; s < 2; ++s) {
        const int ss = ss0 + s;
        if constexpr (C::BMODE == 0) rdB[s] = C::RA + ((ss0 * 4 + fq) * C::BN + brow) * 16;
        else rdB[s] = C::RA + brow * 128 + ((((ss & 1) * 4 + fq) ^ ((fr >> 1) & 7)) * 16);
      }
    } else {
      const int g = lane >> 4, q = (lane >> 2) & 3, p = lane & 3;
#pragma unroll
      for (int j = 0; j < 4; ++j) {
        const int n = j;
        const int jj = (C::BMODE == 4) ? j : 2 * wn + (n & 1), mat = (C::BMODE == 4) ? 0 : (n >> 1);
        rdB[j] = C::RA + mat * (C::BK * 128) + (32 * ss0 + 8 * g + q) * 128 + ((((jj ^ (((g & 1) << 1) | (q >> 1))) << 1) | (p >> 1)) * 16) + 8 * (p & 1);
      }
    }
  }

  __device__ __forceinline__ void read(Frags<C>& f, unsigned bufa, unsigned bufb) {
    LAS char* a = lds + bufa; LAS char* b = lds + bufb;
#pragma unroll
    for (int s = 0; s < C::KSW; ++s) {
      if constexpr (C::BMODE == 0) {
#pragma unroll
        for (int n = 0; n < C::NF; ++n) f.b[s][n] = *(const LAS bf16x8*)(b + rdB[0] + s * (C::BN * 64) + n * 256);
      } else if constexpr (C::BMODE == 1) {
#pragma unroll
        for (int n = 0; n < C::NF; ++n) f.b[s][n] = *(const LAS bf16x8*)(b + rdB[s] + n * 2048);
      } else {
#pragma unroll
        for (int n = 0; n < C::NF; ++n) {
          bf16x4 lo = __builtin_amdgcn_ds_read_tr16_b64_v4i16((LAS bf16x4*)(b + rdB[n] + s * 4096));
          bf16x4 hi = __builtin_amdgcn_ds_read_tr16_b64_v4i16((LAS bf16x4*)(b + rdB[n] + s * 4096 + 512));
          f.b[s][n] = __builtin_shufflevector(lo, hi, 0, 1, 2, 3, 4, 5, 6, 7);
        }
      }
      if constexpr (C::AMODE == 0) {
#pragma unroll
        for (int m = 0; m < C::MF; ++m) f.a[s][m] = *(const LAS bf16x8*)(a + rdA[0] + s * (C::BM * 64) + m * 256);
      } else {
#pragma unroll
        for (int m = 0; m < C::MF; ++m) f.a[s][m] = *(const LAS bf16x8*)(a + rdA[s] + m * 2048);
      }
    }
  }

  __device__ __forceinline__ void mma(f32x4 (&acc)[C::MF][C::NF], const Frags<C>& f) {
#pragma unroll
    for (int s = 0; s < C::KSW; ++s)
#pragma unroll
      for (int m = 0; m < C::MF; ++m)
#pragma unroll
        for (int n = 0; n < C::NF; ++n)
          acc[m][n] = C::SWAP ? __builtin_amdgcn_mfma_f32_16x16x32_bf16(f.b[s][n], f.a[s][m], acc[m][n], 0, 0, 0)
                              : __builtin_amdgcn_mfma_f32_16x16x32_bf16(f.a[s][m], f.b[s][n], acc[m][n], 0, 0, 0);
  }
  static constexpr int NRD = C::KSW * (C::MF + (C::BMODE >= 2 ? 2 : 1) * C::NF);
  static constexpr int NMM = C::KSW * C::MF * C::NF;
  __device__ __forceinline__ void interleave() {
    constexpr int RPM = (NRD + NMM - 3) / (NMM - 2);
#pragma unroll
    for (int i = 0; i < NMM; ++i) {
      __builtin_amdgcn_sched_group_barrier(0x100, RPM, 0);
      __builtin_amdgcn_sched_group_barrier(0x008, 1, 0);
    }
  }
  unsigned na, nb;
  __device__ __forceinline__ void step(f32x4 (&acc)[C::MF][C::NF], Frags<C>& cur, Frags<C>& nxt) {
    na += C::ABYTES; if (na == C::RA) na = 0;
    nb += C::BBYTES; if (nb == C::RB) nb = 0;
    wait_lgkm0(); bar();
    __builtin_amdgcn_sched_barrier(0);
    read(nxt, na, nb);
    mma(acc, cur);
    interleave();
    __builtin_amdgcn_sched_barrier(0);
  }

  template <class Hook, class Hook2> __device__ __forceinline__ void run(f32x4 (&acc)[C::MF][C::NF], Hook hook, Hook2 hook2) {
    Frags<C> f0, f1;
#pragma unroll
    for (int m = 0; m < C::MF; ++m)
#pragma unroll
      for (int n = 0; n < C::NF; ++n) { acc[m][n] = (f32x4){0.f, 0.f, 0.f, 0.f}; asm volatile("" : "+v"(acc[m][n])); }
    bar();
    if (wid == 0) { STAMP(C::KID, 1); CSTAMP(C::KID, 8); }
    hook();
    read(f0, 0, 0);
    na = 0; nb = 0;
    if (wid == 0) STAMP(C::KID, 4);
#pragma unroll 1
    for (int k = 0; k + 2 < C::NK; k += 2) {
      step(acc, f0, f1);
      step(acc, f1, f0);
    }
    if (wid == 0) STAMP(C::KID, 5);
    if constexpr (C::NK % 2 == 0) {
      step(acc, f0, f1);
      wait_lgkm0(); bar();
      hook2();
      mma(acc, f1);
    } else {
      wait_lgkm0(); bar();
      mma(acc, f0);
    }
    if (wid == 0) { STAMP(C::KID, 2); CSTAMP(C::KID, 9); }
  }

  static constexpr bool SPLM = (C::MF % 2 == 0);
  static constexpr int RM = SPLM ? C::MF / 2 : C::MF, RN = SPLM ? C::NF : C::NF / 2;
  __device__ __forceinline__ void reduce_k(f32x4 (&acc)[C::MF][C::NF], f32x4 (&red)[RM][RN]) {
    static_assert(C::WK == 2, "");
    static_assert(LDS_X + C::NC * RM * RN * 1024 <= LDS_MISC || RM * RN == 6, "");
    LAS f32x4* x = (LAS f32x4*)(lds + LDS_X - (RM * RN == 6 ? 1024 : 0));
#pragma unroll
    for (int m = 0; m < RM; ++m)
#pragma unroll
      for (int n = 0; n < RN; ++n) {
        const f32x4 lo = acc[m][n], hi = SPLM ? acc[m + RM][n] : acc[m][n + RN];
        x[(wid * RM * RN + m * RN + n) * 64 + lane] = wk ? lo : hi;
        red[m][n] = wk ? hi : lo;
      }
    wait_lgkm0(); bar();
#pragma unroll
    for (int m = 0; m < RM; ++m)
#pragma unroll
      for (int n = 0; n < RN; ++n) red[m][n] += x[((wid ^ 1) * RM * RN + m * RN + n) * 64 + lane];
  }
};

constexpr int FLE = 0;
constexpr int FL0 = 32;
constexpr int FL1 = FL0 + 2048;
constexpr int FL2 = FL1 + 16 * 16;
constexpr int FL3 = FL2 + 8 * 32;
constexpr int FLW = FL3 + 8 * 32;
constexpr int FLC = FLW + 48;

__device__ __forceinline__ void publish(LAS unsigned* arr, unsigned nwaves, u64* flag, u64 tag, int lane, int kid) {
  if (threadIdx.x < 64) STAMP(kid, 7);
  asm volatile("" ::: "memory");
  if (lane == 0) __hip_atomic_fetch_add(arr + 8, 1u, RLX_WG);
  wait_vm<0>();
  if (lane == 0) {
    const unsigned old = __hip_atomic_fetch_add(arr, 1u, RLX_WG);
    if (old == nwaves - 1) {
      __hip_atomic_store(flag, tag, RLX_AGENT);
      STAMP(kid, 6);
    }
  }
}
__device__ __forceinline__ void lds_wait(LAS unsigned* w, unsigned want) {
  const unsigned a = (unsigned)(size_t)w;
#pragma unroll 1
  for (unsigned i = 0; i < (1u << 24); ++i) {
    unsigned v;
    asm volatile("ds_read_b32 %0, %1\n\ts_waitcnt lgkmcnt(0)" : "=v"(v) : "v"(a) : "memory");
    if (__builtin_amdgcn_readfirstlane(v) >= want) break;
    __builtin_amdgcn_s_sleep(1);
  }
}
template <int SLEEP, int NW>
__device__ __forceinline__ void await(const u64* word, bool need, u64 tag, int lw, LAS unsigned* go, unsigned epoch) {
  if (lw == 0) {
    const u64 t0 = wall_clock64();
    if (PDELAY) __builtin_amdgcn_s_sleep(PDELAY);
    for (;;) {
      bool ok = true;
#pragma unroll
      for (int i = 0; i < NW; ++i) ok &= __hip_atomic_load(word + i * 64, RLX_AGENT) == tag;
      if (__all(!need || ok)) break;
      if (wall_clock64() - t0 > 2000000ull) break;
      if (SLEEP > PSLEEP && __any(need && ok)) __builtin_amdgcn_s_sleep(PSLEEP); else __builtin_amdgcn_s_sleep(SLEEP);
    }
    __hip_atomic_store(go, epoch, RLX_WG);
  } else {
    lds_wait(go, epoch);
  }
  __builtin_amdgcn_fence(__ATOMIC_ACQUIRE, "wavefront");
}
#define P1_PRE 1
#define PL1 1
#define PL2 1
#define PL3 1

#define PRE1 2
#define PRE2 6
#define PRE3 5
#define PRE4 6
using C1 = Cfg<1, 80, 128, 64, 1, 4, 1, 4, 6, 32, 0, 1, 0, PRE1>;
#define P2A 4
#define P2B 6
using C2 = Cfg<2, 64, 80, 64, 2, 1, 2, P2A, P2B, 20, 0, 1, 1, PRE2>;
#define P3A 4
#define P3B 5
using C3 = Cfg<3, 256, 128, 32, 2, 2, 1, P3A, P3B, 20, 0, 3, 1, PRE3>;
#define P4A 4
#define P4B 6
using C4 = Cfg<4, 64, 80, 64, 2, 1, 2, P4A, P4B, 27, 0, 0, 1, PRE4>;
#define ROT 15
struct Src1 {
  const char* a; const char* b; int o, c;
  static constexpr int ldA = 0, ldB = S_ * 2, AUXA = AUXH, AUXB = 0, rows = 80;
  __device__ __forceinline__ int kt(int k) const { const int s = (k + o) & 31; return (ROT & 1) ? ((s & 24) | ((s + c) & 7)) : k; }
  __device__ __forceinline__ const char* baseA(int k) const { return a + kt(k) * (80 * 128); }
  __device__ __forceinline__ const char* baseB(int k, int) const { return b + kt(k) * 128; }
};
struct Src2 {
  const char* a; const char* bre; const char* bim; int rot;
  static constexpr int ldA = 0, ldB = D_ * 2, AUXA = AUXH, AUXB = 0, rows = 64;
  __device__ __forceinline__ int kt(int k) const { const int s = k + ((ROT & 2) ? rot : 0); return s >= 20 ? s - 20 : s; }
  __device__ __forceinline__ const char* baseA(int k) const { return a + kt(k) * (64 * 128); }
  __device__ __forceinline__ const char* baseB(int k, int) const { const int t = kt(k); return t < 10 ? bre + t * 128 : bim + (t - 10) * 128; }
};
struct Src3 {
  const char* a; const char* bg; const char* bu; int rot;
  static constexpr int ldA = 0, ldB = F_ * 2, AUXA = AUXH, AUXB = 0, rows = 256;
  __device__ __forceinline__ int kt(int k) const { const int s = k + ((ROT & 4) ? rot : 0); return s >= 20 ? s - 20 : s; }
  __device__ __forceinline__ const char* baseA(int k) const { return a + kt(k) * (256 * 64); }
  __device__ __forceinline__ const char* baseB(int k, int up) const { return (up ? bu : bg) + kt(k) * (32 * F_ * 2); }
};
struct Src4 {
  const char* a; const char* b;
  static constexpr int ldA = 0, ldB = 0, AUXA = AUXH, AUXB = AUXH, rows = 64;
  __device__ __forceinline__ const char* baseA(int k) const { return a + k * (64 * 128); }
  __device__ __forceinline__ const char* baseB(int k, int) const { return b + k * (80 * 128); }
};

struct Params {
  const u16* x; const u16* sre; const u16* sim; const u16* hre; const u16* him; const u16* wg; const u16* wu; const u16* wd;
  u16* xnI; u16* pqI; u16* hI; u16* hhI; u16* wdI; f32x2* stats; float* cs; u64* flags; u16* out;
};

template <int CTRL, int RMASK> __device__ __forceinline__ float dpp_add(float v) {
  return v + __int_as_float(__builtin_amdgcn_update_dpp(0, __float_as_int(v), CTRL, RMASK, 0xf, true));
}
__device__ __forceinline__ float wave_sum(float v) {
  v = dpp_add<0x111, 0xf>(v); v = dpp_add<0x112, 0xf>(v); v = dpp_add<0x114, 0xf>(v); v = dpp_add<0x118, 0xf>(v);
  v = dpp_add<0x142, 0xa>(v); v = dpp_add<0x143, 0xc>(v);
  return __int_as_float(__builtin_amdgcn_readlane(__float_as_int(v), 63));
}
__device__ __forceinline__ float sum_rows(float v) {
  const auto a = __builtin_amdgcn_permlane16_swap(__float_as_uint(v), __float_as_uint(v), false, false);
  v = __uint_as_float(a[0]) + __uint_as_float(a[1]);
  const auto b = __builtin_amdgcn_permlane32_swap(__float_as_uint(v), __float_as_uint(v), false, false);
  return __uint_as_float(b[0]) + __uint_as_float(b[1]);
}
__device__ __forceinline__ u32x4 col8(LAS char* tile, int cb, int lane) {
  LAS char* a = tile + ((lane >> 2) & 3) * 1296 + (cb * 64 + (lane >> 4) * 16 + (lane & 3) * 4) * 2;
  const bf16x4 lo = __builtin_amdgcn_ds_read_tr16_b64_v4i16((LAS bf16x4*)a);
  const bf16x4 hi = __builtin_amdgcn_ds_read_tr16_b64_v4i16((LAS bf16x4*)(a + 4 * 1296));
  return __builtin_bit_cast(u32x4, __builtin_shufflevector(lo, hi, 0, 1, 2, 3, 4, 5, 6, 7));
}
template <int N> __device__ __forceinline__ void bars() {
#pragma unroll 1
  for (int i = 0; i < N; ++i) bar();
}

struct Hook3 {
  const Params& p; LAS f32x4* rs; LAS float* csl; LAS unsigned* arr; u64 tag; int mt3, j3, lw, lane;
  f32x2 (&pv)[2][8]; u64& fc; float& cv;
  static constexpr int K0 = C3::NK - C3::NSB - 1;
  static constexpr int extra(int k) { return k <= K0 + 2 ? 17 : 1; }
  template <int K> __device__ __forceinline__ void at() {
    if constexpr (K == K0) {
      fc = __hip_atomic_load(p.flags + FLC + j3 * 16, RLX_AGENT);
#pragma unroll
      for (int h = 0; h < 2; ++h)
#pragma unroll
        for (int i = 0; i < 8; ++i) pv[h][i] = __builtin_bit_cast(f32x2, ld8(p.stats + i * S_ + mt3 * 256 + lw * 128 + h * 64 + lane));
    } else if constexpr (K == K0 + 2) {
      if (!__all(fc == tag)) await<PSLEEP, 1>(p.flags + FLC + j3 * 16, true, tag, 0, arr + 11, 1u);
      __builtin_amdgcn_fence(__ATOMIC_ACQUIRE, "wavefront");
      cv = __hip_atomic_load(p.cs + j3 * 128 + lw * 64 + lane, RLX_AGENT);
#pragma unroll
      for (int h = 0; h < 2; ++h) {
        float mean = 0.f, q = 0.f, dd = 0.f;
#pragma unroll
        for (int i = 0; i < 8; ++i) { mean += pv[h][i][0]; q += pv[h][i][1]; }
        mean *= 0.125f;
#pragma unroll
        for (int i = 0; i < 8; ++i) { const float d = pv[h][i][0] - mean; dd += d * d; }
        q += 80.0f * dd;
        const float den = rbf(__builtin_amdgcn_sqrtf(rbf(rbf(q * (1.0f / D_)) + 1e-5f)));
        rs[lw * 128 + h * 64 + lane] = (f32x4){-rbf(mean), den, -1.44269504089f * __builtin_amdgcn_rcpf(den), 0.f};
      }
    } else if constexpr (K == C3::NK - 2) {
      csl[lw * 64 + lane] = cv;
      wait_lgkm0();
    }
  }
};

__global__ __launch_bounds__(NWAVES * 64) void mega(const Params p) {
  __shared__ __attribute__((aligned(1024))) char smem_[LDS_TOTAL];
  LAS char* lds = (LAS char*)smem_;
  const int wid = __builtin_amdgcn_readfirstlane(threadIdx.x >> 6), lane = threadIdx.x & 63;
  const int bid = blockIdx.x, g = bid & 7, idx = bid >> 3;
  LAS unsigned* arr = (LAS unsigned*)(lds + LDS_MISC + 256);
  if (wid == 0) STAMP(0, 0);
  const u16* xr = p.x + (bid * 8 + (wid & 7)) * D_;
  const bf16x8 v0 = *(const bf16x8*)(xr + lane * 8);
  const bf16x8 v1 = *(const bf16x8*)(xr + 512 + (lane & 15) * 8);
  const u64 ep = __hip_atomic_load(p.flags + FLE, RLX_AGENT);
  if (threadIdx.x < 16) arr[threadIdx.x] = 0u;
  wait_lgkm0(); bar();
  const int j1 = idx & 7, mq = idx >> 3, isQ = mq >> 1, t1 = g * 256 + (mq & 1) * 128;
  const int j2 = idx & 7, mt2 = g * 4 + (idx >> 3);
  const int mt3 = (g & 6) + (idx & 1), j3 = (g & 1) * 14 + (idx >> 1);
  const bool on3 = idx < 28 && j3 < 27;
  LAS f32x4* rs = (LAS f32x4*)(lds + LDS_X);
  LAS float* csl = (LAS float*)(lds + LDS_X + 4096);

  {
    float f[16];
#pragma unroll
    for (int i = 0; i < 8; ++i) { f[i] = bf2f((u16)v0[i]); f[8 + i] = bf2f((u16)v1[i]); }
    const float piv = __int_as_float(__builtin_amdgcn_readfirstlane(__float_as_int(f[0])));
    asm volatile("" :: "v"(f[8]) : "memory");
    if (lane == 0) __hip_atomic_fetch_add(arr + 4, 1u, RLX_WG);
    if (wid == 0) STAMP(0, 4);
    float s0 = 0.f, s1 = 0.f, q0 = 0.f, q1 = 0.f;
#pragma unroll
    for (int i = 0; i < 8; ++i) { const float d = f[i] - piv, e = f[8 + i] - piv; s0 += d; q0 += d * d; s1 += e; q1 += e * e; }
    const float sd = wave_sum(s0 + (lane < 16 ? s1 : 0.f)) * (1.0f / D_);
    const float qd = wave_sum(q0 + (lane < 16 ? q1 : 0.f)) * (1.0f / D_);
    const float mb = rbf(piv + sd), vb = rbf(fmaxf(qd - sd * sd, 0.f));
    const float rden = __builtin_amdgcn_rcpf(rbf(__builtin_amdgcn_sqrtf(rbf(vb + 1e-5f))));
    u32x4 w0, w1;
#pragma unroll
    for (int i = 0; i < 4; ++i) {
      w0[i] = pk2(rbf(f[2 * i] - mb) * rden, rbf(f[2 * i + 1] - mb) * rden);
      w1[i] = pk2(rbf(f[8 + 2 * i] - mb) * rden, rbf(f[9 + 2 * i] - mb) * rden);
    }
    LAS char* tb = lds + LDS_X;
    *(LAS u32x4*)(tb + wid * 1296 + lane * 16) = w0;
    if (lane < 16) *(LAS u32x4*)(tb + wid * 1296 + 1024 + lane * 16) = w1;
    wait_lgkm0();
    if (lane == 0) __hip_atomic_fetch_add(arr + 7, 1u, RLX_WG);
  }
  const u64 tag = (((u64)(unsigned)__builtin_amdgcn_readfirstlane((unsigned)(ep >> 32)) << 32) | (unsigned)__builtin_amdgcn_readfirstlane((unsigned)ep)) + 1ull;
  {
    LAS char* tb = lds + LDS_X;
    lds_wait(arr + 7, 8);
    auto rsrc = __builtin_amdgcn_make_buffer_rsrc(p.xnI, 0, S_ * D_ * 2, 0x00020000);
#pragma unroll
    for (int h = 0; h < 2; ++h) {
      const int cb = h * 8 + wid;
      if (cb < 10) {
        const int d = cb * 64 + lane, j = d / 80, n = d % 80;
        __builtin_amdgcn_raw_buffer_store_b128(col8(tb, cb, lane), rsrc, ((j * 256 + bid) * 80 + n) * 16, 0, 16);
      }
    }
    if (wid == 0) STAMP(0, 7);
    wait_vm<0>();
    unsigned old = 0;
    if (lane == 0) old = __hip_atomic_fetch_add(arr + 5, 1u, RLX_WG);
    if (__builtin_amdgcn_readfirstlane(old) == 7u && lane < 8) __hip_atomic_store(p.flags + FL0 + lane * 256 + bid, tag, RLX_AGENT);
    if (wid == 0) STAMP(0, 3);
  }
  const Src1 s1{(const char*)(p.xnI + (size_t)j1 * 256 * 80 * 8), (const char*)((isQ ? p.sim : p.sre) + (size_t)t1 * S_), (g + 2 * mq) & 7, j1};
  const Src2 s2{(const char*)(p.pqI + (size_t)g * PQG + (size_t)(mt2 & 3) * 160 * 64 * 8), (const char*)(p.hre + (size_t)j2 * 80 * D_), (const char*)(p.him + (size_t)j2 * 80 * D_), (mt2 * 5) >> 3};
  const Src3 s3{(const char*)(p.hI + (size_t)mt3 * 80 * 256 * 8), (const char*)(p.wg + j3 * 64), (const char*)(p.wu + j3 * 64), (mt3 * 5) >> 1};
  const Src4 s4{(const char*)(p.hhI + (size_t)mt2 * 216 * 64 * 8), (const char*)(p.wdI + (size_t)j2 * 216 * 80 * 8)};
  if (wid >= 6) {
    const int lw = wid - 6;
    {
      Loader<C1, Src1, true> L(lds, s1, lw, lane);
      lds_wait(arr + 5, 8);
      await<PSLEEP, 4>(p.flags + FL0 + g * 256 + lane, true, tag, lw, arr + 3, 1u);
      if (bid == 0 && lw == 0 && lane == 0) __hip_atomic_store(p.flags + FLE, tag, RLX_AGENT);
      if (lw == 0) STAMP(1, 0);
      wait_vm<0>();
      L.fill(0, NRAMP); L.run<NRAMP>(NoHook{});
      bar();
    }
    {
      Loader<C2, Src2, true> L(lds, s2, lw, lane);
      lds_wait(arr + 0, C1::NC);
      await<PSLEEP, 1>(p.flags + FL1 + (mt2 >> 1) * 16 + (lane & 15), true, tag, lw, arr + 3, 2u);
      if (lw == 0) STAMP(2, 0);
      wait_vm<0>();
      L.fill(0, NRAMP); L.run<NRAMP>(NoHook{});
      bar(); bar();
    }
    if (on3) {
      Loader<C3, Src3, true> L(lds, s3, lw, lane);
      lds_wait(arr + 1, C2::NC);
      await<PSLEEP, 1>(p.flags + FL2 + mt3 * 32 + (lane & 31), true, tag, lw, arr + 3, 3u);
      if (lw == 0) STAMP(3, 0);
      wait_vm<0>();
      L.fill(0, NRAMP); L.run<NRAMP>(NoHook{});
      bar();
    }
    {
      Loader<C4, Src4, true> L(lds, s4, lw, lane);
      lds_wait(arr + 2, 4);
      if (on3) await<PSLEEP, 1>(p.flags + FL3 + g * 32 + (lane & 31), (lane & 31) < 27, tag, lw, arr + 3, 4u);
      else await<ISLEEP, 1>(p.flags + FL3 + g * 32 + (lane & 31), (lane & 31) < 27, tag, lw, arr + 3, 4u);
      if (lw == 0) STAMP(4, 0);
      wait_vm<0>();
      L.fill(0, NRAMP); L.run<NRAMP>(NoHook{});
      bar(); bar();
    }
    return;
  }
  if (wid >= 4) {
    const int lw = wid - 4;
    {
      Loader<C1, Src1, false> L(lds, s1, lw, lane);
      lds_wait(arr + 5, 8);
      L.fill(0, C1::NPRE); L.run<C1::NPRE>(NoHook{});
      bar();
    }
    {
      Loader<C2, Src2, false> L(lds, s2, lw, lane);
      lds_wait(arr + 0, C1::NC);
      L.fill(0, C2::NPRE); L.run<C2::NPRE>(NoHook{});
      bar(); bar();
    }
    if (on3) {
      Loader<C3, Src3, false> L(lds, s3, lw, lane);
      lds_wait(arr + 1, C2::NC);
      static_assert(Hook3::K0 + 2 < C3::NK - 2, "");
      f32x2 pv[2][8]; u64 fc; float cv;
      L.fill(0, C3::NPRE); L.run<C3::NPRE>(Hook3{p, rs, csl, arr, tag, mt3, j3, lw, lane, pv, fc, cv});
      bar();
    }
    {
      Loader<C4, Src4, false> L(lds, s4, lw, lane);
      await<ISLEEP, 1>(p.flags + FLW + min(lane, 39), true, tag, lw, arr + 13, 1u);
      lds_wait(arr + 2, 4);
      wait_vm<0>();
      L.fill(0, C4::NPRE); L.run<C4::NPRE>(NoHook{});
      bar(); bar();
    }
    return;
  }

#define FRESH_LANE int lane_ = threadIdx.x & 63; asm volatile("" : "+v"(lane_)); const int lane = lane_, fr = lane & 15, fq = lane >> 4; (void)fr; (void)fq;
  {
    FRESH_LANE
    Consumer<C1> L(lds, wid, lane);
    f32x4 acc[C1::MF][C1::NF];
    L.run(acc, [] {}, [] {});
    const float sg = isQ ? -1.f : 1.f;
#pragma unroll
    for (int n = 0; n < 2; ++n) {
      const int t = t1 + L.wn * 32 + n * 16 + fr, mt = t >> 6, r = t & 63;
#pragma unroll
      for (int m = 0; m < 5; ++m) {
        const int koct = isQ * 80 + j1 * 10 + m * 2 + (fq >> 1);
        u32x2 w; w[0] = pk2(sg * acc[m][n][0], sg * acc[m][n][1]); w[1] = pk2(sg * acc[m][n][2], sg * acc[m][n][3]);
        st8(p.pqI + (size_t)g * PQG + (((size_t)(mt & 3) * 160 + koct) * 64 + r) * 8 + (fq & 1) * 4, w);
      }
    }
    publish(arr + 0, C1::NC, p.flags + FL1 + (t1 >> 7) * 16 + isQ * 8 + j1, tag, lane, 1);
    if (wid == 0) STAMP(1, 3);
  }
  {
    FRESH_LANE
    Consumer<C2> L(lds, wid, lane);
    f32x4 acc[C2::MF][C2::NF];
    const int t = mt2 * 64 + L.wm * 32 + L.wk * 16 + fr;
    bf16x4 xv[C2::NF];
#pragma unroll
    for (int n = 0; n < C2::NF; ++n) xv[n] = *(const bf16x4*)(p.x + (size_t)t * D_ + j2 * 80 + n * 16 + fq * 4);
#pragma unroll
    for (int n = 0; n < C2::NF; ++n) asm volatile("" : "+v"(xv[n]));
    L.run(acc, [] {}, [] {});
    f32x4 red[1][5];
    L.reduce_k(acc, red);
    float hv[C2::NF][4];
    float s = 0.f;
#pragma unroll
    for (int n = 0; n < C2::NF; ++n) {
      u32x2 w;
      w[0] = pk2(red[0][n][0] + bf2f((u16)xv[n][0]), red[0][n][1] + bf2f((u16)xv[n][1]));
      w[1] = pk2(red[0][n][2] + bf2f((u16)xv[n][2]), red[0][n][3] + bf2f((u16)xv[n][3]));
      hv[n][0] = __uint_as_float(w[0] << 16); hv[n][1] = __uint_as_float(w[0] & 0xffff0000u);
      hv[n][2] = __uint_as_float(w[1] << 16); hv[n][3] = __uint_as_float(w[1] & 0xffff0000u);
      s += (hv[n][0] + hv[n][1]) + (hv[n][2] + hv[n][3]);
      const int koct = j2 * 10 + n * 2 + (fq >> 1);
      st8(p.hI + (((size_t)(t >> 8) * 80 + koct) * 256 + (t & 255)) * 8 + (fq & 1) * 4, w);
      *(LAS u32x2*)(lds + LDS_H + ((wid * 5 + n) * 64 + lane) * 8) = w;
    }
    const float mean = sum_rows(s) * (1.0f / 80.0f);
    float q = 0.f;
#pragma unroll
    for (int n = 0; n < C2::NF; ++n)
#pragma unroll
      for (int r = 0; r < 4; ++r) { const float d = hv[n][r] - mean; q += d * d; }
    q = sum_rows(q);
    if (fq == 0) st8(p.stats + j2 * S_ + t, __builtin_bit_cast(u32x2, (f32x2){mean, q}));
    publish(arr + 1, C2::NC, p.flags + FL2 + g * 32 + idx, tag, lane, 2);
    if (wid == 0) STAMP(2, 3);
  }
  if (on3) {
    FRESH_LANE
    Consumer<C3> L(lds, wid, lane);
    f32x4 acc[C3::MF][C3::NF];
    f32x4 st[C3::MF], cg[2], cu[2];
    L.run(acc, [] {}, [&] {
#pragma unroll
      for (int m = 0; m < C3::MF; ++m) st[m] = rs[L.wm * 128 + m * 16 + fr];
#pragma unroll
      for (int i = 0; i < 2; ++i) {
        const int fl = L.wn * 32 + i * 16 + fq * 4;
        cg[i] = *(const LAS f32x4*)(csl + fl); cu[i] = *(const LAS f32x4*)(csl + 64 + fl);
      }
    });
    u16* hb = p.hhI + (((size_t)(mt3 * 4 + L.wm * 2) * 216 + j3 * 8 + L.wn * 4 + (fq >> 1)) * 64 + fr) * 8 + (fq & 1) * 4;
#pragma unroll
    for (int m = 0; m < C3::MF; ++m)
#pragma unroll
      for (int i = 0; i < 2; ++i) {
        const f32x4 s0 = {st[m][0], st[m][0], st[m][0], st[m][0]}, s1 = {st[m][1], st[m][1], st[m][1], st[m][1]};
        const f32x4 x = __builtin_elementwise_fma(cg[i], s0, acc[m][i]) * st[m][2];
        f32x4 e;
#pragma unroll
        for (int r = 0; r < 4; ++r) e[r] = __builtin_amdgcn_exp2f(x[r]);
        const f32x4 d = __builtin_elementwise_fma(e, s1, s1);
        f32x4 rc;
#pragma unroll
        for (int r = 0; r < 4; ++r) rc[r] = __builtin_amdgcn_rcpf(d[r]);
        const f32x4 o = __builtin_elementwise_fma(cu[i], s0, acc[m][i + 2]) * rc;
        u32x2 w; w[0] = pk2(o[0], o[1]); w[1] = pk2(o[2], o[3]);
        st8(hb + (m >> 2) * (216 * 64 * 8) + i * (2 * 64 * 8) + (m & 3) * 128, w);
      }
    publish(arr + 2, C3::NC, p.flags + FL3 + mt3 * 32 + j3, tag, lane, 3);
    if (wid == 0) STAMP(3, 3);
  } else {
    FRESH_LANE
    const int id = idx >= 28 ? g * 4 + idx - 28 : 32 + (g >> 1) * 2 + idx - 26;
    auto rsrc = __builtin_amdgcn_make_buffer_rsrc(p.wdI, 0, F_ * D_ * 2, 0x00020000);
    lds_wait(arr + 1, C2::NC);
    if (id < 27) {
      const u16* w = ((wid & 2) ? p.wu : p.wg) + (size_t)((wid & 1) * 320 + (lane >> 3)) * F_ + id * 64 + (lane & 7) * 8;
      f32x2 a[4] = {{0.f, 0.f}, {0.f, 0.f}, {0.f, 0.f}, {0.f, 0.f}};
#pragma unroll 1
      for (int b = 0; b < 2; ++b) {
        u32x4 v[20];
#pragma unroll
        for (int i = 0; i < 20; ++i) v[i] = *(const u32x4*)(w + (size_t)(b * 20 + i) * 8 * F_);
#pragma unroll
        for (int i = 0; i < 20; ++i)
#pragma unroll
          for (int e = 0; e < 4; ++e) a[e] += (f32x2){__uint_as_float(v[i][e] << 16), __uint_as_float(v[i][e] & 0xffff0000u)};
      }
      LAS float* sf = (LAS float*)(lds + LDS_X);
#pragma unroll
      for (int e = 0; e < 4; ++e)
#pragma unroll
        for (int c = 0; c < 2; ++c) { float t = sum_rows(a[e][c]); t += __shfl_xor(t, 8); a[e][c] = t; }
      if (lane < 8) {
        *(LAS f32x4*)(sf + wid * 64 + lane * 8) = (f32x4){a[0][0], a[0][1], a[1][0], a[1][1]};
        *(LAS f32x4*)(sf + wid * 64 + lane * 8 + 4) = (f32x4){a[2][0], a[2][1], a[3][0], a[3][1]};
      }
      wait_lgkm0();
      unsigned old = 0;
      if (lane == 0) old = __hip_atomic_fetch_add(arr + 12, 1u, RLX_WG);
      if (__builtin_amdgcn_readfirstlane(old) == 3u) {
        __hip_atomic_store(p.cs + id * 128 + lane, sf[lane] + sf[64 + lane], RLX_AGENT);
        __hip_atomic_store(p.cs + id * 128 + 64 + lane, sf[128 + lane] + sf[192 + lane], RLX_AGENT);
        wait_vm<0>();
        if (lane == 0) __hip_atomic_store(p.flags + FLC + id * 16, tag, RLX_AGENT);
        STAMP(0, 5);
      }
    }
    const int k0 = id < 27 ? id * 3 : 81 + (id - 27) * 11, nk = id < 27 ? 3 : min(11, 216 - k0);
#pragma unroll 1
    for (int i = 0; i < 2 * nk; ++i) {
      const int row = (i & 1) * 4 + wid;
      const char* wr = (const char*)(p.wd + (size_t)((k0 + (i >> 1)) * 8 + row) * D_);
      set_m0((unsigned)(size_t)lds + (i >> 1) * 10368 + row * 1296);
      dma16<0, 0>(wr, lane * 16);
      if (lane < 16) dma16<0, 1024>(wr, lane * 16);
    }
    wait_vm<0>();
    if (lane == 0) __hip_atomic_fetch_add(arr + 6, 1u, RLX_WG);
    lds_wait(arr + 6, 4);
#pragma unroll 1
    for (int i = 0; i < nk; ++i) {
#pragma unroll
      for (int h = 0; h < 3; ++h) {
        const int cb = h * 4 + wid;
        if (cb < 10) {
          const int d = cb * 64 + lane, j = d / 80, n = d % 80;
          __builtin_amdgcn_raw_buffer_store_b128(col8(lds + i * 10368, cb, lane), rsrc, ((j * 216 + k0 + i) * 80 + n) * 16, 0, 16);
        }
      }
    }
    publish(arr + 2, 4, p.flags + FLW + id, tag, lane, 0);
  }
  {
    FRESH_LANE
    Consumer<C4> L(lds, wid, lane);
    f32x4 acc[C4::MF][C4::NF];
    const int t = mt2 * 64 + L.wm * 32 + L.wk * 16 + fr;
    u32x2 hv[5];
    L.run(acc, [] {}, [] {});
#pragma unroll
    for (int n = 0; n < 5; ++n) hv[n] = *(const LAS u32x2*)(lds + LDS_H + ((wid * 5 + n) * 64 + lane) * 8);
    f32x4 red[1][5];
    L.reduce_k(acc, red);
#pragma unroll
    for (int n = 0; n < 5; ++n) {
      u32x2 w;
      w[0] = pk2(red[0][n][0] + __uint_as_float(hv[n][0] << 16), red[0][n][1] + __uint_as_float(hv[n][0] & 0xffff0000u));
      w[1] = pk2(red[0][n][2] + __uint_as_float(hv[n][1] << 16), red[0][n][3] + __uint_as_float(hv[n][1] & 0xffff0000u));
      *(u32x2*)(p.out + (size_t)t * D_ + j2 * 80 + n * 16 + fq * 4) = w;
    }
    if (wid == 0) { wait_vm<0>(); STAMP(4, 3); }
  }
}

extern "C" void kernel_launch(void* const* d_in, const int* in_sizes, int n_in, void* d_out, int out_size,
                              void* d_ws, size_t ws_size, hipStream_t stream) {
  char* ws = (char*)d_ws;
  Params p;
  p.x = (const u16*)d_in[0]; p.sre = (const u16*)d_in[1]; p.sim = (const u16*)d_in[2];
  p.hre = (const u16*)d_in[3]; p.him = (const u16*)d_in[4];
  p.wg = (const u16*)d_in[6]; p.wu = (const u16*)d_in[7]; p.wd = (const u16*)d_in[8];
  p.flags = (u64*)ws;
  p.cs = (float*)(ws + 65536);
  p.stats = (f32x2*)(ws + 131072);
  p.xnI = (u16*)(ws + (1u << 20));
  p.pqI = (u16*)(ws + ((ALIAS ? 13u : 4u) << 20));
  p.hI = ALIAS ? (u16*)d_out : (u16*)(ws + (10u << 20));
  p.hhI = (u16*)(ws + (13u << 20));
  p.wdI = (u16*)(ws + (21u << 20));
  p.out = (u16*)d_out;
  mega<<<256, NWAVES * 64, 0, stream>>>(p);
}
```

```cpp
#include <hip/hip_runtime.h>
#include <hip/hip_bf16.h>

constexpr int PSLEEP = 4;
constexpr int ISLEEP = 16;
constexpr int PDELAY = 40;
constexpr int NRAMP = 3;
constexpr int AUXH = 16;

typedef unsigned short u16;
typedef unsigned long long u64;
typedef short bf16x8 __attribute__((ext_vector_type(8)));
typedef short bf16x4 __attribute__((ext_vector_type(4)));
typedef float f32x4 __attribute__((ext_vector_type(4)));
typedef float f32x2 __attribute__((ext_vector_type(2)));
typedef unsigned u32x2 __attribute__((ext_vector_type(2)));
typedef unsigned u32x4 __attribute__((ext_vector_type(4)));
#define LAS __attribute__((address_space(3)))
#define RLX_AGENT __ATOMIC_RELAXED, __HIP_MEMORY_SCOPE_AGENT
#define RLX_WG __ATOMIC_RELAXED, __HIP_MEMORY_SCOPE_WORKGROUP

constexpr int S_ = 2048, D_ = 640, F_ = 1728;

__device__ __forceinline__ float bf2f(u16 v) { return __uint_as_float(((unsigned)v) << 16); }
typedef __bf16 bf16x2n __attribute__((ext_vector_type(2)));
__device__ __forceinline__ unsigned pk2(float lo, float hi) {
  return __builtin_bit_cast(unsigned, __builtin_convertvector((f32x2){lo, hi}, bf16x2n));
}
__device__ __forceinline__ float rbf(float v) { return __uint_as_float(pk2(v, v) << 16); }

template <int N> __device__ __forceinline__ void wait_vm() { asm volatile("s_waitcnt vmcnt(%0)" ::"n"(N) : "memory"); }
__device__ __forceinline__ void wait_lgkm0() { asm volatile("" ::: "memory"); __builtin_amdgcn_s_waitcnt(0xC07F); asm volatile("" ::: "memory"); }
__device__ __forceinline__ void bar() { asm volatile("" ::: "memory"); __builtin_amdgcn_s_barrier(); asm volatile("" ::: "memory"); }
template <int AUX, int OFF> __device__ __forceinline__ void dma16(const char* sbase, unsigned voff) {
  if constexpr (AUX == 16) asm volatile("global_load_lds_dwordx4 %0, %1 offset:%2 sc1" :: "v"(voff), "s"(sbase), "n"(OFF) : "memory");
  else asm volatile("global_load_lds_dwordx4 %0, %1 offset:%2" :: "v"(voff), "s"(sbase), "n"(OFF) : "memory");
}
__device__ __forceinline__ void set_m0(unsigned v) { asm volatile("s_mov_b32 m0, %0\n\ts_nop 0" :: "s"(v) : "memory"); }
__device__ __forceinline__ void st8(void* p, u32x2 v) { __hip_atomic_store((u64*)p, __builtin_bit_cast(u64, v), RLX_AGENT); }
__device__ __forceinline__ u32x2 ld8(const void* p) { return __builtin_bit_cast(u32x2, __hip_atomic_load((u64*)p, RLX_AGENT)); }

constexpr int NL = 2;
constexpr int NWAVES = 8;
constexpr int LDS_H = 120 * 1024;
constexpr int LDS_RING = 136 * 1024;
constexpr int LDS_X = LDS_RING;
constexpr int LDS_TOTAL = 160 * 1024;
constexpr int LDS_MISC = LDS_TOTAL - 256;

template <int KID_, int BM_, int BN_, int BK_, int WM_, int WN_, int WK_, int NSA_, int NSB_, int NK_, int AMODE_, int BMODE_, int SWAP_, int NPRE_>
struct Cfg {
  static constexpr int KID = KID_, BM = BM_, BN = BN_, BK = BK_, WM = WM_, WN = WN_, WK = WK_, NSA = NSA_, NSB = NSB_, NK = NK_;
  static constexpr int AMODE = AMODE_, BMODE = BMODE_, SWAP = SWAP_, NPRE = NPRE_;
  static constexpr int NC = WM * WN * WK;
  static constexpr int MF = BM / WM / 16, NF = BN / WN / 16, KSW = BK / 32 / WK;
  static constexpr int ABYTES = BM * BK * 2, BBYTES = BN * BK * 2;
  static constexpr int PA = ABYTES / 1024, PB = BBYTES / 1024;
  static constexpr int GA = PA / NL, GB = PB / NL, G = GA + GB;
  static constexpr int RA = NSA * ABYTES, RB = NSB * BBYTES;
  static_assert(PA % NL == 0 && PB % NL == 0, "pieces");
  static_assert(RA + RB <= (KID == 1 ? LDS_RING : LDS_H), "lds");
  static_assert(NSA <= NK && NSB <= NK, "stages");
  static_assert(KSW >= 1 && KSW <= 2, "ksw");
};

template <class C> struct Frags { bf16x8 a[C::KSW][C::MF]; bf16x8 b[C::KSW][C::NF]; };

template <class C, class Src, bool ISA>
struct Loader {
  static constexpr int G = ISA ? C::GA : C::GB, NS = ISA ? C::NSA : C::NSB, BYTES = ISA ? C::ABYTES : C::BBYTES;
  static constexpr int RING = NS * BYTES, MODE = ISA ? C::AMODE : C::BMODE, AUX = ISA ? Src::AUXA : Src::AUXB;
  const Src& src;
  int lw;
  unsigned lbase, cur;
  unsigned voff[G];
  __device__ __forceinline__ Loader(LAS char* lds, const Src& s, int lw_, int lane) : src(s), lw(lw_) {
    lbase = (unsigned)(size_t)lds + (ISA ? 0 : C::RA) + lw * (G * 1024);
#pragma unroll
    for (int i = 0; i < G; ++i) {
      const int p = (lw * G + i) * 64 + lane;
      unsigned v;
      if constexpr (MODE == 0) v = p * 16;
      else if constexpr (MODE == 1) { const int row = p >> 3, c = (p & 7) ^ ((row >> 1) & 7); v = row * (ISA ? Src::ldA : Src::ldB) + c * 16; }
      else { const int q = p % (C::BK * 8), k = q >> 3, c = (q & 7) ^ ((((k >> 3) & 1) << 2) | (((k >> 1) & 1) << 1)); v = k * Src::ldB + c * 16; }
      voff[i] = v - (i & 3) * 1024;
    }
  }
  template <int I> __device__ __forceinline__ void pieces(const char* gb, unsigned l) {
    if constexpr (I < G) {
      if constexpr ((I & 3) == 0) set_m0(l + I * 1024);
      dma16<AUX, (I & 3) * 1024>(gb, voff[I]);
      pieces<I + 1>(gb, l);
    }
  }
  __device__ __forceinline__ void issue(int k, unsigned buf) {
    pieces<0>(ISA ? src.baseA(k) : src.baseB(k, lw), lbase + buf);
  }
  __device__ __forceinline__ void fill(int s0, int s1) {
#pragma unroll 1
    for (int s = s0; s < s1; ++s) issue(s, s * BYTES);
  }
  template <int K, class Hook> __device__ __forceinline__ void tail(Hook hook) {
    if constexpr (K < C::NK - 1) {
      wait_vm<(C::NK - 2 - K) * G + Hook::extra(K)>();
      bar();
      hook.template at<K>();
      tail<K + 1>(hook);
    }
  }
  template <int N0, class Hook> __device__ __forceinline__ void run(Hook hook) {
    static_assert(N0 >= 1 && N0 <= NS && (NS - 1) * G <= 63, "ring");
    wait_vm<(N0 - 1) * G>();
    bar();
    fill(N0, NS);
    cur = 0;
#pragma unroll 1
    for (int k = 0; k < C::NK - NS; ++k) {
      wait_vm<(NS - 2) * G>();
      bar();
      issue(k + NS, cur);
      cur += BYTES; if (cur == RING) cur = 0;
    }
    hook.template at<C::NK - NS - 1>();
    tail<C::NK - NS>(hook);
  }
};
struct NoHook {
  static constexpr int extra(int) { return 0; }
  template <int K> __device__ __forceinline__ void at() {}
};

template <class C>
struct Consumer {
  LAS char* lds;
  int wid, lane, wm, wn, wk;
  unsigned rdA[2], rdB[4];

  __device__ __forceinline__ Consumer(LAS char* lds_, int wid_, int lane_) : lds(lds_), wid(wid_), lane(lane_) {
    wk = wid % C::WK; wn = (wid / C::WK) % C::WN; wm = wid / (C::WK * C::WN);
    const int fr = lane & 15, fq = lane >> 4;
    const int arow = wm * (C::BM / C::WM) + fr;
    const int ss0 = wk * C::KSW;
#pragma unroll
    for (int s = 0; s < 2; ++s) {
      const int ss = ss0 + s;
      if constexpr (C::AMODE == 0) rdA[s] = ((ss0 * 4 + fq) * C::BM + arow) * 16;
      else rdA[s] = arow * 128 + ((((ss & 1) * 4 + fq) ^ ((fr >> 1) & 7)) * 16);
    }
    if constexpr (C::BMODE <= 1) {
      const int brow = wn * (C::BN / C::WN) + fr;
#pragma unroll
      for (int s = 0; s < 2; ++s) {
        const int ss = ss0 + s;
        if constexpr (C::BMODE == 0) rdB[s] = C::RA + ((ss0 * 4 + fq) * C::BN + brow) * 16;
        else rdB[s] = C::RA + brow * 128 + ((((ss & 1) * 4 + fq) ^ ((fr >> 1) & 7)) * 16);
      }
    } else {
      const int g = lane >> 4, q = (lane >> 2) & 3, p = lane & 3;
#pragma unroll
      for (int j = 0; j < 4; ++j) {
        const int jj = 2 * wn + (j & 1), mat = j >> 1;
        rdB[j] = C::RA + mat * (C::BK * 128) + (32 * ss0 + 8 * g + q) * 128 + ((((jj ^ (((g & 1) << 1) | (q >> 1))) << 1) | (p >> 1)) * 16) + 8 * (p & 1);
      }
    }
  }

  __device__ __forceinline__ void read(Frags<C>& f, unsigned bufa, unsigned bufb) {
    LAS char* a = lds + bufa; LAS char* b = lds + bufb;
#pragma unroll
    for (int s = 0; s < C::KSW; ++s) {
      if constexpr (C::BMODE == 0) {
#pragma unroll
        for (int n = 0; n < C::NF; ++n) f.b[s][n] = *(const LAS bf16x8*)(b + rdB[0] + s * (C::BN * 64) + n * 256);
      } else if constexpr (C::BMODE == 1) {
#pragma unroll
        for (int n = 0; n < C::NF; ++n) f.b[s][n] = *(const LAS bf16x8*)(b + rdB[s] + n * 2048);
      } else {
#pragma unroll
        for (int n = 0; n < C::NF; ++n) {
          bf16x4 lo = __builtin_amdgcn_ds_read_tr16_b64_v4i16((LAS bf16x4*)(b + rdB[n] + s * 4096));
          bf16x4 hi = __builtin_amdgcn_ds_read_tr16_b64_v4i16((LAS bf16x4*)(b + rdB[n] + s * 4096 + 512));
          f.b[s][n] = __builtin_shufflevector(lo, hi, 0, 1, 2, 3, 4, 5, 6, 7);
        }
      }
      if constexpr (C::AMODE == 0) {
#pragma unroll
        for (int m = 0; m < C::MF; ++m) f.a[s][m] = *(const LAS bf16x8*)(a + rdA[0] + s * (C::BM * 64) + m * 256);
      } else {
#pragma unroll
        for (int m = 0; m < C::MF; ++m) f.a[s][m] = *(const LAS bf16x8*)(a + rdA[s] + m * 2048);
      }
    }
  }

  __device__ __forceinline__ void mma(f32x4 (&acc)[C::MF][C::NF], const Frags<C>& f) {
#pragma unroll
    for (int s = 0; s < C::KSW; ++s)
#pragma unroll
      for (int m = 0; m < C::MF; ++m)
#pragma unroll
        for (int n = 0; n < C::NF; ++n)
          acc[m][n] = C::SWAP ? __builtin_amdgcn_mfma_f32_16x16x32_bf16(f.b[s][n], f.a[s][m], acc[m][n], 0, 0, 0)
                              : __builtin_amdgcn_mfma_f32_16x16x32_bf16(f.a[s][m], f.b[s][n], acc[m][n], 0, 0, 0);
  }
  static constexpr int NRD = C::KSW * (C::MF + (C::BMODE >= 2 ? 2 : 1) * C::NF);
  static constexpr int NMM = C::KSW * C::MF * C::NF;
  __device__ __forceinline__ void interleave() {
    constexpr int RPM = (NRD + NMM - 3) / (NMM - 2);
#pragma unroll
    for (int i = 0; i < NMM; ++i) {
      __builtin_amdgcn_sched_group_barrier(0x100, RPM, 0);
      __builtin_amdgcn_sched_group_barrier(0x008, 1, 0);
    }
  }
  unsigned na, nb;
  __device__ __forceinline__ void step(f32x4 (&acc)[C::MF][C::NF], Frags<C>& cur, Frags<C>& nxt) {
    na += C::ABYTES; if (na == C::RA) na = 0;
    nb += C::BBYTES; if (nb == C::RB) nb = 0;
    wait_lgkm0(); bar();
    __builtin_amdgcn_sched_barrier(0);
    read(nxt, na, nb);
    mma(acc, cur);
    interleave();
    __builtin_amdgcn_sched_barrier(0);
  }

  template <class Hook> __device__ __forceinline__ void run(f32x4 (&acc)[C::MF][C::NF], Hook hook) {
    Frags<C> f0, f1;
#pragma unroll
    for (int m = 0; m < C::MF; ++m)
#pragma unroll
      for (int n = 0; n < C::NF; ++n) { acc[m][n] = (f32x4){0.f, 0.f, 0.f, 0.f}; asm volatile("" : "+v"(acc[m][n])); }
    bar();
    read(f0, 0, 0);
    na = 0; nb = 0;
#pragma unroll 1
    for (int k = 0; k + 2 < C::NK; k += 2) {
      step(acc, f0, f1);
      step(acc, f1, f0);
    }
    if constexpr (C::NK % 2 == 0) {
      step(acc, f0, f1);
      wait_lgkm0(); bar();
      hook();
      mma(acc, f1);
    } else {
      wait_lgkm0(); bar();
      mma(acc, f0);
    }
  }

  static constexpr bool SPLM = (C::MF % 2 == 0);
  static constexpr int RM = SPLM ? C::MF / 2 : C::MF, RN = SPLM ? C::NF : C::NF / 2;
  __device__ __forceinline__ void reduce_k(f32x4 (&acc)[C::MF][C::NF], f32x4 (&red)[RM][RN]) {
    static_assert(C::WK == 2, "");
    static_assert(LDS_X + C::NC * RM * RN * 1024 <= LDS_MISC, "");
    LAS f32x4* x = (LAS f32x4*)(lds + LDS_X);
#pragma unroll
    for (int m = 0; m < RM; ++m)
#pragma unroll
      for (int n = 0; n < RN; ++n) {
        const f32x4 lo = acc[m][n], hi = SPLM ? acc[m + RM][n] : acc[m][n + RN];
        x[(wid * RM * RN + m * RN + n) * 64 + lane] = wk ? lo : hi;
        red[m][n] = wk ? hi : lo;
      }
    wait_lgkm0(); bar();
#pragma unroll
    for (int m = 0; m < RM; ++m)
#pragma unroll
      for (int n = 0; n < RN; ++n) red[m][n] += x[((wid ^ 1) * RM * RN + m * RN + n) * 64 + lane];
  }
};

constexpr int FLE = 0;
constexpr int FL0 = 32;
constexpr int FL1 = FL0 + 2048;
constexpr int FL2 = FL1 + 16 * 16;
constexpr int FL3 = FL2 + 8 * 32;
constexpr int FLW = FL3 + 8 * 32;
constexpr int FLC = FLW + 48;

__device__ __forceinline__ void publish(LAS unsigned* arr, unsigned nwaves, u64* flag, u64 tag, int lane) {
  wait_vm<0>();
  if (lane == 0) {
    const unsigned old = __hip_atomic_fetch_add(arr, 1u, RLX_WG);
    if (old == nwaves - 1) __hip_atomic_store(flag, tag, RLX_AGENT);
  }
}
__device__ __forceinline__ void lds_wait(LAS unsigned* w, unsigned want) {
  const unsigned a = (unsigned)(size_t)w;
#pragma unroll 1
  for (unsigned i = 0; i < (1u << 24); ++i) {
    unsigned v;
    asm volatile("ds_read_b32 %0, %1\n\ts_waitcnt lgkmcnt(0)" : "=v"(v) : "v"(a) : "memory");
    if (__builtin_amdgcn_readfirstlane(v) >= want) break;
    __builtin_amdgcn_s_sleep(1);
  }
}
template <int SLEEP, int NW, bool DELAY = true>
__device__ __forceinline__ void await(const u64* word, bool need, u64 tag, int lw, LAS unsigned* go, unsigned epoch) {
  if (lw == 0) {
    const u64 t0 = wall_clock64();
    if (DELAY) __builtin_amdgcn_s_sleep(PDELAY);
    for (;;) {
      bool ok = true;
#pragma unroll
      for (int i = 0; i < NW; ++i) ok &= __hip_atomic_load(word + i * 64, RLX_AGENT) == tag;
      if (__all(!need || ok)) break;
      if (wall_clock64() - t0 > 2000000ull) break;
      if (SLEEP > PSLEEP && __any(need && ok)) __builtin_amdgcn_s_sleep(PSLEEP); else __builtin_amdgcn_s_sleep(SLEEP);
    }
    __hip_atomic_store(go, epoch, RLX_WG);
  } else {
    lds_wait(go, epoch);
  }
  __builtin_amdgcn_fence(__ATOMIC_ACQUIRE, "wavefront");
}
using C1 = Cfg<1, 80, 128, 64, 1, 4, 1, 4, 6, 32, 0, 1, 0, 2>;
using C2 = Cfg<2, 64, 80, 64, 2, 1, 2, 4, 6, 20, 0, 1, 1, 6>;
using C3 = Cfg<3, 256, 128, 32, 2, 2, 1, 4, 5, 20, 0, 3, 1, 5>;
using C4 = Cfg<4, 64, 80, 64, 2, 1, 2, 4, 6, 27, 0, 0, 1, 6>;
struct Src1 {
  const char* a; const char* b; int o, c;
  static constexpr int ldA = 0, ldB = S_ * 2, AUXA = AUXH, AUXB = 0;
  __device__ __forceinline__ int kt(int k) const { const int s = (k + o) & 31; return (s & 24) | ((s + c) & 7); }
  __device__ __forceinline__ const char* baseA(int k) const { return a + kt(k) * (80 * 128); }
  __device__ __forceinline__ const char* baseB(int k, int) const { return b + kt(k) * 128; }
};
struct Src2 {
  const char* a; const char* bre; const char* bim; int rot;
  static constexpr int ldA = 0, ldB = D_ * 2, AUXA = AUXH, AUXB = 0;
  __device__ __forceinline__ int kt(int k) const { const int s = k + rot; return s >= 20 ? s - 20 : s; }
  __device__ __forceinline__ const char* baseA(int k) const { return a + kt(k) * (64 * 128); }
  __device__ __forceinline__ const char* baseB(int k, int) const { const int t = kt(k); return t < 10 ? bre + t * 128 : bim + (t - 10) * 128; }
};
struct Src3 {
  const char* a; const char* bg; const char* bu; int rot;
  static constexpr int ldA = 0, ldB = F_ * 2, AUXA = AUXH, AUXB = 0;
  __device__ __forceinline__ int kt(int k) const { const int s = k + rot; return s >= 20 ? s - 20 : s; }
  __device__ __forceinline__ const char* baseA(int k) const { return a + kt(k) * (256 * 64); }
  __device__ __forceinline__ const char* baseB(int k, int up) const { return (up ? bu : bg) + kt(k) * (32 * F_ * 2); }
};
struct Src4 {
  const char* a; const char* b;
  static constexpr int ldA = 0, ldB = 0, AUXA = AUXH, AUXB = AUXH;
  __device__ __forceinline__ const char* baseA(int k) const { return a + k * (64 * 128); }
  __device__ __forceinline__ const char* baseB(int k, int) const { return b + k * (80 * 128); }
};

struct Params {
  const u16* x; const u16* sre; const u16* sim; const u16* hre; const u16* him; const u16* wg; const u16* wu; const u16* wd;
  u16* xnI; u16* pqI; u16* hI; u16* hhI; u16* wdI; f32x2* stats; float* cs; u64* flags; u16* out;
};

template <int CTRL, int RMASK> __device__ __forceinline__ float dpp_add(float v) {
  return v + __int_as_float(__builtin_amdgcn_update_dpp(0, __float_as_int(v), CTRL, RMASK, 0xf, true));
}
__device__ __forceinline__ float wave_sum(float v) {
  v = dpp_add<0x111, 0xf>(v); v = dpp_add<0x112, 0xf>(v); v = dpp_add<0x114, 0xf>(v); v = dpp_add<0x118, 0xf>(v);
  v = dpp_add<0x142, 0xa>(v); v = dpp_add<0x143, 0xc>(v);
  return __int_as_float(__builtin_amdgcn_readlane(__float_as_int(v), 63));
}
__device__ __forceinline__ float sum_rows(float v) {
  const auto a = __builtin_amdgcn_permlane16_swap(__float_as_uint(v), __float_as_uint(v), false, false);
  v = __uint_as_float(a[0]) + __uint_as_float(a[1]);
  const auto b = __builtin_amdgcn_permlane32_swap(__float_as_uint(v), __float_as_uint(v), false, false);
  return __uint_as_float(b[0]) + __uint_as_float(b[1]);
}
__device__ __forceinline__ u32x4 col8(LAS char* tile, int cb, int lane) {
  LAS char* a = tile + ((lane >> 2) & 3) * 1296 + (cb * 64 + (lane >> 4) * 16 + (lane & 3) * 4) * 2;
  const bf16x4 lo = __builtin_amdgcn_ds_read_tr16_b64_v4i16((LAS bf16x4*)a);
  const bf16x4 hi = __builtin_amdgcn_ds_read_tr16_b64_v4i16((LAS bf16x4*)(a + 4 * 1296));
  return __builtin_bit_cast(u32x4, __builtin_shufflevector(lo, hi, 0, 1, 2, 3, 4, 5, 6, 7));
}
struct Hook3 {
  const Params& p; LAS f32x4* rs; LAS float* csl; LAS unsigned* arr; u64 tag; int mt3, j3, lw, lane;
  f32x2 (&pv)[2][8]; u64& fc; float& cv;
  static constexpr int K0 = C3::NK - C3::NSB - 1;
  static constexpr int extra(int k) { return k <= K0 + 2 ? 17 : 1; }
  template <int K> __device__ __forceinline__ void at() {
    if constexpr (K == K0) {
      fc = __hip_atomic_load(p.flags + FLC + j3 * 16, RLX_AGENT);
#pragma unroll
      for (int h = 0; h < 2; ++h)
#pragma unroll
        for (int i = 0; i < 8; ++i) pv[h][i] = __builtin_bit_cast(f32x2, ld8(p.stats + i * S_ + mt3 * 256 + lw * 128 + h * 64 + lane));
    } else if constexpr (K == K0 + 2) {
      if (!__all(fc == tag)) await<PSLEEP, 1>(p.flags + FLC + j3 * 16, true, tag, 0, arr + 11, 1u);
      __builtin_amdgcn_fence(__ATOMIC_ACQUIRE, "wavefront");
      cv = __hip_atomic_load(p.cs + j3 * 128 + lw * 64 + lane, RLX_AGENT);
#pragma unroll
      for (int h = 0; h < 2; ++h) {
        float mean = 0.f, q = 0.f, dd = 0.f;
#pragma unroll
        for (int i = 0; i < 8; ++i) { mean += pv[h][i][0]; q += pv[h][i][1]; }
        mean *= 0.125f;
#pragma unroll
        for (int i = 0; i < 8; ++i) { const float d = pv[h][i][0] - mean; dd += d * d; }
        q += 80.0f * dd;
        const float den = rbf(__builtin_amdgcn_sqrtf(rbf(rbf(q * (1.0f / D_)) + 1e-5f)));
        rs[lw * 128 + h * 64 + lane] = (f32x4){-rbf(mean), den, -1.44269504089f * __builtin_amdgcn_rcpf(den), 0.f};
      }
    } else if constexpr (K == C3::NK - 2) {
      csl[lw * 64 + lane] = cv;
      wait_lgkm0();
    }
  }
};

__global__ __launch_bounds__(NWAVES * 64) void mega(const Params p) {
  __shared__ __attribute__((aligned(1024))) char smem_[LDS_TOTAL];
  LAS char* lds = (LAS char*)smem_;
  const int wid = __builtin_amdgcn_readfirstlane(threadIdx.x >> 6), lane = threadIdx.x & 63;
  const int bid = blockIdx.x, g = bid & 7, idx = bid >> 3;
  LAS unsigned* arr = (LAS unsigned*)(lds + LDS_MISC);
  const u16* xr = p.x + (bid * 8 + wid) * D_;
  const bf16x8 v0 = *(const bf16x8*)(xr + lane * 8);
  const bf16x8 v1 = *(const bf16x8*)(xr + 512 + (lane & 15) * 8);
  const u64 ep = __hip_atomic_load(p.flags + ((wid == 7 && lane == 63) ? bid * 16 : FLE), RLX_AGENT);
  if (threadIdx.x < 16) arr[threadIdx.x] = 0u;
  const int j1 = idx & 7, mq = idx >> 3, isQ = mq >> 1, t1 = g * 256 + (mq & 1) * 128;
  const int j2 = idx & 7, mt2 = g * 4 + (idx >> 3);
  const int mt3 = (g & 6) + (idx & 1), j3 = (g & 1) * 14 + (idx >> 1);
  const bool on3 = idx < 28 && j3 < 27;
  LAS f32x4* rs = (LAS f32x4*)(lds + LDS_X);
  LAS float* csl = (LAS float*)(lds + LDS_X + 4096);

  const Src1 s1{(const char*)(p.xnI + (size_t)j1 * 256 * 80 * 8), (const char*)((isQ ? p.sim : p.sre) + (size_t)t1 * S_), (g + 2 * mq) & 7, j1};
  {
    float f[16];
#pragma unroll
    for (int i = 0; i < 8; ++i) { f[i] = bf2f((u16)v0[i]); f[8 + i] = bf2f((u16)v1[i]); }
    const float piv = __int_as_float(__builtin_amdgcn_readfirstlane(__float_as_int(f[0])));
    float s0 = 0.f, s1 = 0.f, q0 = 0.f, q1 = 0.f;
#pragma unroll
    for (int i = 0; i < 8; ++i) { const float d = f[i] - piv, e = f[8 + i] - piv; s0 += d; q0 += d * d; s1 += e; q1 += e * e; }
    const float sd = wave_sum(s0 + (lane < 16 ? s1 : 0.f)) * (1.0f / D_);
    const float qd = wave_sum(q0 + (lane < 16 ? q1 : 0.f)) * (1.0f / D_);
    const float mb = rbf(piv + sd), vb = rbf(fmaxf(qd - sd * sd, 0.f));
    const float rden = __builtin_amdgcn_rcpf(rbf(__builtin_amdgcn_sqrtf(rbf(vb + 1e-5f))));
    u32x4 w0, w1;
#pragma unroll
    for (int i = 0; i < 4; ++i) {
      w0[i] = pk2(rbf(f[2 * i] - mb) * rden, rbf(f[2 * i + 1] - mb) * rden);
      w1[i] = pk2(rbf(f[8 + 2 * i] - mb) * rden, rbf(f[9 + 2 * i] - mb) * rden);
    }
    LAS char* tb = lds + LDS_X;
    *(LAS u32x4*)(tb + wid * 1296 + lane * 16) = w0;
    if (lane < 16) *(LAS u32x4*)(tb + wid * 1296 + 1024 + lane * 16) = w1;
    wait_lgkm0(); bar();
  }
  const u64 tag = (((u64)(unsigned)__builtin_amdgcn_readfirstlane((unsigned)(ep >> 32)) << 32) | (unsigned)__builtin_amdgcn_readfirstlane((unsigned)ep)) + 1ull;
  {
    LAS char* tb = lds + LDS_X;
    auto rsrc = __builtin_amdgcn_make_buffer_rsrc(p.xnI, 0, S_ * D_ * 2, 0x00020000);
#pragma unroll
    for (int h = 0; h < 2; ++h) {
      const int cb = h * 8 + wid;
      if (cb < 10) {
        const int d = cb * 64 + lane, j = d / 80, n = d % 80;
        __builtin_amdgcn_raw_buffer_store_b128(col8(tb, cb, lane), rsrc, ((j * 256 + bid) * 80 + n) * 16, 0, AUXH);
      }
    }
    wait_vm<0>();
    unsigned old = 0;
    if (lane == 0) old = __hip_atomic_fetch_add(arr + 5, 1u, RLX_WG);
    if (__builtin_amdgcn_readfirstlane(old) == 7u && lane < 8) __hip_atomic_store(p.flags + FL0 + lane * 256 + bid, tag, RLX_AGENT);
  }
  const Src2 s2{(const char*)(p.pqI + (size_t)mt2 * 160 * 64 * 8), (const char*)(p.hre + (size_t)j2 * 80 * D_), (const char*)(p.him + (size_t)j2 * 80 * D_), (mt2 * 5) >> 3};
  const Src3 s3{(const char*)(p.hI + (size_t)mt3 * 80 * 256 * 8), (const char*)(p.wg + j3 * 64), (const char*)(p.wu + j3 * 64), (mt3 * 5) >> 1};
  const Src4 s4{(const char*)(p.hhI + (size_t)mt2 * 216 * 64 * 8), (const char*)(p.wdI + (size_t)j2 * 216 * 80 * 8)};
  if (wid >= 6) {
    const int lw = wid - 6;
    {
      Loader<C1, Src1, true> L(lds, s1, lw, lane);
      lds_wait(arr + 5, 8);
      await<PSLEEP, 4>(p.flags + FL0 + g * 256 + lane, true, tag, lw, arr + 3, 1u);
      if (bid == 0 && lw == 0 && lane == 0) __hip_atomic_store(p.flags + FLE, tag, RLX_AGENT);
      wait_vm<0>();
      L.fill(0, NRAMP); L.run<NRAMP>(NoHook{});
      bar();
    }
    {
      Loader<C2, Src2, true> L(lds, s2, lw, lane);
      lds_wait(arr + 0, C1::NC);
      await<PSLEEP, 1>(p.flags + FL1 + ((mt2 >> 1) * 16 + (lane & 15)), true, tag, lw, arr + 3, 2u);
      wait_vm<0>();
      L.fill(0, NRAMP); L.run<NRAMP>(NoHook{});
      bar(); bar();
    }
    if (on3) {
      Loader<C3, Src3, true> L(lds, s3, lw, lane);
      lds_wait(arr + 1, C2::NC);
      await<PSLEEP, 1>(p.flags + FL2 + (mt3 * 32 + (lane & 31)), true, tag, lw, arr + 3, 3u);
      wait_vm<0>();
      L.fill(0, NRAMP); L.run<NRAMP>(NoHook{});
      bar();
    }
    {
      Loader<C4, Src4, true> L(lds, s4, lw, lane);
      lds_wait(arr + 2, 4);
      if (on3) await<PSLEEP, 1>(p.flags + FL3 + (g * 32 + (lane & 31)), (lane & 31) < 27, tag, lw, arr + 3, 4u);
      else await<ISLEEP, 1>(p.flags + FL3 + (g * 32 + (lane & 31)), (lane & 31) < 27, tag, lw, arr + 3, 4u);
      wait_vm<0>();
      L.fill(0, NRAMP); L.run<NRAMP>(NoHook{});
      bar(); bar();
    }
    return;
  }
  if (wid >= 4) {
    const int lw = wid - 4;
    {
      Loader<C1, Src1, false> L(lds, s1, lw, lane);
      lds_wait(arr + 5, 8); L.fill(0, C1::NPRE);
      L.run<C1::NPRE>(NoHook{});
      bar();
    }
    {
      Loader<C2, Src2, false> L(lds, s2, lw, lane);
      lds_wait(arr + 0, C1::NC); L.fill(0, C2::NPRE);
      L.run<C2::NPRE>(NoHook{});
      bar(); bar();
    }
    if (on3) {
      Loader<C3, Src3, false> L(lds, s3, lw, lane);
      static_assert(Hook3::K0 + 2 < C3::NK - 2, "");
      f32x2 pv[2][8]; u64 fc; float cv;
      lds_wait(arr + 1, C2::NC); L.fill(0, C3::NPRE);
      L.run<C3::NPRE>(Hook3{p, rs, csl, arr, tag, mt3, j3, lw, lane, pv, fc, cv});
      bar();
    }
    {
      Loader<C4, Src4, false> L(lds, s4, lw, lane);
      await<ISLEEP, 1, false>(p.flags + FLW + min(lane, 39), true, tag, lw, arr + 13, 1u);
      wait_vm<0>();
      lds_wait(arr + 2, 4); L.fill(0, C4::NPRE);
      L.run<C4::NPRE>(NoHook{});
      bar(); bar();
    }
    return;
  }

#define FRESH_LANE int lane_ = threadIdx.x & 63; asm volatile("" : "+v"(lane_)); const int lane = lane_, fr = lane & 15, fq = lane >> 4; (void)fr; (void)fq;
  {
    FRESH_LANE
    Consumer<C1> L(lds, wid, lane);
    f32x4 acc[C1::MF][C1::NF];
    L.run(acc, [] {});
    const float sg = isQ ? -1.f : 1.f;
#pragma unroll
    for (int n = 0; n < 2; ++n) {
      const int t = t1 + L.wn * 32 + n * 16 + fr, mt = t >> 6, r = t & 63;
#pragma unroll
      for (int m = 0; m < 5; ++m) {
        const int koct = isQ * 80 + j1 * 10 + m * 2 + (fq >> 1);
        u32x2 w; w[0] = pk2(sg * acc[m][n][0], sg * acc[m][n][1]); w[1] = pk2(sg * acc[m][n][2], sg * acc[m][n][3]);
        st8(p.pqI + (((size_t)mt * 160 + koct) * 64 + r) * 8 + (fq & 1) * 4, w);
      }
    }
    publish(arr + 0, C1::NC, p.flags + FL1 + ((t1 >> 7) * 16 + isQ * 8 + j1), tag, lane);
  }
  {
    FRESH_LANE
    Consumer<C2> L(lds, wid, lane);
    f32x4 acc[C2::MF][C2::NF];
    const int t = mt2 * 64 + wid * 16 + fr;
    bf16x4 xv[C2::NF];
#pragma unroll
    for (int n = 0; n < C2::NF; ++n) xv[n] = *(const bf16x4*)(p.x + (size_t)t * D_ + j2 * 80 + n * 16 + fq * 4);
#pragma unroll
    for (int n = 0; n < C2::NF; ++n) asm volatile("" : "+v"(xv[n]));
    L.run(acc, [] {});
    f32x4 red[1][5];
    L.reduce_k(acc, red);
    float hv[C2::NF][4];
    float s = 0.f;
#pragma unroll
    for (int n = 0; n < C2::NF; ++n) {
      u32x2 w;
      w[0] = pk2(red[0][n][0] + bf2f((u16)xv[n][0]), red[0][n][1] + bf2f((u16)xv[n][1]));
      w[1] = pk2(red[0][n][2] + bf2f((u16)xv[n][2]), red[0][n][3] + bf2f((u16)xv[n][3]));
      hv[n][0] = __uint_as_float(w[0] << 16); hv[n][1] = __uint_as_float(w[0] & 0xffff0000u);
      hv[n][2] = __uint_as_float(w[1] << 16); hv[n][3] = __uint_as_float(w[1] & 0xffff0000u);
      s += (hv[n][0] + hv[n][1]) + (hv[n][2] + hv[n][3]);
      const int koct = j2 * 10 + n * 2 + (fq >> 1);
      st8(p.hI + (((size_t)(t >> 8) * 80 + koct) * 256 + (t & 255)) * 8 + (fq & 1) * 4, w);
      *(LAS u32x2*)(lds + LDS_H + ((wid * 5 + n) * 64 + lane) * 8) = w;
    }
    const float mean = sum_rows(s) * (1.0f / 80.0f);
    float q = 0.f;
#pragma unroll
    for (int n = 0; n < C2::NF; ++n)
#pragma unroll
      for (int r = 0; r < 4; ++r) { const float d = hv[n][r] - mean; q += d * d; }
    q = sum_rows(q);
    if (fq == 0) st8(p.stats + j2 * S_ + t, __builtin_bit_cast(u32x2, (f32x2){mean, q}));
    publish(arr + 1, C2::NC, p.flags + FL2 + (g * 32 + idx), tag, lane);
  }
  if (on3) {
    FRESH_LANE
    Consumer<C3> L(lds, wid, lane);
    f32x4 acc[C3::MF][C3::NF];
    f32x4 st[C3::MF], cg[2], cu[2];
    L.run(acc, [&] {
#pragma unroll
      for (int m = 0; m < C3::MF; ++m) st[m] = rs[L.wm * 128 + m * 16 + fr];
#pragma unroll
      for (int i = 0; i < 2; ++i) {
        const int fl = L.wn * 32 + i * 16 + fq * 4;
        cg[i] = *(const LAS f32x4*)(csl + fl); cu[i] = *(const LAS f32x4*)(csl + 64 + fl);
      }
    });
    u16* hb = p.hhI + (((size_t)(mt3 * 4 + L.wm * 2) * 216 + j3 * 8 + L.wn * 4 + (fq >> 1)) * 64 + fr) * 8 + (fq & 1) * 4;
#pragma unroll
    for (int m = 0; m < C3::MF; ++m) {
      u32x2 w[2];
#pragma unroll
      for (int i = 0; i < 2; ++i) {
        const f32x4 s0 = {st[m][0], st[m][0], st[m][0], st[m][0]}, s1 = {st[m][1], st[m][1], st[m][1], st[m][1]};
        const f32x4 x = __builtin_elementwise_fma(cg[i], s0, acc[m][i]) * st[m][2];
        f32x4 e;
#pragma unroll
        for (int r = 0; r < 4; ++r) e[r] = __builtin_amdgcn_exp2f(x[r]);
        const f32x4 d = __builtin_elementwise_fma(e, s1, s1);
        f32x4 rc;
#pragma unroll
        for (int r = 0; r < 4; ++r) rc[r] = __builtin_amdgcn_rcpf(d[r]);
        const f32x4 o = __builtin_elementwise_fma(cu[i], s0, acc[m][i + 2]) * rc;
        w[i][0] = pk2(o[0], o[1]); w[i][1] = pk2(o[2], o[3]);
        st8(hb + (m >> 2) * (216 * 64 * 8) + i * (2 * 64 * 8) + (m & 3) * 128, w[i]);
      }
    }
    publish(arr + 2, C3::NC, p.flags + FL3 + (mt3 * 32 + j3), tag, lane);
  } else {
    FRESH_LANE
    const int id = idx >= 28 ? g * 4 + idx - 28 : 32 + (g >> 1) * 2 + idx - 26;
    auto rsrc = __builtin_amdgcn_make_buffer_rsrc(p.wdI, 0, F_ * D_ * 2, 0x00020000);
    lds_wait(arr + 1, C2::NC);
    if (id < 27) {
      const u16* w = ((wid & 2) ? p.wu : p.wg) + (size_t)((wid & 1) * 320 + (lane >> 3)) * F_ + id * 64 + (lane & 7) * 8;
      f32x2 a[4] = {{0.f, 0.f}, {0.f, 0.f}, {0.f, 0.f}, {0.f, 0.f}};
#pragma unroll 1
      for (int b = 0; b < 2; ++b) {
        u32x4 v[20];
#pragma unroll
        for (int i = 0; i < 20; ++i) v[i] = *(const u32x4*)(w + (size_t)(b * 20 + i) * 8 * F_);
#pragma unroll
        for (int i = 0; i < 20; ++i)
#pragma unroll
          for (int e = 0; e < 4; ++e) a[e] += (f32x2){__uint_as_float(v[i][e] << 16), __uint_as_float(v[i][e] & 0xffff0000u)};
      }
      LAS float* sf = (LAS float*)(lds + LDS_X);
#pragma unroll
      for (int e = 0; e < 4; ++e)
#pragma unroll
        for (int c = 0; c < 2; ++c) { float t = sum_rows(a[e][c]); t += __shfl_xor(t, 8); a[e][c] = t; }
      if (lane < 8) {
        *(LAS f32x4*)(sf + wid * 64 + lane * 8) = (f32x4){a[0][0], a[0][1], a[1][0], a[1][1]};
        *(LAS f32x4*)(sf + wid * 64 + lane * 8 + 4) = (f32x4){a[2][0], a[2][1], a[3][0], a[3][1]};
      }
      wait_lgkm0();
      unsigned old = 0;
      if (lane == 0) old = __hip_atomic_fetch_add(arr + 12, 1u, RLX_WG);
      if (__builtin_amdgcn_readfirstlane(old) == 3u) {
        __hip_atomic_store(p.cs + id * 128 + lane, sf[lane] + sf[64 + lane], RLX_AGENT);
        __hip_atomic_store(p.cs + id * 128 + 64 + lane, sf[128 + lane] + sf[192 + lane], RLX_AGENT);
        wait_vm<0>();
        if (lane == 0) __hip_atomic_store(p.flags + FLC + id * 16, tag, RLX_AGENT);
      }
    }
    const int k0 = id < 27 ? id * 3 : 81 + (id - 27) * 11, nk = id < 27 ? 3 : min(11, 216 - k0);
#pragma unroll 1
    for (int i = 0; i < 2 * nk; ++i) {
      const int row = (i & 1) * 4 + wid;
      const char* wr = (const char*)(p.wd + (size_t)((k0 + (i >> 1)) * 8 + row) * D_);
      set_m0((unsigned)(size_t)lds + (i >> 1) * 10368 + row * 1296);
      dma16<0, 0>(wr, lane * 16);
      if (lane < 16) dma16<0, 1024>(wr, lane * 16);
    }
    wait_vm<0>();
    if (lane == 0) __hip_atomic_fetch_add(arr + 6, 1u, RLX_WG);
    lds_wait(arr + 6, 4);
#pragma unroll 1
    for (int i = 0; i < nk; ++i) {
#pragma unroll
      for (int h = 0; h < 3; ++h) {
        const int cb = h * 4 + wid;
        if (cb < 10) {
          const int d = cb * 64 + lane, j = d / 80, n = d % 80;
          __builtin_amdgcn_raw_buffer_store_b128(col8(lds + i * 10368, cb, lane), rsrc, ((j * 216 + k0 + i) * 80 + n) * 16, 0, AUXH);
        }
      }
    }
    publish(arr + 2, 4, p.flags + FLW + id, tag, lane);
  }
  {
    FRESH_LANE
    Consumer<C4> L(lds, wid, lane);
    f32x4 acc[C4::MF][C4::NF];
    const int t = mt2 * 64 + wid * 16 + fr;
    u32x2 hv[5];
    L.run(acc, [] {});
#pragma unroll
    for (int n = 0; n < 5; ++n) hv[n] = *(const LAS u32x2*)(lds + LDS_H + ((wid * 5 + n) * 64 + lane) * 8);
    f32x4 red[1][5];
    L.reduce_k(acc, red);
#pragma unroll
    for (int n = 0; n < 5; ++n) {
      u32x2 w;
      w[0] = pk2(red[0][n][0] + __uint_as_float(hv[n][0] << 16), red[0][n][1] + __uint_as_float(hv[n][0] & 0xffff0000u));
      w[1] = pk2(red[0][n][2] + __uint_as_float(hv[n][1] << 16), red[0][n][3] + __uint_as_float(hv[n][1] & 0xffff0000u));
      *(u32x2*)(p.out + (size_t)t * D_ + j2 * 80 + n * 16 + fq * 4) = w;
    }
  }
}

extern "C" void kernel_launch(void* const* d_in, const int* in_sizes, int n_in, void* d_out, int out_size,
                              void* d_ws, size_t ws_size, hipStream_t stream) {
  char* ws = (char*)d_ws;
  Params p;
  p.x = (const u16*)d_in[0]; p.sre = (const u16*)d_in[1]; p.sim = (const u16*)d_in[2];
  p.hre = (const u16*)d_in[3]; p.him = (const u16*)d_in[4];
  p.wg = (const u16*)d_in[6]; p.wu = (const u16*)d_in[7]; p.wd = (const u16*)d_in[8];
  p.flags = (u64*)ws;
  p.cs = (float*)(ws + 262144);
  p.stats = (f32x2*)(ws + 524288);
  p.xnI = (u16*)(ws + (1u << 20));
  p.pqI = (u16*)(ws + (4u << 20));
  p.hI = (u16*)(ws + (10u << 20));
  p.hhI = (u16*)(ws + (13u << 20));
  p.wdI = (u16*)(ws + (21u << 20));
  p.out = (u16*)d_out;
  mega<<<256, NWAVES * 64, 0, stream>>>(p);
}
```

```cpp
#include <hip/hip_runtime.h>
#include <hip/hip_bf16.h>

#define EARLY 1
#define PD0 20
#define PR1 2
#define PD1 40
#define PD2 28
#define PD3 40
#define PR2 6
#define PR3 3
#define PR4 6
constexpr int PSLEEP = 4;
constexpr int ISLEEP = 16;
constexpr int PDELAY = 40;
constexpr int NRAMP = 3;
constexpr int AUXH = 16;

typedef unsigned short u16;
typedef unsigned long long u64;
typedef short bf16x8 __attribute__((ext_vector_type(8)));
typedef short bf16x4 __attribute__((ext_vector_type(4)));
typedef float f32x4 __attribute__((ext_vector_type(4)));
typedef float f32x2 __attribute__((ext_vector_type(2)));
typedef unsigned u32x2 __attribute__((ext_vector_type(2)));
typedef unsigned u32x4 __attribute__((ext_vector_type(4)));
#define LAS __attribute__((address_space(3)))
#define RLX_AGENT __ATOMIC_RELAXED, __HIP_MEMORY_SCOPE_AGENT
#define RLX_WG __ATOMIC_RELAXED, __HIP_MEMORY_SCOPE_WORKGROUP

constexpr int S_ = 2048, D_ = 640, F_ = 1728;

__device__ __forceinline__ float bf2f(u16 v) { return __uint_as_float(((unsigned)v) << 16); }
typedef __bf16 bf16x2n __attribute__((ext_vector_type(2)));
__device__ __forceinline__ unsigned pk2(float lo, float hi) {
  return __builtin_bit_cast(unsigned, __builtin_convertvector((f32x2){lo, hi}, bf16x2n));
}
__device__ __forceinline__ float rbf(float v) { return __uint_as_float(pk2(v, v) << 16); }

#define CSTAMP(kid, i)
#define STAMP(kid, i)
template <int N> __device__ __forceinline__ void wait_vm() { asm volatile("s_waitcnt vmcnt(%0)" ::"n"(N) : "memory"); }
__device__ __forceinline__ void wait_lgkm0() { asm volatile("" ::: "memory"); __builtin_amdgcn_s_waitcnt(0xC07F); asm volatile("" ::: "memory"); }
__device__ __forceinline__ void bar() { asm volatile("" ::: "memory"); __builtin_amdgcn_s_barrier(); asm volatile("" ::: "memory"); }
template <int AUX, int OFF> __device__ __forceinline__ void dma16(const char* sbase, unsigned voff) {
  if constexpr (AUX == 16) asm volatile("global_load_lds_dwordx4 %0, %1 offset:%2 sc1" :: "v"(voff), "s"(sbase), "n"(OFF) : "memory");
  else asm volatile("global_load_lds_dwordx4 %0, %1 offset:%2" :: "v"(voff), "s"(sbase), "n"(OFF) : "memory");
}
__device__ __forceinline__ void set_m0(unsigned v) { asm volatile("s_mov_b32 m0, %0\n\ts_nop 0" :: "s"(v) : "memory"); }
__device__ __forceinline__ void st8(void* p, u32x2 v) { __hip_atomic_store((u64*)p, __builtin_bit_cast(u64, v), RLX_AGENT); }
__device__ __forceinline__ u32x2 ld8(const void* p) { return __builtin_bit_cast(u32x2, __hip_atomic_load((u64*)p, RLX_AGENT)); }

constexpr int NL = 2;
constexpr int NWAVES = 8;
constexpr int LDS_H = 120 * 1024;
constexpr int LDS_RING = 136 * 1024;
constexpr int LDS_X = LDS_RING;
constexpr int LDS_TOTAL = 160 * 1024;
constexpr int LDS_MISC = LDS_TOTAL - 256;

template <int KID_, int BM_, int BN_, int BK_, int WM_, int WN_, int WK_, int NSA_, int NSB_, int NK_, int AMODE_, int BMODE_, int SWAP_, int NPRE_>
struct Cfg {
  static constexpr int KID = KID_, BM = BM_, BN = BN_, BK = BK_, WM = WM_, WN = WN_, WK = WK_, NSA = NSA_, NSB = NSB_, NK = NK_;
  static constexpr int AMODE = AMODE_, BMODE = BMODE_, SWAP = SWAP_, NPRE = NPRE_;
  static constexpr int NC = WM * WN * WK;
  static constexpr int MF = BM / WM / 16, NF = BN / WN / 16, KSW = BK / 32 / WK;
  static constexpr int ABYTES = BM * BK * 2, BBYTES = BN * BK * 2;
  static constexpr int PA = ABYTES / 1024, PB = BBYTES / 1024;
  static constexpr int GA = PA / NL, GB = PB / NL, G = GA + GB;
  static constexpr int RA = NSA * ABYTES, RB = NSB * BBYTES;
  static_assert(PA % NL == 0 && PB % NL == 0, "pieces");
  static_assert(RA + RB <= (KID == 1 ? LDS_RING : LDS_H), "lds");
  static_assert(NSA <= NK && NSB <= NK, "stages");
  static_assert(KSW >= 1 && KSW <= 2, "ksw");
};

template <class C> struct Frags { bf16x8 a[C::KSW][C::MF]; bf16x8 b[C::KSW][C::NF]; };

template <class C, class Src, bool ISA>
struct Loader {
  static constexpr int G = ISA ? C::GA : C::GB, NS = ISA ? C::NSA : C::NSB, BYTES = ISA ? C::ABYTES : C::BBYTES;
  static constexpr int RING = NS * BYTES, MODE = ISA ? C::AMODE : C::BMODE, AUX = ISA ? Src::AUXA : Src::AUXB;
  const Src& src;
  int lw;
  unsigned lbase, cur;
  unsigned voff[G];
  __device__ __forceinline__ Loader(LAS char* lds, const Src& s, int lw_, int lane) : src(s), lw(lw_) {
    lbase = (unsigned)(size_t)lds + (ISA ? 0 : C::RA) + lw * (G * 1024);
#pragma unroll
    for (int i = 0; i < G; ++i) {
      const int p = (lw * G + i) * 64 + lane;
      unsigned v;
      if constexpr (MODE == 0) v = p * 16;
      else if constexpr (MODE == 1) { const int row = p >> 3, c = (p & 7) ^ ((row >> 1) & 7); v = row * (ISA ? Src::ldA : Src::ldB) + c * 16; }
      else { const int q = p % (C::BK * 8), k = q >> 3, c = (q & 7) ^ ((((k >> 3) & 1) << 2) | (((k >> 1) & 1) << 1)); v = k * Src::ldB + c * 16; }
      voff[i] = v - (i & 3) * 1024;
    }
  }
  template <int I> __device__ __forceinline__ void pieces(const char* gb, unsigned l) {
    if constexpr (I < G) {
      if constexpr ((I & 3) == 0) set_m0(l + I * 1024);
      dma16<AUX, (I & 3) * 1024>(gb, voff[I]);
      pieces<I + 1>(gb, l);
    }
  }
  __device__ __forceinline__ void issue(int k, unsigned buf) {
    pieces<0>(ISA ? src.baseA(k) : src.baseB(k, lw), lbase + buf);
  }
  __device__ __forceinline__ void fill(int s0, int s1) {
#pragma unroll 1
    for (int s = s0; s < s1; ++s) issue(s, s * BYTES);
  }
  template <int K, class Hook> __device__ __forceinline__ void tail(Hook hook) {
    if constexpr (K < C::NK - 1) {
      wait_vm<(C::NK - 2 - K) * G + Hook::extra(K)>();
      bar();
      hook.template at<K>();
      tail<K + 1>(hook);
    }
  }
  template <int N0, class Hook> __device__ __forceinline__ void run(Hook hook) {
    static_assert(N0 >= 1 && N0 <= NS && (NS - 1) * G <= 63, "ring");
    wait_vm<(N0 - 1) * G>();
    bar();
    fill(N0, NS);
    cur = 0;
#pragma unroll 1
    for (int k = 0; k < C::NK - NS; ++k) {
      wait_vm<(NS - 2) * G>();
      bar();
      issue(k + NS, cur);
      cur += BYTES; if (cur == RING) cur = 0;
    }
    hook.template at<C::NK - NS - 1>();
    tail<C::NK - NS>(hook);
  }
};
struct NoHook {
  static constexpr int extra(int) { return 0; }
  template <int K> __device__ __forceinline__ void at() {}
};

template <class C>
struct Consumer {
  LAS char* lds;
  int wid, lane, wm, wn, wk;
  unsigned rdA[2], rdB[4];

  __device__ __forceinline__ Consumer(LAS char* lds_, int wid_, int lane_) : lds(lds_), wid(wid_), lane(lane_) {
    wk = wid % C::WK; wn = (wid / C::WK) % C::WN; wm = wid / (C::WK * C::WN);
    const int fr = lane & 15, fq = lane >> 4;
    const int arow = wm * (C::BM / C::WM) + fr;
    const int ss0 = wk * C::KSW;
#pragma unroll
    for (int s = 0; s < 2; ++s) {
      const int ss = ss0 + s;
      if constexpr (C::AMODE == 0) rdA[s] = ((ss0 * 4 + fq) * C::BM + arow) * 16;
      else rdA[s] = arow * 128 + ((((ss & 1) * 4 + fq) ^ ((fr >> 1) & 7)) * 16);
    }
    if constexpr (C::BMODE <= 1) {
      const int brow = wn * (C::BN / C::WN) + fr;
#pragma unroll
      for (int s = 0; s < 2; ++s) {
        const int ss = ss0 + s;
        if constexpr (C::BMODE == 0) rdB[s] = C::RA + ((ss0 * 4 + fq) * C::BN + brow) * 16;
        else rdB[s] = C::RA + brow * 128 + ((((ss & 1) * 4 + fq) ^ ((fr >> 1) & 7)) * 16);
      }
    } else {
      const int g = lane >> 4, q = (lane >> 2) & 3, p = lane & 3;
#pragma unroll
      for (int j = 0; j < 4; ++j) {
        const int jj = 2 * wn + (j & 1), mat = j >> 1;
        rdB[j] = C::RA + mat * (C::BK * 128) + (32 * ss0 + 8 * g + q) * 128 + ((((jj ^ (((g & 1) << 1) | (q >> 1))) << 1) | (p >> 1)) * 16) + 8 * (p & 1);
      }
    }
  }

  __device__ __forceinline__ void read(Frags<C>& f, unsigned bufa, unsigned bufb) {
    LAS char* a = lds + bufa; LAS char* b = lds + bufb;
#pragma unroll
    for (int s = 0; s < C::KSW; ++s) {
      if constexpr (C::BMODE == 0) {
#pragma unroll
        for (int n = 0; n < C::NF; ++n) f.b[s][n] = *(const LAS bf16x8*)(b + rdB[0] + s * (C::BN * 64) + n * 256);
      } else if constexpr (C::BMODE == 1) {
#pragma unroll
        for (int n = 0; n < C::NF; ++n) f.b[s][n] = *(const LAS bf16x8*)(b + rdB[s] + n * 2048);
      } else {
#pragma unroll
        for (int n = 0; n < C::NF; ++n) {
          bf16x4 lo = __builtin_amdgcn_ds_read_tr16_b64_v4i16((LAS bf16x4*)(b + rdB[n] + s * 4096));
          bf16x4 hi = __builtin_amdgcn_ds_read_tr16_b64_v4i16((LAS bf16x4*)(b + rdB[n] + s * 4096 + 512));
          f.b[s][n] = __builtin_shufflevector(lo, hi, 0, 1, 2, 3, 4, 5, 6, 7);
        }
      }
      if constexpr (C::AMODE == 0) {
#pragma unroll
        for (int m = 0; m < C::MF; ++m) f.a[s][m] = *(const LAS bf16x8*)(a + rdA[0] + s * (C::BM * 64) + m * 256);
      } else {
#pragma unroll
        for (int m = 0; m < C::MF; ++m) f.a[s][m] = *(const LAS bf16x8*)(a + rdA[s] + m * 2048);
      }
    }
  }

  __device__ __forceinline__ void mma(f32x4 (&acc)[C::MF][C::NF], const Frags<C>& f) {
#pragma unroll
    for (int s = 0; s < C::KSW; ++s)
#pragma unroll
      for (int m = 0; m < C::MF; ++m)
#pragma unroll
        for (int n = 0; n < C::NF; ++n)
          acc[m][n] = C::SWAP ? __builtin_amdgcn_mfma_f32_16x16x32_bf16(f.b[s][n], f.a[s][m], acc[m][n], 0, 0, 0)
                              : __builtin_amdgcn_mfma_f32_16x16x32_bf16(f.a[s][m], f.b[s][n], acc[m][n], 0, 0, 0);
  }
  static constexpr int NRD = C::KSW * (C::MF + (C::BMODE >= 2 ? 2 : 1) * C::NF);
  static constexpr int NMM = C::KSW * C::MF * C::NF;
  __device__ __forceinline__ void interleave() {
    constexpr int RPM = (NRD + NMM - 3) / (NMM - 2);
#pragma unroll
    for (int i = 0; i < NMM; ++i) {
      __builtin_amdgcn_sched_group_barrier(0x100, RPM, 0);
      __builtin_amdgcn_sched_group_barrier(0x008, 1, 0);
    }
  }
  unsigned na, nb;
  __device__ __forceinline__ void step(f32x4 (&acc)[C::MF][C::NF], Frags<C>& cur, Frags<C>& nxt) {
    na += C::ABYTES; if (na == C::RA) na = 0;
    nb += C::BBYTES; if (nb == C::RB) nb = 0;
    wait_lgkm0(); bar();
    __builtin_amdgcn_sched_barrier(0);
    read(nxt, na, nb);
    mma(acc, cur);
    interleave();
    __builtin_amdgcn_sched_barrier(0);
  }

  template <class Hook> __device__ __forceinline__ void run(f32x4 (&acc)[C::MF][C::NF], Hook hook) {
    Frags<C> f0, f1;
#pragma unroll
    for (int m = 0; m < C::MF; ++m)
#pragma unroll
      for (int n = 0; n < C::NF; ++n) { acc[m][n] = (f32x4){0.f, 0.f, 0.f, 0.f}; asm volatile("" : "+v"(acc[m][n])); }
    bar();
    if (wid == 0) { STAMP(C::KID, 1); CSTAMP(C::KID, 8); }
    read(f0, 0, 0);
    na = 0; nb = 0;
    if (wid == 0) STAMP(C::KID, 4);
#pragma unroll 1
    for (int k = 0; k + 2 < C::NK; k += 2) {
      step(acc, f0, f1);
      step(acc, f1, f0);
    }
    if (wid == 0) STAMP(C::KID, 5);
    if constexpr (C::NK % 2 == 0) {
      step(acc, f0, f1);
      wait_lgkm0(); bar();
      hook();
      mma(acc, f1);
    } else {
      wait_lgkm0(); bar();
      mma(acc, f0);
    }
    if (wid == 0) { STAMP(C::KID, 2); CSTAMP(C::KID, 9); }
  }

  static constexpr bool SPLM = (C::MF % 2 == 0);
  static constexpr int RM = SPLM ? C::MF / 2 : C::MF, RN = SPLM ? C::NF : C::NF / 2;
  __device__ __forceinline__ void reduce_k(f32x4 (&acc)[C::MF][C::NF], f32x4 (&red)[RM][RN]) {
    static_assert(C::WK == 2, "");
    static_assert(LDS_X + C::NC * RM * RN * 1024 <= LDS_MISC, "");
    LAS f32x4* x = (LAS f32x4*)(lds + LDS_X);
#pragma unroll
    for (int m = 0; m < RM; ++m)
#pragma unroll
      for (int n = 0; n < RN; ++n) {
        const f32x4 lo = acc[m][n], hi = SPLM ? acc[m + RM][n] : acc[m][n + RN];
        x[(wid * RM * RN + m * RN + n) * 64 + lane] = wk ? lo : hi;
        red[m][n] = wk ? hi : lo;
      }
    wait_lgkm0(); bar();
#pragma unroll
    for (int m = 0; m < RM; ++m)
#pragma unroll
      for (int n = 0; n < RN; ++n) red[m][n] += x[((wid ^ 1) * RM * RN + m * RN + n) * 64 + lane];
  }
};

constexpr int FLE = 0;
constexpr int FL0 = 32;
constexpr int FL1 = FL0 + 2048;
constexpr int FL2 = FL1 + 16 * 16;
constexpr int FL3 = FL2 + 8 * 32;
constexpr int FLW = FL3 + 8 * 32;
constexpr int FLC = FLW + 48;

__device__ __forceinline__ void publish(LAS unsigned* arr, unsigned nwaves, u64* flag, u64 tag, int lane, int kid) {
  if (threadIdx.x < 64) STAMP(kid, 7);
  wait_vm<0>();
  if (lane == 0) {
    const unsigned old = __hip_atomic_fetch_add(arr, 1u, RLX_WG);
    if (old == nwaves - 1) {
      __hip_atomic_store(flag, tag, RLX_AGENT);
      STAMP(kid, 6);
    }
  }
}
__device__ __forceinline__ void lds_wait(LAS unsigned* w, unsigned want) {
  const unsigned a = (unsigned)(size_t)w;
#pragma unroll 1
  for (unsigned i = 0; i < (1u << 24); ++i) {
    unsigned v;
    asm volatile("ds_read_b32 %0, %1\n\ts_waitcnt lgkmcnt(0)" : "=v"(v) : "v"(a) : "memory");
    if (__builtin_amdgcn_readfirstlane(v) >= want) break;
    __builtin_amdgcn_s_sleep(1);
  }
}
template <int SLEEP, int NW, int DELAY = PDELAY>
__device__ __forceinline__ void await(const u64* word, bool need, u64 tag, int lw, LAS unsigned* go, unsigned epoch) {
  if (lw == 0) {
    const u64 t0 = wall_clock64();
    if (DELAY) __builtin_amdgcn_s_sleep(DELAY);
    for (;;) {
      bool ok = true;
#pragma unroll
      for (int i = 0; i < NW; ++i) ok &= __hip_atomic_load(word + i * 64, RLX_AGENT) == tag;
      if (__all(!need || ok)) break;
      if (wall_clock64() - t0 > 2000000ull) break;
      if (SLEEP > PSLEEP && __any(need && ok)) __builtin_amdgcn_s_sleep(PSLEEP); else __builtin_amdgcn_s_sleep(SLEEP);
    }
    __hip_atomic_store(go, epoch, RLX_WG);
  } else {
    lds_wait(go, epoch);
  }
  __builtin_amdgcn_fence(__ATOMIC_ACQUIRE, "wavefront");
}
using C1 = Cfg<1, 80, 128, 64, 1, 4, 1, 4, 6, 32, 0, 1, 0, PR1>;
using C2 = Cfg<2, 64, 80, 64, 2, 1, 2, 4, 6, 20, 0, 1, 1, PR2>;
using C3 = Cfg<3, 256, 128, 32, 2, 2, 1, 4, 5, 20, 0, 3, 1, PR3>;
using C4 = Cfg<4, 64, 80, 64, 2, 1, 2, 4, 6, 27, 0, 0, 1, PR4>;
struct Src1 {
  const char* a; const char* bre; const char* bim; int o, c;
  static constexpr int ldA = 0, ldB = S_ * 2, AUXA = AUXH, AUXB = 0;
  __device__ __forceinline__ int kt(int k) const { const int s = (k + o) & 31; return (s & 24) | ((s + c) & 7); }
  __device__ __forceinline__ const char* baseA(int k) const { return a + kt(k) * (80 * 128); }
  __device__ __forceinline__ const char* baseB(int k, int im) const { return (im ? bim : bre) + kt(k) * 128; }
};
struct Src2 {
  const char* a; const char* bre; const char* bim; int rot;
  static constexpr int ldA = 0, ldB = D_ * 2, AUXA = AUXH, AUXB = 0;
  __device__ __forceinline__ int kt(int k) const { const int s = k + rot; return s >= 20 ? s - 20 : s; }
  __device__ __forceinline__ const char* baseA(int k) const { return a + kt(k) * (64 * 128); }
  __device__ __forceinline__ const char* baseB(int k, int) const { const int t = kt(k); return t < 10 ? bre + t * 128 : bim + (t - 10) * 128; }
};
struct Src3 {
  const char* a; const char* bg; const char* bu; int rot;
  static constexpr int ldA = 0, ldB = F_ * 2, AUXA = AUXH, AUXB = 0;
  __device__ __forceinline__ int kt(int k) const { const int s = k + rot; return s >= 20 ? s - 20 : s; }
  __device__ __forceinline__ const char* baseA(int k) const { return a + kt(k) * (256 * 64); }
  __device__ __forceinline__ const char* baseB(int k, int up) const { return (up ? bu : bg) + kt(k) * (32 * F_ * 2); }
};
struct Src4 {
  const char* a; const char* b;
  static constexpr int ldA = 0, ldB = 0, AUXA = AUXH, AUXB = AUXH;
  __device__ __forceinline__ const char* baseA(int k) const { return a + k * (64 * 128); }
  __device__ __forceinline__ const char* baseB(int k, int) const { return b + k * (80 * 128); }
};

struct Params {
  const u16* x; const u16* sre; const u16* sim; const u16* hre; const u16* him; const u16* wg; const u16* wu; const u16* wd;
  u16* xnI; u16* pqI; u16* hI; u16* hhI; u16* wdI; f32x2* stats; float* cs; u64* flags; u16* out;
};

template <int CTRL, int RMASK> __device__ __forceinline__ float dpp_add(float v) {
  return v + __int_as_float(__builtin_amdgcn_update_dpp(0, __float_as_int(v), CTRL, RMASK, 0xf, true));
}
__device__ __forceinline__ float wave_sum(float v) {
  v = dpp_add<0x111, 0xf>(v); v = dpp_add<0x112, 0xf>(v); v = dpp_add<0x114, 0xf>(v); v = dpp_add<0x118, 0xf>(v);
  v = dpp_add<0x142, 0xa>(v); v = dpp_add<0x143, 0xc>(v);
  return __int_as_float(__builtin_amdgcn_readlane(__float_as_int(v), 63));
}
__device__ __forceinline__ float sum_rows(float v) {
  const auto a = __builtin_amdgcn_permlane16_swap(__float_as_uint(v), __float_as_uint(v), false, false);
  v = __uint_as_float(a[0]) + __uint_as_float(a[1]);
  const auto b = __builtin_amdgcn_permlane32_swap(__float_as_uint(v), __float_as_uint(v), false, false);
  return __uint_as_float(b[0]) + __uint_as_float(b[1]);
}
__device__ __forceinline__ u32x4 col8(LAS char* tile, int cb, int lane) {
  LAS char* a = tile + ((lane >> 2) & 3) * 1296 + (cb * 64 + (lane >> 4) * 16 + (lane & 3) * 4) * 2;
  const bf16x4 lo = __builtin_amdgcn_ds_read_tr16_b64_v4i16((LAS bf16x4*)a);
  const bf16x4 hi = __builtin_amdgcn_ds_read_tr16_b64_v4i16((LAS bf16x4*)(a + 4 * 1296));
  return __builtin_bit_cast(u32x4, __builtin_shufflevector(lo, hi, 0, 1, 2, 3, 4, 5, 6, 7));
}
struct Hook3 {
  const Params& p; LAS f32x4* rs; LAS float* csl; LAS unsigned* arr; u64 tag; int mt3, j3, lw, lane;
  f32x2 (&pv)[2][8]; u64& fc; float& cv;
  static constexpr int K0 = C3::NK - C3::NSB - 1;
  static constexpr int extra(int k) { return k <= K0 + 2 ? 17 : 1; }
  template <int K> __device__ __forceinline__ void at() {
    if constexpr (K == K0) {
      fc = __hip_atomic_load(p.flags + FLC + j3 * 16, RLX_AGENT);
#pragma unroll
      for (int h = 0; h < 2; ++h)
#pragma unroll
        for (int i = 0; i < 8; ++i) pv[h][i] = __builtin_bit_cast(f32x2, ld8(p.stats + i * S_ + mt3 * 256 + lw * 128 + h * 64 + lane));
    } else if constexpr (K == K0 + 2) {
      if (!__all(fc == tag)) await<PSLEEP, 1>(p.flags + FLC + j3 * 16, true, tag, 0, arr + 11, 1u);
      __builtin_amdgcn_fence(__ATOMIC_ACQUIRE, "wavefront");
      cv = __hip_atomic_load(p.cs + j3 * 128 + lw * 64 + lane, RLX_AGENT);
#pragma unroll
      for (int h = 0; h < 2; ++h) {
        float mean = 0.f, q = 0.f, dd = 0.f;
#pragma unroll
        for (int i = 0; i < 8; ++i) { mean += pv[h][i][0]; q += pv[h][i][1]; }
        mean *= 0.125f;
#pragma unroll
        for (int i = 0; i < 8; ++i) { const float d = pv[h][i][0] - mean; dd += d * d; }
        q += 80.0f * dd;
        const float den = rbf(__builtin_amdgcn_sqrtf(rbf(rbf(q * (1.0f / D_)) + 1e-5f)));
        rs[lw * 128 + h * 64 + lane] = (f32x4){-rbf(mean), den, -1.44269504089f * __builtin_amdgcn_rcpf(den), 0.f};
      }
    } else if constexpr (K == C3::NK - 2) {
      csl[lw * 64 + lane] = cv;
      wait_lgkm0();
    }
  }
};

__global__ __launch_bounds__(NWAVES * 64) void mega(const Params p) {
  __shared__ __attribute__((aligned(1024))) char smem_[LDS_TOTAL];
  LAS char* lds = (LAS char*)smem_;
  const int wid = __builtin_amdgcn_readfirstlane(threadIdx.x >> 6), lane = threadIdx.x & 63;
  const int bid = blockIdx.x, g = bid & 7, idx = bid >> 3;
  LAS unsigned* arr = (LAS unsigned*)(lds + LDS_MISC);
  if (wid == 0) STAMP(0, 0);
  bf16x8 v0[2], v1[2];
  if (wid < 4) {
#pragma unroll
    for (int h = 0; h < 2; ++h) {
      const u16* xr = p.x + (bid * 8 + h * 4 + wid) * D_;
      v0[h] = *(const bf16x8*)(xr + lane * 8);
      v1[h] = *(const bf16x8*)(xr + 512 + (lane & 15) * 8);
    }
  }
  u64 ep = 0;
  if (wid == 7) ep = __hip_atomic_load(p.flags + (lane == 63 ? bid * 16 : FLE), RLX_AGENT);
  if (threadIdx.x < 16) arr[threadIdx.x] = 0u;
  bar();
  const int j2 = idx & 7, mq = idx >> 3, mt2 = g * 4 + mq;
  const int mt3 = (g & 6) + (idx & 1), j3 = (g & 1) * 14 + (idx >> 1);
  const bool on3 = idx < 28 && j3 < 27;
  LAS f32x4* rs = (LAS f32x4*)(lds + LDS_X);
  LAS float* csl = (LAS float*)(lds + LDS_X + 4096);

  const Src1 s1{(const char*)(p.xnI + (size_t)j2 * 256 * 80 * 8), (const char*)(p.sre + (size_t)mt2 * 64 * S_),
                (const char*)(p.sim + ((ptrdiff_t)mt2 - 1) * 64 * S_), (g + 2 * mq) & 7, j2};
  if (wid < 4) {
#pragma unroll
    for (int h = 0; h < 2; ++h) {
      float f[16];
#pragma unroll
      for (int i = 0; i < 8; ++i) { f[i] = bf2f((u16)v0[h][i]); f[8 + i] = bf2f((u16)v1[h][i]); }
      const float piv = __int_as_float(__builtin_amdgcn_readfirstlane(__float_as_int(f[0])));
      if (wid == 0 && h == 0) STAMP(0, 4);
      float s0 = 0.f, s1 = 0.f, q0 = 0.f, q1 = 0.f;
#pragma unroll
      for (int i = 0; i < 8; ++i) { const float d = f[i] - piv, e = f[8 + i] - piv; s0 += d; q0 += d * d; s1 += e; q1 += e * e; }
      const float sd = wave_sum(s0 + (lane < 16 ? s1 : 0.f)) * (1.0f / D_);
      const float qd = wave_sum(q0 + (lane < 16 ? q1 : 0.f)) * (1.0f / D_);
      const float mb = rbf(piv + sd), vb = rbf(fmaxf(qd - sd * sd, 0.f));
      const float rden = __builtin_amdgcn_rcpf(rbf(__builtin_amdgcn_sqrtf(rbf(vb + 1e-5f))));
      u32x4 w0, w1;
#pragma unroll
      for (int i = 0; i < 4; ++i) {
        w0[i] = pk2(rbf(f[2 * i] - mb) * rden, rbf(f[2 * i + 1] - mb) * rden);
        w1[i] = pk2(rbf(f[8 + 2 * i] - mb) * rden, rbf(f[9 + 2 * i] - mb) * rden);
      }
      LAS char* tb = lds + LDS_X + (h * 4 + wid) * 1296;
      *(LAS u32x4*)(tb + lane * 16) = w0;
      if (lane < 16) *(LAS u32x4*)(tb + 1024 + lane * 16) = w1;
    }
  }
  if (wid == 7 && lane == 0) *(LAS u64*)(arr + 16) = ep + 1ull;
  if (wid == 4 || wid == 5) {
    Loader<C1, Src1, false> L(lds, s1, wid - 4, lane);
    L.fill(0, C1::NPRE);
  }
  wait_lgkm0(); bar();
  const u64 tv = *(const LAS u64*)(arr + 16);
  const u64 tag = ((u64)(unsigned)__builtin_amdgcn_readfirstlane((unsigned)(tv >> 32)) << 32) | (unsigned)__builtin_amdgcn_readfirstlane((unsigned)tv);
  if (wid != 4 && wid != 5) {
    LAS char* tb = lds + LDS_X;
    auto rsrc = __builtin_amdgcn_make_buffer_rsrc(p.xnI, 0, S_ * D_ * 2, 0x00020000);
#pragma unroll
    for (int h = 0; h < 2; ++h) {
      const int cb = h * 6 + (wid < 4 ? wid : wid - 2);
      if (cb < 10) {
        const int d = cb * 64 + lane, j = d / 80, n = d % 80;
        __builtin_amdgcn_raw_buffer_store_b128(col8(tb, cb, lane), rsrc, ((j * 256 + bid) * 80 + n) * 16, 0, AUXH);
      }
    }
    if (wid == 0) STAMP(0, 7);
    wait_vm<0>();
    unsigned old = 0;
    if (lane == 0) old = __hip_atomic_fetch_add(arr + 5, 1u, RLX_WG);
    if (__builtin_amdgcn_readfirstlane(old) == 5u && lane < 8) __hip_atomic_store(p.flags + FL0 + lane * 256 + bid, tag, RLX_AGENT);
    if (wid == 0) STAMP(0, 3);
  }
  const Src2 s2{(const char*)(p.pqI + (size_t)mt2 * 160 * 64 * 8), (const char*)(p.hre + (size_t)j2 * 80 * D_), (const char*)(p.him + (size_t)j2 * 80 * D_), (mt2 * 5) >> 3};
  const Src3 s3{(const char*)(p.hI + (size_t)mt3 * 80 * 256 * 8), (const char*)(p.wg + j3 * 64), (const char*)(p.wu + j3 * 64), (mt3 * 5) >> 1};
  const Src4 s4{(const char*)(p.hhI + (size_t)mt2 * 216 * 64 * 8), (const char*)(p.wdI + (size_t)j2 * 216 * 80 * 8)};
  if (wid >= 6) {
    const int lw = wid - 6;
    {
      Loader<C1, Src1, true> L(lds, s1, lw, lane);
      lds_wait(arr + 5, 6);
      await<PSLEEP, 4, PD0>(p.flags + FL0 + g * 256 + lane, true, tag, lw, arr + 3, 1u);
      if (bid == 0 && lw == 0 && lane == 0) __hip_atomic_store(p.flags + FLE, tag, RLX_AGENT);
      if (lw == 0) STAMP(1, 0);
      wait_vm<0>();
      L.fill(0, NRAMP); L.run<NRAMP>(NoHook{});
      bar();
    }
    {
      Loader<C2, Src2, true> L(lds, s2, lw, lane);
      lds_wait(arr + 0, C1::NC);
      await<PSLEEP, 1, PD1>(p.flags + FL1 + mt2 * 8 + (lane & 7), true, tag, lw, arr + 3, 2u);
      if (lw == 0) STAMP(2, 0);
      wait_vm<0>();
      L.fill(0, NRAMP); L.run<NRAMP>(NoHook{});
      bar(); bar();
    }
    if (on3) {
      Loader<C3, Src3, true> L(lds, s3, lw, lane);
      lds_wait(arr + 1, C2::NC);
      await<PSLEEP, 1, PD2>(p.flags + FL2 + (mt3 * 32 + (lane & 31)), true, tag, lw, arr + 3, 3u);
      if (lw == 0) STAMP(3, 0);
      wait_vm<0>();
      L.fill(0, NRAMP); L.run<NRAMP>(NoHook{});
      bar();
    }
    {
      Loader<C4, Src4, true> L(lds, s4, lw, lane);
      lds_wait(arr + 2, 4);
      if (on3) await<PSLEEP, 1, PD3>(p.flags + FL3 + (g * 32 + (lane & 31)), (lane & 31) < 27, tag, lw, arr + 3, 4u);
      else await<ISLEEP, 1>(p.flags + FL3 + (g * 32 + (lane & 31)), (lane & 31) < 27, tag, lw, arr + 3, 4u);
      if (lw == 0) STAMP(4, 0);
      wait_vm<0>();
      L.fill(0, NRAMP); L.run<NRAMP>(NoHook{});
      bar(); bar();
    }
    return;
  }
  if (wid >= 4) {
    const int lw = wid - 4;
    {
      Loader<C1, Src1, false> L(lds, s1, lw, lane);
      L.run<C1::NPRE>(NoHook{});
      bar();
    }
    {
      Loader<C2, Src2, false> L(lds, s2, lw, lane);
      lds_wait(arr + 0, C1::NC); L.fill(0, C2::NPRE);
      L.run<C2::NPRE>(NoHook{});
      bar(); bar();
    }
    if (on3) {
      Loader<C3, Src3, false> L(lds, s3, lw, lane);
      static_assert(Hook3::K0 + 2 < C3::NK - 2, "");
      f32x2 pv[2][8]; u64 fc; float cv;
      lds_wait(arr + 1, C2::NC); L.fill(0, C3::NPRE);
      L.run<C3::NPRE>(Hook3{p, rs, csl, arr, tag, mt3, j3, lw, lane, pv, fc, cv});
      bar();
    }
    {
      Loader<C4, Src4, false> L(lds, s4, lw, lane);
      await<ISLEEP, 1, 0>(p.flags + FLW + min(lane, 39), true, tag, lw, arr + 13, 1u);
      wait_vm<0>();
      lds_wait(arr + 2, 4); L.fill(0, C4::NPRE);
      L.run<C4::NPRE>(NoHook{});
      bar(); bar();
    }
    return;
  }

#define FRESH_LANE int lane_ = threadIdx.x & 63; asm volatile("" : "+v"(lane_)); const int lane = lane_, fr = lane & 15, fq = lane >> 4; (void)fr; (void)fq;
  {
    FRESH_LANE
    Consumer<C1> L(lds, wid, lane);
    f32x4 acc[C1::MF][C1::NF];
    L.run(acc, [] {});
    const int isQ = wid >> 1;
    const float sg = isQ ? -1.f : 1.f;
#pragma unroll
    for (int n = 0; n < 2; ++n) {
      const int r = (wid & 1) * 32 + n * 16 + fr;
#pragma unroll
      for (int m = 0; m < 5; ++m) {
        const int koct = isQ * 80 + j2 * 10 + m * 2 + (fq >> 1);
        u32x2 w; w[0] = pk2(sg * acc[m][n][0], sg * acc[m][n][1]); w[1] = pk2(sg * acc[m][n][2], sg * acc[m][n][3]);
        st8(p.pqI + (((size_t)mt2 * 160 + koct) * 64 + r) * 8 + (fq & 1) * 4, w);
      }
    }
    publish(arr + 0, C1::NC, p.flags + FL1 + mt2 * 8 + j2, tag, lane, 1);
    if (wid == 0) STAMP(1, 3);
  }
  {
    FRESH_LANE
    Consumer<C2> L(lds, wid, lane);
    f32x4 acc[C2::MF][C2::NF];
    const int t = mt2 * 64 + wid * 16 + fr;
    bf16x4 xv[C2::NF];
#pragma unroll
    for (int n = 0; n < C2::NF; ++n) xv[n] = *(const bf16x4*)(p.x + (size_t)t * D_ + j2 * 80 + n * 16 + fq * 4);
#pragma unroll
    for (int n = 0; n < C2::NF; ++n) asm volatile("" : "+v"(xv[n]));
    L.run(acc, [] {});
    f32x4 red[1][5];
    L.reduce_k(acc, red);
    float hv[C2::NF][4];
    float s = 0.f;
#pragma unroll
    for (int n = 0; n < C2::NF; ++n) {
      u32x2 w;
      w[0] = pk2(red[0][n][0] + bf2f((u16)xv[n][0]), red[0][n][1] + bf2f((u16)xv[n][1]));
      w[1] = pk2(red[0][n][2] + bf2f((u16)xv[n][2]), red[0][n][3] + bf2f((u16)xv[n][3]));
      hv[n][0] = __uint_as_float(w[0] << 16); hv[n][1] = __uint_as_float(w[0] & 0xffff0000u);
      hv[n][2] = __uint_as_float(w[1] << 16); hv[n][3] = __uint_as_float(w[1] & 0xffff0000u);
      s += (hv[n][0] + hv[n][1]) + (hv[n][2] + hv[n][3]);
      const int koct = j2 * 10 + n * 2 + (fq >> 1);
      st8(p.hI + (((size_t)(t >> 8) * 80 + koct) * 256 + (t & 255)) * 8 + (fq & 1) * 4, w);
      *(LAS u32x2*)(lds + LDS_H + ((wid * 5 + n) * 64 + lane) * 8) = w;
    }
    const float mean = sum_rows(s) * (1.0f / 80.0f);
    float q = 0.f;
#pragma unroll
    for (int n = 0; n < C2::NF; ++n)
#pragma unroll
      for (int r = 0; r < 4; ++r) { const float d = hv[n][r] - mean; q += d * d; }
    q = sum_rows(q);
    if (fq == 0) st8(p.stats + j2 * S_ + t, __builtin_bit_cast(u32x2, (f32x2){mean, q}));
    publish(arr + 1, C2::NC, p.flags + FL2 + (g * 32 + idx), tag, lane, 2);
    if (wid == 0) STAMP(2, 3);
  }
  if (on3) {
    FRESH_LANE
    Consumer<C3> L(lds, wid, lane);
    f32x4 acc[C3::MF][C3::NF];
    f32x4 st[C3::MF], cg[2], cu[2];
    L.run(acc, [&] {
#pragma unroll
      for (int m = 0; m < C3::MF; ++m) st[m] = rs[L.wm * 128 + m * 16 + fr];
#pragma unroll
      for (int i = 0; i < 2; ++i) {
        const int fl = L.wn * 32 + i * 16 + fq * 4;
        cg[i] = *(const LAS f32x4*)(csl + fl); cu[i] = *(const LAS f32x4*)(csl + 64 + fl);
      }
    });
    u16* hb = p.hhI + (((size_t)(mt3 * 4 + L.wm * 2) * 216 + j3 * 8 + L.wn * 4 + (fq >> 1)) * 64 + fr) * 8 + (fq & 1) * 4;
#pragma unroll
    for (int m = 0; m < C3::MF; ++m) {
      u32x2 w[2];
#pragma unroll
      for (int i = 0; i < 2; ++i) {
        const f32x4 s0 = {st[m][0], st[m][0], st[m][0], st[m][0]}, s1 = {st[m][1], st[m][1], st[m][1], st[m][1]};
        const f32x4 x = __builtin_elementwise_fma(cg[i], s0, acc[m][i]) * st[m][2];
        f32x4 e;
#pragma unroll
        for (int r = 0; r < 4; ++r) e[r] = __builtin_amdgcn_exp2f(x[r]);
        const f32x4 d = __builtin_elementwise_fma(e, s1, s1);
        f32x4 rc;
#pragma unroll
        for (int r = 0; r < 4; ++r) rc[r] = __builtin_amdgcn_rcpf(d[r]);
        const f32x4 o = __builtin_elementwise_fma(cu[i], s0, acc[m][i + 2]) * rc;
        w[i][0] = pk2(o[0], o[1]); w[i][1] = pk2(o[2], o[3]);
        st8(hb + (m >> 2) * (216 * 64 * 8) + i * (2 * 64 * 8) + (m & 3) * 128, w[i]);
      }
    }
    publish(arr + 2, C3::NC, p.flags + FL3 + (mt3 * 32 + j3), tag, lane, 3);
    if (wid == 0) STAMP(3, 3);
  } else {
    FRESH_LANE
    const int id = idx >= 28 ? g * 4 + idx - 28 : 32 + (g >> 1) * 2 + idx - 26;
    auto rsrc = __builtin_amdgcn_make_buffer_rsrc(p.wdI, 0, F_ * D_ * 2, 0x00020000);
    lds_wait(arr + 1, C2::NC);
    if (id < 27) {
      const u16* w = ((wid & 2) ? p.wu : p.wg) + (size_t)((wid & 1) * 320 + (lane >> 3)) * F_ + id * 64 + (lane & 7) * 8;
      f32x2 a[4] = {{0.f, 0.f}, {0.f, 0.f}, {0.f, 0.f}, {0.f, 0.f}};
#pragma unroll 1
      for (int b = 0; b < 2; ++b) {
        u32x4 v[20];
#pragma unroll
        for (int i = 0; i < 20; ++i) v[i] = *(const u32x4*)(w + (size_t)(b * 20 + i) * 8 * F_);
#pragma unroll
        for (int i = 0; i < 20; ++i)
#pragma unroll
          for (int e = 0; e < 4; ++e) a[e] += (f32x2){__uint_as_float(v[i][e] << 16), __uint_as_float(v[i][e] & 0xffff0000u)};
      }
      LAS float* sf = (LAS float*)(lds + LDS_X);
#pragma unroll
      for (int e = 0; e < 4; ++e)
#pragma unroll
        for (int c = 0; c < 2; ++c) { float t = sum_rows(a[e][c]); t += __shfl_xor(t, 8); a[e][c] = t; }
      if (lane < 8) {
        *(LAS f32x4*)(sf + wid * 64 + lane * 8) = (f32x4){a[0][0], a[0][1], a[1][0], a[1][1]};
        *(LAS f32x4*)(sf + wid * 64 + lane * 8 + 4) = (f32x4){a[2][0], a[2][1], a[3][0], a[3][1]};
      }
      wait_lgkm0();
      unsigned old = 0;
      if (lane == 0) old = __hip_atomic_fetch_add(arr + 12, 1u, RLX_WG);
      if (__builtin_amdgcn_readfirstlane(old) == 3u) {
        __hip_atomic_store(p.cs + id * 128 + lane, sf[lane] + sf[64 + lane], RLX_AGENT);
        __hip_atomic_store(p.cs + id * 128 + 64 + lane, sf[128 + lane] + sf[192 + lane], RLX_AGENT);
        wait_vm<0>();
        if (lane == 0) __hip_atomic_store(p.flags + FLC + id * 16, tag, RLX_AGENT);
        STAMP(0, 5);
      }
    }
    const int k0 = id < 27 ? id * 3 : 81 + (id - 27) * 11, nk = id < 27 ? 3 : min(11, 216 - k0);
#pragma unroll 1
    for (int i = 0; i < 2 * nk; ++i) {
      const int row = (i & 1) * 4 + wid;
      const char* wr = (const char*)(p.wd + (size_t)((k0 + (i >> 1)) * 8 + row) * D_);
      set_m0((unsigned)(size_t)lds + (i >> 1) * 10368 + row * 1296);
      dma16<0, 0>(wr, lane * 16);
      if (lane < 16) dma16<0, 1024>(wr, lane * 16);
    }
    wait_vm<0>();
    if (lane == 0) __hip_atomic_fetch_add(arr + 6, 1u, RLX_WG);
    lds_wait(arr + 6, 4);
#pragma unroll 1
    for (int i = 0; i < nk; ++i) {
#pragma unroll
      for (int h = 0; h < 3; ++h) {
        const int cb = h * 4 + wid;
        if (cb < 10) {
          const int d = cb * 64 + lane, j = d / 80, n = d % 80;
          __builtin_amdgcn_raw_buffer_store_b128(col8(lds + i * 10368, cb, lane), rsrc, ((j * 216 + k0 + i) * 80 + n) * 16, 0, AUXH);
        }
      }
    }
    publish(arr + 2, 4, p.flags + FLW + id, tag, lane, 0);
  }
  {
    FRESH_LANE
    Consumer<C4> L(lds, wid, lane);
    f32x4 acc[C4::MF][C4::NF];
    const int t = mt2 * 64 + wid * 16 + fr;
    u32x2 hv[5];
    L.run(acc, [] {});
#pragma unroll
    for (int n = 0; n < 5; ++n) hv[n] = *(const LAS u32x2*)(lds + LDS_H + ((wid * 5 + n) * 64 + lane) * 8);
    f32x4 red[1][5];
    L.reduce_k(acc, red);
#pragma unroll
    for (int n = 0; n < 5; ++n) {
      u32x2 w;
      w[0] = pk2(red[0][n][0] + __uint_as_float(hv[n][0] << 16), red[0][n][1] + __uint_as_float(hv[n][0] & 0xffff0000u));
      w[1] = pk2(red[0][n][2] + __uint_as_float(hv[n][1] << 16), red[0][n][3] + __uint_as_float(hv[n][1] & 0xffff0000u));
      *(u32x2*)(p.out + (size_t)t * D_ + j2 * 80 + n * 16 + fq * 4) = w;
    }
    if (wid == 0) { wait_vm<0>(); STAMP(4, 3); }
  }
}

extern "C" void kernel_launch(void* const* d_in, const int* in_sizes, int n_in, void* d_out, int out_size,
                              void* d_ws, size_t ws_size, hipStream_t stream) {
  char* ws = (char*)d_ws;
  Params p;
  p.x = (const u16*)d_in[0]; p.sre = (const u16*)d_in[1]; p.sim = (const u16*)d_in[2];
  p.hre = (const u16*)d_in[3]; p.him = (const u16*)d_in[4];
  p.wg = (const u16*)d_in[6]; p.wu = (const u16*)d_in[7]; p.wd = (const u16*)d_in[8];
  p.flags = (u64*)ws;
  p.cs = (float*)(ws + 262144);
  p.stats = (f32x2*)(ws + 524288);
  p.xnI = (u16*)(ws + (1u << 20));
  p.pqI = (u16*)(ws + (4u << 20));
  p.hI = (u16*)(ws + (10u << 20));
  p.hhI = (u16*)(ws + (13u << 20));
  p.wdI = (u16*)(ws + (21u << 20));
  p.out = (u16*)d_out;
  mega<<<256, NWAVES * 64, 0, stream>>>(p);
}
```

```cpp
#include <hip/hip_runtime.h>
#include <hip/hip_bf16.h>

constexpr int PSLEEP = 4;
constexpr int ISLEEP = 16;
constexpr int PDELAY = 40;
constexpr int NRAMP = 3;
constexpr int AUXH = 16;

typedef unsigned short u16;
typedef unsigned long long u64;
typedef short bf16x8 __attribute__((ext_vector_type(8)));
typedef short bf16x4 __attribute__((ext_vector_type(4)));
typedef float f32x4 __attribute__((ext_vector_type(4)));
typedef float f32x2 __attribute__((ext_vector_type(2)));
typedef unsigned u32x2 __attribute__((ext_vector_type(2)));
typedef unsigned u32x4 __attribute__((ext_vector_type(4)));
#define LAS __attribute__((address_space(3)))
#define RLX_AGENT __ATOMIC_RELAXED, __HIP_MEMORY_SCOPE_AGENT
#define RLX_WG __ATOMIC_RELAXED, __HIP_MEMORY_SCOPE_WORKGROUP

constexpr int S_ = 2048, D_ = 640, F_ = 1728;

__device__ __forceinline__ float bf2f(u16 v) { return __uint_as_float(((unsigned)v) << 16); }
typedef __bf16 bf16x2n __attribute__((ext_vector_type(2)));
__device__ __forceinline__ unsigned pk2(float lo, float hi) {
  return __builtin_bit_cast(unsigned, __builtin_convertvector((f32x2){lo, hi}, bf16x2n));
}
__device__ __forceinline__ float rbf(float v) { return __uint_as_float(pk2(v, v) << 16); }

template <int N> __device__ __forceinline__ void wait_vm() { asm volatile("s_waitcnt vmcnt(%0)" ::"n"(N) : "memory"); }
__device__ __forceinline__ void wait_lgkm0() { asm volatile("" ::: "memory"); __builtin_amdgcn_s_waitcnt(0xC07F); asm volatile("" ::: "memory"); }
__device__ __forceinline__ void bar() { asm volatile("" ::: "memory"); __builtin_amdgcn_s_barrier(); asm volatile("" ::: "memory"); }
template <int AUX, int OFF> __device__ __forceinline__ void dma16(const char* sbase, unsigned voff) {
  if constexpr (AUX == 16) asm volatile("global_load_lds_dwordx4 %0, %1 offset:%2 sc1" :: "v"(voff), "s"(sbase), "n"(OFF) : "memory");
  else asm volatile("global_load_lds_dwordx4 %0, %1 offset:%2" :: "v"(voff), "s"(sbase), "n"(OFF) : "memory");
}
__device__ __forceinline__ void set_m0(unsigned v) { asm volatile("s_mov_b32 m0, %0\n\ts_nop 0" :: "s"(v) : "memory"); }
__device__ __forceinline__ void st8(void* p, u32x2 v) { __hip_atomic_store((u64*)p, __builtin_bit_cast(u64, v), RLX_AGENT); }
__device__ __forceinline__ u32x2 ld8(const void* p) { return __builtin_bit_cast(u32x2, __hip_atomic_load((u64*)p, RLX_AGENT)); }

constexpr int NL = 2;
constexpr int NWAVES = 8;
constexpr int LDS_H = 120 * 1024;
constexpr int LDS_RING = 136 * 1024;
constexpr int LDS_X = LDS_RING;
constexpr int LDS_TOTAL = 160 * 1024;
constexpr int LDS_MISC = LDS_TOTAL - 256;

template <int KID_, int BM_, int BN_, int BK_, int WM_, int WN_, int WK_, int NSA_, int NSB_, int NK_, int AMODE_, int BMODE_, int SWAP_, int NPRE_>
struct Cfg {
  static constexpr int KID = KID_, BM = BM_, BN = BN_, BK = BK_, WM = WM_, WN = WN_, WK = WK_, NSA = NSA_, NSB = NSB_, NK = NK_;
  static constexpr int AMODE = AMODE_, BMODE = BMODE_, SWAP = SWAP_, NPRE = NPRE_;
  static constexpr int NC = WM * WN * WK;
  static constexpr int MF = BM / WM / 16, NF = BN / WN / 16, KSW = BK / 32 / WK;
  static constexpr int ABYTES = BM * BK * 2, BBYTES = BN * BK * 2;
  static constexpr int PA = ABYTES / 1024, PB = BBYTES / 1024;
  static constexpr int GA = PA / NL, GB = PB / NL, G = GA + GB;
  static constexpr int RA = NSA * ABYTES, RB = NSB * BBYTES;
  static_assert(PA % NL == 0 && PB % NL == 0, "pieces");
  static_assert(RA + RB <= (KID == 1 ? LDS_RING : LDS_H), "lds");
  static_assert(NSA <= NK && NSB <= NK, "stages");
  static_assert(KSW >= 1 && KSW <= 2, "ksw");
};

template <class C> struct Frags { bf16x8 a[C::KSW][C::MF]; bf16x8 b[C::KSW][C::NF]; };

template <class C, class Src, bool ISA>
struct Loader {
  static constexpr int G = ISA ? C::GA : C::GB, NS = ISA ? C::NSA : C::NSB, BYTES = ISA ? C::ABYTES : C::BBYTES;
  static constexpr int RING = NS * BYTES, MODE = ISA ? C::AMODE : C::BMODE, AUX = ISA ? Src::AUXA : Src::AUXB;
  const Src& src;
  int lw;
  unsigned lbase, cur;
  unsigned voff[G];
  __device__ __forceinline__ Loader(LAS char* lds, const Src& s, int lw_, int lane) : src(s), lw(lw_) {
    lbase = (unsigned)(size_t)lds + (ISA ? 0 : C::RA) + lw * (G * 1024);
#pragma unroll
    for (int i = 0; i < G; ++i) {
      const int p = (lw * G + i) * 64 + lane;
      unsigned v;
      if constexpr (MODE == 0) v = p * 16;
      else if constexpr (MODE == 1) { const int row = p >> 3, c = (p & 7) ^ ((row >> 1) & 7); v = row * (ISA ? Src::ldA : Src::ldB) + c * 16; }
      else { const int q = p % (C::BK * 8), k = q >> 3, c = (q & 7) ^ ((((k >> 3) & 1) << 2) | (((k >> 1) & 1) << 1)); v = k * Src::ldB + c * 16; }
      voff[i] = v - (i & 3) * 1024;
    }
  }
  template <int I> __device__ __forceinline__ void pieces(const char* gb, unsigned l) {
    if constexpr (I < G) {
      if constexpr ((I & 3) == 0) set_m0(l + I * 1024);
      dma16<AUX, (I & 3) * 1024>(gb, voff[I]);
      pieces<I + 1>(gb, l);
    }
  }
  __device__ __forceinline__ void issue(int k, unsigned buf) {
    pieces<0>(ISA ? src.baseA(k) : src.baseB(k, lw), lbase + buf);
  }
  __device__ __forceinline__ void fill(int s0, int s1) {
#pragma unroll 1
    for (int s = s0; s < s1; ++s) issue(s, s * BYTES);
  }
  template <int K, class Hook> __device__ __forceinline__ void tail(Hook hook) {
    if constexpr (K < C::NK - 1) {
      wait_vm<(C::NK - 2 - K) * G + Hook::extra(K)>();
      bar();
      hook.template at<K>();
      tail<K + 1>(hook);
    }
  }
  template <int N0, class Hook> __device__ __forceinline__ void run(Hook hook) {
    static_assert(N0 >= 1 && N0 <= NS && (NS - 1) * G <= 63, "ring");
    wait_vm<(N0 - 1) * G>();
    bar();
    fill(N0, NS);
    cur = 0;
#pragma unroll 1
    for (int k = 0; k < C::NK - NS; ++k) {
      wait_vm<(NS - 2) * G>();
      bar();
      issue(k + NS, cur);
      cur += BYTES; if (cur == RING) cur = 0;
    }
    hook.template at<C::NK - NS - 1>();
    tail<C::NK - NS>(hook);
  }
};
struct NoHook {
  static constexpr int extra(int) { return 0; }
  template <int K> __device__ __forceinline__ void at() {}
};

template <class C>
struct Consumer {
  LAS char* lds;
  int wid, lane, wm, wn, wk;
  unsigned rdA[2], rdB[4];

  __device__ __forceinline__ Consumer(LAS char* lds_, int wid_, int lane_) : lds(lds_), wid(wid_), lane(lane_) {
    wk = wid % C::WK; wn = (wid / C::WK) % C::WN; wm = wid / (C::WK * C::WN);
    const int fr = lane & 15, fq = lane >> 4;
    const int arow = wm * (C::BM / C::WM) + fr;
    const int ss0 = wk * C::KSW;
#pragma unroll
    for (int s = 0; s < 2; ++s) {
      const int ss = ss0 + s;
      if constexpr (C::AMODE == 0) rdA[s] = ((ss0 * 4 + fq) * C::BM + arow) * 16;
      else rdA[s] = arow * 128 + ((((ss & 1) * 4 + fq) ^ ((fr >> 1) & 7)) * 16);
    }
    if constexpr (C::BMODE <= 1) {
      const int brow = wn * (C::BN / C::WN) + fr;
#pragma unroll
      for (int s = 0; s < 2; ++s) {
        const int ss = ss0 + s;
        if constexpr (C::BMODE == 0) rdB[s] = C::RA + ((ss0 * 4 + fq) * C::BN + brow) * 16;
        else rdB[s] = C::RA + brow * 128 + ((((ss & 1) * 4 + fq) ^ ((fr >> 1) & 7)) * 16);
      }
    } else {
      const int g = lane >> 4, q = (lane >> 2) & 3, p = lane & 3;
#pragma unroll
      for (int j = 0; j < 4; ++j) {
        const int jj = 2 * wn + (j & 1), mat = j >> 1;
        rdB[j] = C::RA + mat * (C::BK * 128) + (32 * ss0 + 8 * g + q) * 128 + ((((jj ^ (((g & 1) << 1) | (q >> 1))) << 1) | (p >> 1)) * 16) + 8 * (p & 1);
      }
    }
  }

  __device__ __forceinline__ void read(Frags<C>& f, unsigned bufa, unsigned bufb) {
    LAS char* a = lds + bufa; LAS char* b = lds + bufb;
#pragma unroll
    for (int s = 0; s < C::KSW; ++s) {
      if constexpr (C::BMODE == 0) {
#pragma unroll
        for (int n = 0; n < C::NF; ++n) f.b[s][n] = *(const LAS bf16x8*)(b + rdB[0] + s * (C::BN * 64) + n * 256);
      } else if constexpr (C::BMODE == 1) {
#pragma unroll
        for (int n = 0; n < C::NF; ++n) f.b[s][n] = *(const LAS bf16x8*)(b + rdB[s] + n * 2048);
      } else {
#pragma unroll
        for (int n = 0; n < C::NF; ++n) {
          bf16x4 lo = __builtin_amdgcn_ds_read_tr16_b64_v4i16((LAS bf16x4*)(b + rdB[n] + s * 4096));
          bf16x4 hi = __builtin_amdgcn_ds_read_tr16_b64_v4i16((LAS bf16x4*)(b + rdB[n] + s * 4096 + 512));
          f.b[s][n] = __builtin_shufflevector(lo, hi, 0, 1, 2, 3, 4, 5, 6, 7);
        }
      }
      if constexpr (C::AMODE == 0) {
#pragma unroll
        for (int m = 0; m < C::MF; ++m) f.a[s][m] = *(const LAS bf16x8*)(a + rdA[0] + s * (C::BM * 64) + m * 256);
      } else {
#pragma unroll
        for (int m = 0; m < C::MF; ++m) f.a[s][m] = *(const LAS bf16x8*)(a + rdA[s] + m * 2048);
      }
    }
  }

  __device__ __forceinline__ void mma(f32x4 (&acc)[C::MF][C::NF], const Frags<C>& f) {
#pragma unroll
    for (int s = 0; s < C::KSW; ++s)
#pragma unroll
      for (int m = 0; m < C::MF; ++m)
#pragma unroll
        for (int n = 0; n < C::NF; ++n)
          acc[m][n] = C::SWAP ? __builtin_amdgcn_mfma_f32_16x16x32_bf16(f.b[s][n], f.a[s][m], acc[m][n], 0, 0, 0)
                              : __builtin_amdgcn_mfma_f32_16x16x32_bf16(f.a[s][m], f.b[s][n], acc[m][n], 0, 0, 0);
  }
  static constexpr int NRD = C::KSW * (C::MF + (C::BMODE >= 2 ? 2 : 1) * C::NF);
  static constexpr int NMM = C::KSW * C::MF * C::NF;
  __device__ __forceinline__ void interleave() {
    constexpr int RPM = (NRD + NMM - 3) / (NMM - 2);
#pragma unroll
    for (int i = 0; i < NMM; ++i) {
      __builtin_amdgcn_sched_group_barrier(0x100, RPM, 0);
      __builtin_amdgcn_sched_group_barrier(0x008, 1, 0);
    }
  }
  unsigned na, nb;
  __device__ __forceinline__ void step(f32x4 (&acc)[C::MF][C::NF], Frags<C>& cur, Frags<C>& nxt) {
    na += C::ABYTES; if (na == C::RA) na = 0;
    nb += C::BBYTES; if (nb == C::RB) nb = 0;
    wait_lgkm0(); bar();
    __builtin_amdgcn_sched_barrier(0);
    read(nxt, na, nb);
    mma(acc, cur);
    interleave();
    __builtin_amdgcn_sched_barrier(0);
  }

  template <class Hook> __device__ __forceinline__ void run(f32x4 (&acc)[C::MF][C::NF], Hook hook) {
    Frags<C> f0, f1;
#pragma unroll
    for (int m = 0; m < C::MF; ++m)
#pragma unroll
      for (int n = 0; n < C::NF; ++n) { acc[m][n] = (f32x4){0.f, 0.f, 0.f, 0.f}; asm volatile("" : "+v"(acc[m][n])); }
    bar();
    read(f0, 0, 0);
    na = 0; nb = 0;
#pragma unroll 1
    for (int k = 0; k + 2 < C::NK; k += 2) {
      step(acc, f0, f1);
      step(acc, f1, f0);
    }
    if constexpr (C::NK % 2 == 0) {
      step(acc, f0, f1);
      if constexpr (C::KID == 3) { bar(); hook(); }
      mma(acc, f1);
    } else {
      mma(acc, f0);
    }
  }

  static constexpr bool SPLM = (C::MF % 2 == 0);
  static constexpr int RM = SPLM ? C::MF / 2 : C::MF, RN = SPLM ? C::NF : C::NF / 2;
  __device__ __forceinline__ void reduce_k(f32x4 (&acc)[C::MF][C::NF], f32x4 (&red)[RM][RN]) {
    static_assert(C::WK == 2, "");
    static_assert(LDS_X + C::NC * RM * RN * 1024 <= LDS_MISC, "");
    LAS f32x4* x = (LAS f32x4*)(lds + LDS_X);
#pragma unroll
    for (int m = 0; m < RM; ++m)
#pragma unroll
      for (int n = 0; n < RN; ++n) {
        const f32x4 lo = acc[m][n], hi = SPLM ? acc[m + RM][n] : acc[m][n + RN];
        x[(wid * RM * RN + m * RN + n) * 64 + lane] = wk ? lo : hi;
        red[m][n] = wk ? hi : lo;
      }
    wait_lgkm0(); bar();
#pragma unroll
    for (int m = 0; m < RM; ++m)
#pragma unroll
      for (int n = 0; n < RN; ++n) red[m][n] += x[((wid ^ 1) * RM * RN + m * RN + n) * 64 + lane];
  }
};

constexpr int FLE = 0;
constexpr int FL0 = 32;
constexpr int FL1 = FL0 + 2048;
constexpr int FL2 = FL1 + 16 * 16;
constexpr int FL3 = FL2 + 8 * 32;
constexpr int FLW = FL3 + 8 * 32;
constexpr int FLC = FLW + 48;

__device__ __forceinline__ void publish(LAS unsigned* arr, unsigned nwaves, u64* flag, u64 tag, int lane) {
  wait_vm<0>();
  if (lane == 0) {
    const unsigned old = __hip_atomic_fetch_add(arr, 1u, RLX_WG);
    if (old == nwaves - 1) __hip_atomic_store(flag, tag, RLX_AGENT);
  }
}
__device__ __forceinline__ void lds_wait(LAS unsigned* w, unsigned want) {
  const unsigned a = (unsigned)(size_t)w;
#pragma unroll 1
  for (unsigned i = 0; i < (1u << 24); ++i) {
    unsigned v;
    asm volatile("ds_read_b32 %0, %1\n\ts_waitcnt lgkmcnt(0)" : "=v"(v) : "v"(a) : "memory");
    if (__builtin_amdgcn_readfirstlane(v) >= want) break;
    __builtin_amdgcn_s_sleep(1);
  }
}
template <int SLEEP, int NW, int DELAY = PDELAY>
__device__ __forceinline__ void await(const u64* word, bool need, u64 tag, int lw, LAS unsigned* go, unsigned epoch) {
  if (lw == 0) {
    const u64 t0 = wall_clock64();
    if (DELAY) __builtin_amdgcn_s_sleep(DELAY);
    for (;;) {
      bool ok = true;
#pragma unroll
      for (int i = 0; i < NW; ++i) ok &= __hip_atomic_load(word + i * 64, RLX_AGENT) == tag;
      if (__all(!need || ok)) break;
      if (wall_clock64() - t0 > 2000000ull) break;
      if (SLEEP > PSLEEP && __any(need && ok)) __builtin_amdgcn_s_sleep(PSLEEP); else __builtin_amdgcn_s_sleep(SLEEP);
    }
    __hip_atomic_store(go, epoch, RLX_WG);
  } else {
    lds_wait(go, epoch);
  }
  __builtin_amdgcn_fence(__ATOMIC_ACQUIRE, "wavefront");
}
using C1 = Cfg<1, 80, 128, 64, 1, 4, 1, 4, 6, 32, 0, 1, 0, 2>;
using C2 = Cfg<2, 64, 80, 64, 2, 1, 2, 4, 6, 20, 0, 1, 1, 6>;
using C3 = Cfg<3, 256, 128, 32, 2, 2, 1, 4, 5, 20, 0, 3, 1, 3>;
using C4 = Cfg<4, 64, 80, 64, 2, 1, 2, 4, 6, 27, 0, 0, 1, 6>;
struct Src1 {
  const char* a; const char* bre; const char* bim; int o, c;
  static constexpr int ldA = 0, ldB = S_ * 2, AUXA = AUXH, AUXB = 0;
  __device__ __forceinline__ int kt(int k) const { const int s = (k + o) & 31; return (s & 24) | ((s + c) & 7); }
  __device__ __forceinline__ const char* baseA(int k) const { return a + kt(k) * (80 * 128); }
  __device__ __forceinline__ const char* baseB(int k, int im) const { return (im ? bim : bre) + kt(k) * 128; }
};
struct Src2 {
  const char* a; const char* bre; const char* bim; int rot;
  static constexpr int ldA = 0, ldB = D_ * 2, AUXA = AUXH, AUXB = 0;
  __device__ __forceinline__ int kt(int k) const { const int s = k + rot; return s >= 20 ? s - 20 : s; }
  __device__ __forceinline__ const char* baseA(int k) const { return a + kt(k) * (64 * 128); }
  __device__ __forceinline__ const char* baseB(int k, int) const { const int t = kt(k); return t < 10 ? bre + t * 128 : bim + (t - 10) * 128; }
};
struct Src3 {
  const char* a; const char* bg; const char* bu; int rot;
  static constexpr int ldA = 0, ldB = F_ * 2, AUXA = AUXH, AUXB = 0;
  __device__ __forceinline__ int kt(int k) const { const int s = k + rot; return s >= 20 ? s - 20 : s; }
  __device__ __forceinline__ const char* baseA(int k) const { return a + kt(k) * (256 * 64); }
  __device__ __forceinline__ const char* baseB(int k, int up) const { return (up ? bu : bg) + kt(k) * (32 * F_ * 2); }
};
struct Src4 {
  const char* a; const char* b;
  static constexpr int ldA = 0, ldB = 0, AUXA = AUXH, AUXB = AUXH;
  __device__ __forceinline__ const char* baseA(int k) const { return a + k * (64 * 128); }
  __device__ __forceinline__ const char* baseB(int k, int) const { return b + k * (80 * 128); }
};

struct Params {
  const u16* x; const u16* sre; const u16* sim; const u16* hre; const u16* him; const u16* wg; const u16* wu; const u16* wd;
  u16* xnI; u16* pqI; u16* hI; u16* hhI; u16* wdI; f32x2* stats; float* cs; u64* flags; u16* out;
};

template <int CTRL, int RMASK> __device__ __forceinline__ float dpp_add(float v) {
  return v + __int_as_float(__builtin_amdgcn_update_dpp(0, __float_as_int(v), CTRL, RMASK, 0xf, true));
}
__device__ __forceinline__ float wave_sum(float v) {
  v = dpp_add<0x111, 0xf>(v); v = dpp_add<0x112, 0xf>(v); v = dpp_add<0x114, 0xf>(v); v = dpp_add<0x118, 0xf>(v);
  v = dpp_add<0x142, 0xa>(v); v = dpp_add<0x143, 0xc>(v);
  return __int_as_float(__builtin_amdgcn_readlane(__float_as_int(v), 63));
}
__device__ __forceinline__ float sum_rows(float v) {
  const auto a = __builtin_amdgcn_permlane16_swap(__float_as_uint(v), __float_as_uint(v), false, false);
  v = __uint_as_float(a[0]) + __uint_as_float(a[1]);
  const auto b = __builtin_amdgcn_permlane32_swap(__float_as_uint(v), __float_as_uint(v), false, false);
  return __uint_as_float(b[0]) + __uint_as_float(b[1]);
}
__device__ __forceinline__ u32x4 col8(LAS char* tile, int cb, int lane) {
  LAS char* a = tile + ((lane >> 2) & 3) * 1296 + (cb * 64 + (lane >> 4) * 16 + (lane & 3) * 4) * 2;
  const bf16x4 lo = __builtin_amdgcn_ds_read_tr16_b64_v4i16((LAS bf16x4*)a);
  const bf16x4 hi = __builtin_amdgcn_ds_read_tr16_b64_v4i16((LAS bf16x4*)(a + 4 * 1296));
  return __builtin_bit_cast(u32x4, __builtin_shufflevector(lo, hi, 0, 1, 2, 3, 4, 5, 6, 7));
}
struct Hook3 {
  const Params& p; LAS f32x4* rs; LAS float* csl; LAS unsigned* arr; u64 tag; int mt3, j3, lw, lane;
  f32x2 (&pv)[2][8]; u64& fc; float& cv;
  static constexpr int K0 = C3::NK - C3::NSB - 1;
  static constexpr int extra(int k) { return k <= K0 + 2 ? 17 : 1; }
  template <int K> __device__ __forceinline__ void at() {
    if constexpr (K == K0) {
      fc = __hip_atomic_load(p.flags + FLC + j3 * 16, RLX_AGENT);
#pragma unroll
      for (int h = 0; h < 2; ++h)
#pragma unroll
        for (int i = 0; i < 8; ++i) pv[h][i] = __builtin_bit_cast(f32x2, ld8(p.stats + i * S_ + mt3 * 256 + lw * 128 + h * 64 + lane));
    } else if constexpr (K == K0 + 2) {
      if (!__all(fc == tag)) await<PSLEEP, 1>(p.flags + FLC + j3 * 16, true, tag, 0, arr + 11, 1u);
      __builtin_amdgcn_fence(__ATOMIC_ACQUIRE, "wavefront");
      cv = __hip_atomic_load(p.cs + j3 * 128 + lw * 64 + lane, RLX_AGENT);
#pragma unroll
      for (int h = 0; h < 2; ++h) {
        float mean = 0.f, q = 0.f, dd = 0.f;
#pragma unroll
        for (int i = 0; i < 8; ++i) { mean += pv[h][i][0]; q += pv[h][i][1]; }
        mean *= 0.125f;
#pragma unroll
        for (int i = 0; i < 8; ++i) { const float d = pv[h][i][0] - mean; dd += d * d; }
        q += 80.0f * dd;
        const float den = rbf(__builtin_amdgcn_sqrtf(rbf(rbf(q * (1.0f / D_)) + 1e-5f)));
        rs[lw * 128 + h * 64 + lane] = (f32x4){-rbf(mean), den, -1.44269504089f * __builtin_amdgcn_rcpf(den), 0.f};
      }
    } else if constexpr (K == C3::NK - 2) {
      csl[lw * 64 + lane] = cv;
      wait_lgkm0();
    }
  }
};

__global__ __launch_bounds__(NWAVES * 64) void mega(const Params p) {
  __shared__ __attribute__((aligned(1024))) char smem_[LDS_TOTAL];
  LAS char* lds = (LAS char*)smem_;
  const int wid = __builtin_amdgcn_readfirstlane(threadIdx.x >> 6), lane = threadIdx.x & 63;
  const int bid = blockIdx.x, g = bid & 7, idx = bid >> 3;
  LAS unsigned* arr = (LAS unsigned*)(lds + LDS_MISC);
  const u16* xr = p.x + (bid * 8 + wid) * D_;
  const bf16x8 v0 = *(const bf16x8*)(xr + lane * 8);
  const bf16x8 v1 = *(const bf16x8*)(xr + 512 + (lane & 15) * 8);
  const u64 ep = __hip_atomic_load(p.flags + ((wid == 7 && lane == 63) ? bid * 16 : FLE), RLX_AGENT);
  if (threadIdx.x < 16) arr[threadIdx.x] = 0u;
  const int j2 = idx & 7, mq = idx >> 3, mt2 = g * 4 + mq;
  const int mt3 = (g & 6) + (idx & 1), j3 = (g & 1) * 14 + (idx >> 1);
  const bool on3 = idx < 28 && j3 < 27;
  LAS f32x4* rs = (LAS f32x4*)(lds + LDS_X);
  LAS float* csl = (LAS float*)(lds + LDS_X + 4096);

  const Src1 s1{(const char*)(p.xnI + (size_t)j2 * 256 * 80 * 8), (const char*)(p.sre + (size_t)mt2 * 64 * S_),
                (const char*)(p.sim + ((ptrdiff_t)mt2 - 1) * 64 * S_), (g + 2 * mq) & 7, j2};
  {
    float f[16];
#pragma unroll
    for (int i = 0; i < 8; ++i) { f[i] = bf2f((u16)v0[i]); f[8 + i] = bf2f((u16)v1[i]); }
    const float piv = __int_as_float(__builtin_amdgcn_readfirstlane(__float_as_int(f[0])));
    float s0 = 0.f, s1 = 0.f, q0 = 0.f, q1 = 0.f;
#pragma unroll
    for (int i = 0; i < 8; ++i) { const float d = f[i] - piv, e = f[8 + i] - piv; s0 += d; q0 += d * d; s1 += e; q1 += e * e; }
    const float sd = wave_sum(s0 + (lane < 16 ? s1 : 0.f)) * (1.0f / D_);
    const float qd = wave_sum(q0 + (lane < 16 ? q1 : 0.f)) * (1.0f / D_);
    const float mb = rbf(piv + sd), vb = rbf(fmaxf(qd - sd * sd, 0.f));
    const float rden = __builtin_amdgcn_rcpf(rbf(__builtin_amdgcn_sqrtf(rbf(vb + 1e-5f))));
    u32x4 w0, w1;
#pragma unroll
    for (int i = 0; i < 4; ++i) {
      w0[i] = pk2(rbf(f[2 * i] - mb) * rden, rbf(f[2 * i + 1] - mb) * rden);
      w1[i] = pk2(rbf(f[8 + 2 * i] - mb) * rden, rbf(f[9 + 2 * i] - mb) * rden);
    }
    LAS char* tb = lds + LDS_X;
    *(LAS u32x4*)(tb + wid * 1296 + lane * 16) = w0;
    if (lane < 16) *(LAS u32x4*)(tb + wid * 1296 + 1024 + lane * 16) = w1;
    wait_lgkm0(); bar();
  }
  const u64 tag = (((u64)(unsigned)__builtin_amdgcn_readfirstlane((unsigned)(ep >> 32)) << 32) | (unsigned)__builtin_amdgcn_readfirstlane((unsigned)ep)) + 1ull;
  {
    LAS char* tb = lds + LDS_X;
    auto rsrc = __builtin_amdgcn_make_buffer_rsrc(p.xnI, 0, S_ * D_ * 2, 0x00020000);
#pragma unroll
    for (int h = 0; h < 2; ++h) {
      const int cb = h * 8 + wid;
      if (cb < 10) {
        const int d = cb * 64 + lane, j = d / 80, n = d % 80;
        __builtin_amdgcn_raw_buffer_store_b128(col8(tb, cb, lane), rsrc, ((j * 256 + bid) * 80 + n) * 16, 0, AUXH);
      }
    }
    wait_vm<0>();
    unsigned old = 0;
    if (lane == 0) old = __hip_atomic_fetch_add(arr + 5, 1u, RLX_WG);
    if (__builtin_amdgcn_readfirstlane(old) == 7u && lane < 8) __hip_atomic_store(p.flags + FL0 + lane * 256 + bid, tag, RLX_AGENT);
  }
  const Src2 s2{(const char*)(p.pqI + (size_t)mt2 * 160 * 64 * 8), (const char*)(p.hre + (size_t)j2 * 80 * D_), (const char*)(p.him + (size_t)j2 * 80 * D_), (mt2 * 5) >> 3};
  const Src3 s3{(const char*)(p.hI + (size_t)mt3 * 80 * 256 * 8), (const char*)(p.wg + j3 * 64), (const char*)(p.wu + j3 * 64), (mt3 * 5) >> 1};
  const Src4 s4{(const char*)(p.hhI + (size_t)mt2 * 216 * 64 * 8), (const char*)(p.wdI + (size_t)j2 * 216 * 80 * 8)};
  if (wid >= 6) {
    const int lw = wid - 6;
    {
      Loader<C1, Src1, true> L(lds, s1, lw, lane);
      lds_wait(arr + 5, 8);
      await<PSLEEP, 4>(p.flags + FL0 + g * 256 + lane, true, tag, lw, arr + 3, 1u);
      if (bid == 0 && lw == 0 && lane == 0) __hip_atomic_store(p.flags + FLE, tag, RLX_AGENT);
      wait_vm<0>();
      L.fill(0, NRAMP); L.run<NRAMP>(NoHook{});
    }
    {
      Loader<C2, Src2, true> L(lds, s2, lw, lane);
      lds_wait(arr + 0, C1::NC);
      await<PSLEEP, 1>(p.flags + FL1 + mt2 * 8 + (lane & 7), true, tag, lw, arr + 3, 2u);
      wait_vm<0>();
      L.fill(0, NRAMP); L.run<NRAMP>(NoHook{});
      bar();
    }
    if (on3) {
      Loader<C3, Src3, true> L(lds, s3, lw, lane);
      lds_wait(arr + 1, C2::NC);
      await<PSLEEP, 1, 28>(p.flags + FL2 + (mt3 * 32 + (lane & 31)), true, tag, lw, arr + 3, 3u);
      wait_vm<0>();
      L.fill(0, NRAMP); L.run<NRAMP>(NoHook{});
      bar();
    }
    {
      Loader<C4, Src4, true> L(lds, s4, lw, lane);
      lds_wait(arr + 2, 4);
      if (on3) await<PSLEEP, 1>(p.flags + FL3 + (g * 32 + (lane & 31)), (lane & 31) < 27, tag, lw, arr + 3, 4u);
      else await<ISLEEP, 1>(p.flags + FL3 + (g * 32 + (lane & 31)), (lane & 31) < 27, tag, lw, arr + 3, 4u);
      wait_vm<0>();
      L.fill(0, NRAMP); L.run<NRAMP>(NoHook{});
      bar();
    }
    return;
  }
  if (wid >= 4) {
    const int lw = wid - 4;
    {
      Loader<C1, Src1, false> L(lds, s1, lw, lane);
      lds_wait(arr + 5, 8); L.fill(0, C1::NPRE);
      L.run<C1::NPRE>(NoHook{});
    }
    {
      Loader<C2, Src2, false> L(lds, s2, lw, lane);
      lds_wait(arr + 0, C1::NC); L.fill(0, C2::NPRE);
      L.run<C2::NPRE>(NoHook{});
      bar();
    }
    if (on3) {
      Loader<C3, Src3, false> L(lds, s3, lw, lane);
      static_assert(Hook3::K0 + 2 < C3::NK - 2, "");
      f32x2 pv[2][8]; u64 fc; float cv;
      lds_wait(arr + 1, C2::NC); L.fill(0, C3::NPRE);
      L.run<C3::NPRE>(Hook3{p, rs, csl, arr, tag, mt3, j3, lw, lane, pv, fc, cv});
      bar();
    }
    {
      Loader<C4, Src4, false> L(lds, s4, lw, lane);
      await<ISLEEP, 1, 0>(p.flags + FLW + min(lane, 39), true, tag, lw, arr + 13, 1u);
      wait_vm<0>();
      lds_wait(arr + 2, 4); L.fill(0, C4::NPRE);
      L.run<C4::NPRE>(NoHook{});
      bar();
    }
    return;
  }

#define FRESH_LANE int lane_ = threadIdx.x & 63; asm volatile("" : "+v"(lane_)); const int lane = lane_, fr = lane & 15, fq = lane >> 4; (void)fr; (void)fq;
  {
    FRESH_LANE
    Consumer<C1> L(lds, wid, lane);
    f32x4 acc[C1::MF][C1::NF];
    L.run(acc, [] {});
    const int isQ = wid >> 1;
    const float sg = isQ ? -1.f : 1.f;
#pragma unroll
    for (int n = 0; n < 2; ++n) {
      const int r = (wid & 1) * 32 + n * 16 + fr;
#pragma unroll
      for (int m = 0; m < 5; ++m) {
        const int koct = isQ * 80 + j2 * 10 + m * 2 + (fq >> 1);
        u32x2 w; w[0] = pk2(sg * acc[m][n][0], sg * acc[m][n][1]); w[1] = pk2(sg * acc[m][n][2], sg * acc[m][n][3]);
        st8(p.pqI + (((size_t)mt2 * 160 + koct) * 64 + r) * 8 + (fq & 1) * 4, w);
      }
    }
    publish(arr + 0, C1::NC, p.flags + FL1 + mt2 * 8 + j2, tag, lane);
  }
  {
    FRESH_LANE
    Consumer<C2> L(lds, wid, lane);
    f32x4 acc[C2::MF][C2::NF];
    const int t = mt2 * 64 + wid * 16 + fr;
    bf16x4 xv[C2::NF];
#pragma unroll
    for (int n = 0; n < C2::NF; ++n) xv[n] = *(const bf16x4*)(p.x + (size_t)t * D_ + j2 * 80 + n * 16 + fq * 4);
#pragma unroll
    for (int n = 0; n < C2::NF; ++n) asm volatile("" : "+v"(xv[n]));
    L.run(acc, [] {});
    f32x4 red[1][5];
    L.reduce_k(acc, red);
    float hv[C2::NF][4];
    float s = 0.f;
#pragma unroll
    for (int n = 0; n < C2::NF; ++n) {
      u32x2 w;
      w[0] = pk2(red[0][n][0] + bf2f((u16)xv[n][0]), red[0][n][1] + bf2f((u16)xv[n][1]));
      w[1] = pk2(red[0][n][2] + bf2f((u16)xv[n][2]), red[0][n][3] + bf2f((u16)xv[n][3]));
      hv[n][0] = __uint_as_float(w[0] << 16); hv[n][1] = __uint_as_float(w[0] & 0xffff0000u);
      hv[n][2] = __uint_as_float(w[1] << 16); hv[n][3] = __uint_as_float(w[1] & 0xffff0000u);
      s += (hv[n][0] + hv[n][1]) + (hv[n][2] + hv[n][3]);
      const int koct = j2 * 10 + n * 2 + (fq >> 1);
      st8(p.hI + (((size_t)(t >> 8) * 80 + koct) * 256 + (t & 255)) * 8 + (fq & 1) * 4, w);
      *(LAS u32x2*)(lds + LDS_H + ((wid * 5 + n) * 64 + lane) * 8) = w;
    }
    const float mean = sum_rows(s) * (1.0f / 80.0f);
    float q = 0.f;
#pragma unroll
    for (int n = 0; n < C2::NF; ++n)
#pragma unroll
      for (int r = 0; r < 4; ++r) { const float d = hv[n][r] - mean; q += d * d; }
    q = sum_rows(q);
    if (fq == 0) st8(p.stats + j2 * S_ + t, __builtin_bit_cast(u32x2, (f32x2){mean, q}));
    publish(arr + 1, C2::NC, p.flags + FL2 + (g * 32 + idx), tag, lane);
  }
  if (on3) {
    FRESH_LANE
    Consumer<C3> L(lds, wid, lane);
    f32x4 acc[C3::MF][C3::NF];
    f32x4 st[C3::MF], cg[2], cu[2];
    L.run(acc, [&] {
#pragma unroll
      for (int m = 0; m < C3::MF; ++m) st[m] = rs[L.wm * 128 + m * 16 + fr];
#pragma unroll
      for (int i = 0; i < 2; ++i) {
        const int fl = L.wn * 32 + i * 16 + fq * 4;
        cg[i] = *(const LAS f32x4*)(csl + fl); cu[i] = *(const LAS f32x4*)(csl + 64 + fl);
      }
    });
    u16* hb = p.hhI + (((size_t)(mt3 * 4 + L.wm * 2) * 216 + j3 * 8 + L.wn * 4 + (fq >> 1)) * 64 + fr) * 8 + (fq & 1) * 4;
#pragma unroll
    for (int m = 0; m < C3::MF; ++m) {
      u32x2 w[2];
#pragma unroll
      for (int i = 0; i < 2; ++i) {
        const f32x4 s0 = {st[m][0], st[m][0], st[m][0], st[m][0]}, s1 = {st[m][1], st[m][1], st[m][1], st[m][1]};
        const f32x4 x = __builtin_elementwise_fma(cg[i], s0, acc[m][i]) * st[m][2];
        f32x4 e;
#pragma unroll
        for (int r = 0; r < 4; ++r) e[r] = __builtin_amdgcn_exp2f(x[r]);
        const f32x4 d = __builtin_elementwise_fma(e, s1, s1);
        f32x4 rc;
#pragma unroll
        for (int r = 0; r < 4; ++r) rc[r] = __builtin_amdgcn_rcpf(d[r]);
        const f32x4 o = __builtin_elementwise_fma(cu[i], s0, acc[m][i + 2]) * rc;
        w[i][0] = pk2(o[0], o[1]); w[i][1] = pk2(o[2], o[3]);
        st8(hb + (m >> 2) * (216 * 64 * 8) + i * (2 * 64 * 8) + (m & 3) * 128, w[i]);
      }
    }
    publish(arr + 2, C3::NC, p.flags + FL3 + (mt3 * 32 + j3), tag, lane);
  } else {
    FRESH_LANE
    const int id = idx >= 28 ? g * 4 + idx - 28 : 32 + (g >> 1) * 2 + idx - 26;
    auto rsrc = __builtin_amdgcn_make_buffer_rsrc(p.wdI, 0, F_ * D_ * 2, 0x00020000);
    lds_wait(arr + 1, C2::NC);
    if (id < 27) {
      const u16* w = ((wid & 2) ? p.wu : p.wg) + (size_t)((wid & 1) * 320 + (lane >> 3)) * F_ + id * 64 + (lane & 7) * 8;
      f32x2 a[4] = {{0.f, 0.f}, {0.f, 0.f}, {0.f, 0.f}, {0.f, 0.f}};
#pragma unroll 1
      for (int b = 0; b < 2; ++b) {
        u32x4 v[20];
#pragma unroll
        for (int i = 0; i < 20; ++i) v[i] = *(const u32x4*)(w + (size_t)(b * 20 + i) * 8 * F_);
#pragma unroll
        for (int i = 0; i < 20; ++i)
#pragma unroll
          for (int e = 0; e < 4; ++e) a[e] += (f32x2){__uint_as_float(v[i][e] << 16), __uint_as_float(v[i][e] & 0xffff0000u)};
      }
      LAS float* sf = (LAS float*)(lds + LDS_X);
#pragma unroll
      for (int e = 0; e < 4; ++e)
#pragma unroll
        for (int c = 0; c < 2; ++c) { float t = sum_rows(a[e][c]); t += __shfl_xor(t, 8); a[e][c] = t; }
      if (lane < 8) {
        *(LAS f32x4*)(sf + wid * 64 + lane * 8) = (f32x4){a[0][0], a[0][1], a[1][0], a[1][1]};
        *(LAS f32x4*)(sf + wid * 64 + lane * 8 + 4) = (f32x4){a[2][0], a[2][1], a[3][0], a[3][1]};
      }
      wait_lgkm0();
      unsigned old = 0;
      if (lane == 0) old = __hip_atomic_fetch_add(arr + 12, 1u, RLX_WG);
      if (__builtin_amdgcn_readfirstlane(old) == 3u) {
        __hip_atomic_store(p.cs + id * 128 + lane, sf[lane] + sf[64 + lane], RLX_AGENT);
        __hip_atomic_store(p.cs + id * 128 + 64 + lane, sf[128 + lane] + sf[192 + lane], RLX_AGENT);
        wait_vm<0>();
        if (lane == 0) __hip_atomic_store(p.flags + FLC + id * 16, tag, RLX_AGENT);
      }
    }
    const int k0 = id < 27 ? id * 3 : 81 + (id - 27) * 11, nk = id < 27 ? 3 : min(11, 216 - k0);
#pragma unroll 1
    for (int i = 0; i < 2 * nk; ++i) {
      const int row = (i & 1) * 4 + wid;
      const char* wr = (const char*)(p.wd + (size_t)((k0 + (i >> 1)) * 8 + row) * D_);
      set_m0((unsigned)(size_t)lds + (i >> 1) * 10368 + row * 1296);
      dma16<0, 0>(wr, lane * 16);
      if (lane < 16) dma16<0, 1024>(wr, lane * 16);
    }
    wait_vm<0>();
    if (lane == 0) __hip_atomic_fetch_add(arr + 6, 1u, RLX_WG);
    lds_wait(arr + 6, 4);
#pragma unroll 1
    for (int i = 0; i < nk; ++i) {
#pragma unroll
      for (int h = 0; h < 3; ++h) {
        const int cb = h * 4 + wid;
        if (cb < 10) {
          const int d = cb * 64 + lane, j = d / 80, n = d % 80;
          __builtin_amdgcn_raw_buffer_store_b128(col8(lds + i * 10368, cb, lane), rsrc, ((j * 216 + k0 + i) * 80 + n) * 16, 0, AUXH);
        }
      }
    }
    publish(arr + 2, 4, p.flags + FLW + id, tag, lane);
  }
  {
    FRESH_LANE
    Consumer<C4> L(lds, wid, lane);
    f32x4 acc[C4::MF][C4::NF];
    const int t = mt2 * 64 + wid * 16 + fr;
    u32x2 hv[5];
    L.run(acc, [] {});
#pragma unroll
    for (int n = 0; n < 5; ++n) hv[n] = *(const LAS u32x2*)(lds + LDS_H + ((wid * 5 + n) * 64 + lane) * 8);
    f32x4 red[1][5];
    L.reduce_k(acc, red);
#pragma unroll
    for (int n = 0; n < 5; ++n) {
      u32x2 w;
      w[0] = pk2(red[0][n][0] + __uint_as_float(hv[n][0] << 16), red[0][n][1] + __uint_as_float(hv[n][0] & 0xffff0000u));
      w[1] = pk2(red[0][n][2] + __uint_as_float(hv[n][1] << 16), red[0][n][3] + __uint_as_float(hv[n][1] & 0xffff0000u));
      *(u32x2*)(p.out + (size_t)t * D_ + j2 * 80 + n * 16 + fq * 4) = w;
    }
  }
}

extern "C" void kernel_launch(void* const* d_in, const int* in_sizes, int n_in, void* d_out, int out_size,
                              void* d_ws, size_t ws_size, hipStream_t stream) {
  char* ws = (char*)d_ws;
  Params p;
  p.x = (const u16*)d_in[0]; p.sre = (const u16*)d_in[1]; p.sim = (const u16*)d_in[2];
  p.hre = (const u16*)d_in[3]; p.him = (const u16*)d_in[4];
  p.wg = (const u16*)d_in[6]; p.wu = (const u16*)d_in[7]; p.wd = (const u16*)d_in[8];
  p.flags = (u64*)ws;
  p.cs = (float*)(ws + 262144);
  p.stats = (f32x2*)(ws + 524288);
  p.xnI = (u16*)(ws + (1u << 20));
  p.pqI = (u16*)(ws + (4u << 20));
  p.hI = (u16*)(ws + (10u << 20));
  p.hhI = (u16*)(ws + (13u << 20));
  p.wdI = (u16*)(ws + (21u << 20));
  p.out = (u16*)d_out;
  mega<<<256, NWAVES * 64, 0, stream>>>(p);
}
```

```cpp
#include <hip/hip_runtime.h>
#include <hip/hip_bf16.h>

constexpr int PSLEEP = 4;
constexpr int ISLEEP = 16;
constexpr int PDELAY = 40;
constexpr int NRAMP = 3;
constexpr int AUXH = 16;

typedef unsigned short u16;
typedef unsigned long long u64;
typedef short bf16x8 __attribute__((ext_vector_type(8)));
typedef short bf16x4 __attribute__((ext_vector_type(4)));
typedef float f32x4 __attribute__((ext_vector_type(4)));
typedef float f32x2 __attribute__((ext_vector_type(2)));
typedef unsigned u32x2 __attribute__((ext_vector_type(2)));
typedef unsigned u32x4 __attribute__((ext_vector_type(4)));
#define LAS __attribute__((address_space(3)))
#define RLX_AGENT __ATOMIC_RELAXED, __HIP_MEMORY_SCOPE_AGENT
#define RLX_WG __ATOMIC_RELAXED, __HIP_MEMORY_SCOPE_WORKGROUP

constexpr int S_ = 2048, D_ = 640, F_ = 1728;

__device__ __forceinline__ float bf2f(u16 v) { return __uint_as_float(((unsigned)v) << 16); }
typedef __bf16 bf16x2n __attribute__((ext_vector_type(2)));
__device__ __forceinline__ unsigned pk2(float lo, float hi) {
  return __builtin_bit_cast(unsigned, __builtin_convertvector((f32x2){lo, hi}, bf16x2n));
}
__device__ __forceinline__ float rbf(float v) { return __uint_as_float(pk2(v, v) << 16); }

#define CSTAMP(kid, i)
#define STAMP(kid, i)
template <int N> __device__ __forceinline__ void wait_vm() { asm volatile("s_waitcnt vmcnt(%0)" ::"n"(N) : "memory"); }
__device__ __forceinline__ void wait_lgkm0() { asm volatile("" ::: "memory"); __builtin_amdgcn_s_waitcnt(0xC07F); asm volatile("" ::: "memory"); }
__device__ __forceinline__ void bar() { asm volatile("" ::: "memory"); __builtin_amdgcn_s_barrier(); asm volatile("" ::: "memory"); }
template <int AUX, int OFF> __device__ __forceinline__ void dma16(const char* sbase, unsigned voff) {
  if constexpr (AUX == 16) asm volatile("global_load_lds_dwordx4 %0, %1 offset:%2 sc1" :: "v"(voff), "s"(sbase), "n"(OFF) : "memory");
  else asm volatile("global_load_lds_dwordx4 %0, %1 offset:%2" :: "v"(voff), "s"(sbase), "n"(OFF) : "memory");
}
__device__ __forceinline__ void set_m0(unsigned v) { asm volatile("s_mov_b32 m0, %0\n\ts_nop 0" :: "s"(v) : "memory"); }
__device__ __forceinline__ void st8(void* p, u32x2 v) { __hip_atomic_store((u64*)p, __builtin_bit_cast(u64, v), RLX_AGENT); }
__device__ __forceinline__ u32x2 ld8(const void* p) { return __builtin_bit_cast(u32x2, __hip_atomic_load((u64*)p, RLX_AGENT)); }

constexpr int NL = 2;
constexpr int NWAVES = 8;
constexpr int LDS_H = 120 * 1024;
constexpr int LDS_RING = 136 * 1024;
constexpr int LDS_X = LDS_RING;
constexpr int LDS_TOTAL = 160 * 1024;
constexpr int LDS_MISC = LDS_TOTAL - 256;

template <int KID_, int BM_, int BN_, int BK_, int WM_, int WN_, int WK_, int NSA_, int NSB_, int NK_, int AMODE_, int BMODE_, int SWAP_, int NPRE_>
struct Cfg {
  static constexpr int KID = KID_, BM = BM_, BN = BN_, BK = BK_, WM = WM_, WN = WN_, WK = WK_, NSA = NSA_, NSB = NSB_, NK = NK_;
  static constexpr int AMODE = AMODE_, BMODE = BMODE_, SWAP = SWAP_, NPRE = NPRE_;
  static constexpr int NC = WM * WN * WK;
  static constexpr int MF = BM / WM / 16, NF = BN / WN / 16, KSW = BK / 32 / WK;
  static constexpr int ABYTES = BM * BK * 2, BBYTES = BN * BK * 2;
  static constexpr int PA = ABYTES / 1024, PB = BBYTES / 1024;
  static constexpr int GA = PA / NL, GB = PB / NL, G = GA + GB;
  static constexpr int RA = NSA * ABYTES, RB = NSB * BBYTES;
  static_assert(PA % NL == 0 && PB % NL == 0, "pieces");
  static_assert(RA + RB <= (KID == 1 ? LDS_RING : LDS_H), "lds");
  static_assert(NSA <= NK && NSB <= NK, "stages");
  static_assert(KSW >= 1 && KSW <= 2, "ksw");
};

template <class C> struct Frags { bf16x8 a[C::KSW][C::MF]; bf16x8 b[C::KSW][C::NF]; };

template <class C, class Src, bool ISA>
struct Loader {
  static constexpr int G = ISA ? C::GA : C::GB, NS = ISA ? C::NSA : C::NSB, BYTES = ISA ? C::ABYTES : C::BBYTES;
  static constexpr int RING = NS * BYTES, MODE = ISA ? C::AMODE : C::BMODE, AUX = ISA ? Src::AUXA : Src::AUXB;
  const Src& src;
  int lw;
  unsigned lbase, cur;
  unsigned voff[G];
  __device__ __forceinline__ Loader(LAS char* lds, const Src& s, int lw_, int lane) : src(s), lw(lw_) {
    lbase = (unsigned)(size_t)lds + (ISA ? 0 : C::RA) + lw * (G * 1024);
#pragma unroll
    for (int i = 0; i < G; ++i) {
      const int p = (lw * G + i) * 64 + lane;
      unsigned v;
      if constexpr (MODE == 0) v = p * 16;
      else if constexpr (MODE == 1) { const int row = p >> 3, c = (p & 7) ^ ((row >> 1) & 7); v = row * (ISA ? Src::ldA : Src::ldB) + c * 16; }
      else { const int q = p % (C::BK * 8), k = q >> 3, c = (q & 7) ^ ((((k >> 3) & 1) << 2) | (((k >> 1) & 1) << 1)); v = k * Src::ldB + c * 16; }
      voff[i] = v - (i & 3) * 1024;
    }
  }
  template <int I> __device__ __forceinline__ void pieces(const char* gb, unsigned l) {
    if constexpr (I < G) {
      if constexpr ((I & 3) == 0) set_m0(l + I * 1024);
      dma16<AUX, (I & 3) * 1024>(gb, voff[I]);
      pieces<I + 1>(gb, l);
    }
  }
  __device__ __forceinline__ void issue(int k, unsigned buf) {
    pieces<0>(ISA ? src.baseA(k) : src.baseB(k, lw), lbase + buf);
  }
  __device__ __forceinline__ void fill(int s0, int s1) {
#pragma unroll 1
    for (int s = s0; s < s1; ++s) issue(s, s * BYTES);
  }
  template <int K, class Hook> __device__ __forceinline__ void tail(Hook hook) {
    if constexpr (K < C::NK - 1) {
      wait_vm<(C::NK - 2 - K) * G + Hook::extra(K)>();
      bar();
      hook.template at<K>();
      tail<K + 1>(hook);
    }
  }
  template <int N0, class Hook> __device__ __forceinline__ void run(Hook hook) {
    static_assert(N0 >= 1 && N0 <= NS && (NS - 1) * G <= 63, "ring");
    wait_vm<(N0 - 1) * G>();
    bar();
    fill(N0, NS);
    cur = 0;
#pragma unroll 1
    for (int k = 0; k < C::NK - NS; ++k) {
      wait_vm<(NS - 2) * G>();
      bar();
      issue(k + NS, cur);
      cur += BYTES; if (cur == RING) cur = 0;
    }
    hook.template at<C::NK - NS - 1>();
    tail<C::NK - NS>(hook);
  }
};
struct NoHook {
  static constexpr int extra(int) { return 0; }
  template <int K> __device__ __forceinline__ void at() {}
};

template <class C>
struct Consumer {
  LAS char* lds;
  int wid, lane, wm, wn, wk;
  unsigned rdA[2], rdB[4];

  __device__ __forceinline__ Consumer(LAS char* lds_, int wid_, int lane_) : lds(lds_), wid(wid_), lane(lane_) {
    wk = wid % C::WK; wn = (wid / C::WK) % C::WN; wm = wid / (C::WK * C::WN);
    const int fr = lane & 15, fq = lane >> 4;
    const int arow = wm * (C::BM / C::WM) + fr;
    const int ss0 = wk * C::KSW;
#pragma unroll
    for (int s = 0; s < 2; ++s) {
      const int ss = ss0 + s;
      if constexpr (C::AMODE == 0) rdA[s] = ((ss0 * 4 + fq) * C::BM + arow) * 16;
      else rdA[s] = arow * 128 + ((((ss & 1) * 4 + fq) ^ ((fr >> 1) & 7)) * 16);
    }
    if constexpr (C::BMODE <= 1) {
      const int brow = wn * (C::BN / C::WN) + fr;
#pragma unroll
      for (int s = 0; s < 2; ++s) {
        const int ss = ss0 + s;
        if constexpr (C::BMODE == 0) rdB[s] = C::RA + ((ss0 * 4 + fq) * C::BN + brow) * 16;
        else rdB[s] = C::RA + brow * 128 + ((((ss & 1) * 4 + fq) ^ ((fr >> 1) & 7)) * 16);
      }
    } else {
      const int g = lane >> 4, q = (lane >> 2) & 3, p = lane & 3;
#pragma unroll
      for (int j = 0; j < 4; ++j) {
        const int jj = 2 * wn + (j & 1), mat = j >> 1;
        rdB[j] = C::RA + mat * (C::BK * 128) + (32 * ss0 + 8 * g + q) * 128 + ((((jj ^ (((g & 1) << 1) | (q >> 1))) << 1) | (p >> 1)) * 16) + 8 * (p & 1);
      }
    }
  }

  __device__ __forceinline__ void read(Frags<C>& f, unsigned bufa, unsigned bufb) {
    LAS char* a = lds + bufa; LAS char* b = lds + bufb;
#pragma unroll
    for (int s = 0; s < C::KSW; ++s) {
      if constexpr (C::BMODE == 0) {
#pragma unroll
        for (int n = 0; n < C::NF; ++n) f.b[s][n] = *(const LAS bf16x8*)(b + rdB[0] + s * (C::BN * 64) + n * 256);
      } else if constexpr (C::BMODE == 1) {
#pragma unroll
        for (int n = 0; n < C::NF; ++n) f.b[s][n] = *(const LAS bf16x8*)(b + rdB[s] + n * 2048);
      } else {
#pragma unroll
        for (int n = 0; n < C::NF; ++n) {
          bf16x4 lo = __builtin_amdgcn_ds_read_tr16_b64_v4i16((LAS bf16x4*)(b + rdB[n] + s * 4096));
          bf16x4 hi = __builtin_amdgcn_ds_read_tr16_b64_v4i16((LAS bf16x4*)(b + rdB[n] + s * 4096 + 512));
          f.b[s][n] = __builtin_shufflevector(lo, hi, 0, 1, 2, 3, 4, 5, 6, 7);
        }
      }
      if constexpr (C::AMODE == 0) {
#pragma unroll
        for (int m = 0; m < C::MF; ++m) f.a[s][m] = *(const LAS bf16x8*)(a + rdA[0] + s * (C::BM * 64) + m * 256);
      } else {
#pragma unroll
        for (int m = 0; m < C::MF; ++m) f.a[s][m] = *(const LAS bf16x8*)(a + rdA[s] + m * 2048);
      }
    }
  }

  __device__ __forceinline__ void mma(f32x4 (&acc)[C::MF][C::NF], const Frags<C>& f) {
#pragma unroll
    for (int s = 0; s < C::KSW; ++s)
#pragma unroll
      for (int m = 0; m < C::MF; ++m)
#pragma unroll
        for (int n = 0; n < C::NF; ++n)
          acc[m][n] = C::SWAP ? __builtin_amdgcn_mfma_f32_16x16x32_bf16(f.b[s][n], f.a[s][m], acc[m][n], 0, 0, 0)
                              : __builtin_amdgcn_mfma_f32_16x16x32_bf16(f.a[s][m], f.b[s][n], acc[m][n], 0, 0, 0);
  }
  static constexpr int NRD = C::KSW * (C::MF + (C::BMODE >= 2 ? 2 : 1) * C::NF);
  static constexpr int NMM = C::KSW * C::MF * C::NF;
  __device__ __forceinline__ void interleave() {
    constexpr int RPM = (NRD + NMM - 3) / (NMM - 2);
#pragma unroll
    for (int i = 0; i < NMM; ++i) {
      __builtin_amdgcn_sched_group_barrier(0x100, RPM, 0);
      __builtin_amdgcn_sched_group_barrier(0x008, 1, 0);
    }
  }
  unsigned na, nb;
  __device__ __forceinline__ void step(f32x4 (&acc)[C::MF][C::NF], Frags<C>& cur, Frags<C>& nxt) {
    na += C::ABYTES; if (na == C::RA) na = 0;
    nb += C::BBYTES; if (nb == C::RB) nb = 0;
    wait_lgkm0(); bar();
    __builtin_amdgcn_sched_barrier(0);
    read(nxt, na, nb);
    mma(acc, cur);
    interleave();
    __builtin_amdgcn_sched_barrier(0);
  }

  template <class Hook> __device__ __forceinline__ void run(f32x4 (&acc)[C::MF][C::NF], Hook hook) {
    Frags<C> f0, f1;
#pragma unroll
    for (int m = 0; m < C::MF; ++m)
#pragma unroll
      for (int n = 0; n < C::NF; ++n) { acc[m][n] = (f32x4){0.f, 0.f, 0.f, 0.f}; asm volatile("" : "+v"(acc[m][n])); }
    bar();
    if (wid == 0) { STAMP(C::KID, 1); CSTAMP(C::KID, 8); }
    read(f0, 0, 0);
    na = 0; nb = 0;
    if (wid == 0) STAMP(C::KID, 4);
#pragma unroll 1
    for (int k = 0; k + 2 < C::NK; k += 2) {
      step(acc, f0, f1);
      step(acc, f1, f0);
    }
    if (wid == 0) STAMP(C::KID, 5);
    if constexpr (C::NK % 2 == 0) {
      step(acc, f0, f1);
      wait_lgkm0(); bar();
      hook();
      mma(acc, f1);
    } else {
      wait_lgkm0(); bar();
      mma(acc, f0);
    }
    if (wid == 0) { STAMP(C::KID, 2); CSTAMP(C::KID, 9); }
  }

  static constexpr bool SPLM = (C::MF % 2 == 0);
  static constexpr int RM = SPLM ? C::MF / 2 : C::MF, RN = SPLM ? C::NF : C::NF / 2;
  __device__ __forceinline__ void reduce_k(f32x4 (&acc)[C::MF][C::NF], f32x4 (&red)[RM][RN]) {
    static_assert(C::WK == 2, "");
    static_assert(LDS_X + C::NC * RM * RN * 1024 <= LDS_MISC, "");
    LAS f32x4* x = (LAS f32x4*)(lds + LDS_X);
#pragma unroll
    for (int m = 0; m < RM; ++m)
#pragma unroll
      for (int n = 0; n < RN; ++n) {
        const f32x4 lo = acc[m][n], hi = SPLM ? acc[m + RM][n] : acc[m][n + RN];
        x[(wid * RM * RN + m * RN + n) * 64 + lane] = wk ? lo : hi;
        red[m][n] = wk ? hi : lo;
      }
    wait_lgkm0(); bar();
#pragma unroll
    for (int m = 0; m < RM; ++m)
#pragma unroll
      for (int n = 0; n < RN; ++n) red[m][n] += x[((wid ^ 1) * RM * RN + m * RN + n) * 64 + lane];
  }
};

constexpr int FLE = 0;
constexpr int FL0 = 32;
constexpr int FL1 = FL0 + 2048;
constexpr int FL2 = FL1 + 16 * 16;
constexpr int FL3 = FL2 + 8 * 32;
constexpr int FLW = FL3 + 8 * 32;
constexpr int FLC = FLW + 48;

__device__ __forceinline__ void publish(LAS unsigned* arr, unsigned nwaves, u64* flag, u64 tag, int lane, int kid) {
  if (threadIdx.x < 64) STAMP(kid, 7);
  wait_vm<0>();
  if (lane == 0) {
    const unsigned old = __hip_atomic_fetch_add(arr, 1u, RLX_WG);
    if (old == nwaves - 1) {
      __hip_atomic_store(flag, tag, RLX_AGENT);
      STAMP(kid, 6);
    }
  }
}
__device__ __forceinline__ void lds_wait(LAS unsigned* w, unsigned want) {
  const unsigned a = (unsigned)(size_t)w;
#pragma unroll 1
  for (unsigned i = 0; i < (1u << 24); ++i) {
    unsigned v;
    asm volatile("ds_read_b32 %0, %1\n\ts_waitcnt lgkmcnt(0)" : "=v"(v) : "v"(a) : "memory");
    if (__builtin_amdgcn_readfirstlane(v) >= want) break;
    __builtin_amdgcn_s_sleep(1);
  }
}
template <int SLEEP, int NW, int DELAY = PDELAY>
__device__ __forceinline__ void await(const u64* word, bool need, u64 tag, int lw, LAS unsigned* go, unsigned epoch) {
  if (lw == 0) {
    const u64 t0 = wall_clock64();
    if (DELAY) __builtin_amdgcn_s_sleep(DELAY);
    for (;;) {
      bool ok = true;
#pragma unroll
      for (int i = 0; i < NW; ++i) ok &= __hip_atomic_load(word + i * 64, RLX_AGENT) == tag;
      if (__all(!need || ok)) break;
      if (wall_clock64() - t0 > 2000000ull) break;
      if (SLEEP > PSLEEP && __any(need && ok)) __builtin_amdgcn_s_sleep(PSLEEP); else __builtin_amdgcn_s_sleep(SLEEP);
    }
    __hip_atomic_store(go, epoch, RLX_WG);
  } else {
    lds_wait(go, epoch);
  }
  __builtin_amdgcn_fence(__ATOMIC_ACQUIRE, "wavefront");
}
using C1 = Cfg<1, 80, 128, 64, 1, 4, 1, 4, 6, 32, 0, 1, 0, 2>;
using C2 = Cfg<2, 64, 80, 64, 2, 1, 2, 4, 6, 20, 0, 1, 1, 6>;
using C3 = Cfg<3, 256, 128, 32, 2, 2, 1, 4, 5, 20, 0, 3, 1, 3>;
using C4 = Cfg<4, 64, 80, 64, 2, 1, 2, 4, 6, 27, 0, 0, 1, 6>;
struct Src1 {
  const char* a; const char* bre; const char* bim; int o, c;
  static constexpr int ldA = 0, ldB = S_ * 2, AUXA = AUXH, AUXB = 0;
  __device__ __forceinline__ int kt(int k) const { const int s = (k + o) & 31; return (s & 24) | ((s + c) & 7); }
  __device__ __forceinline__ const char* baseA(int k) const { return a + kt(k) * (80 * 128); }
  __device__ __forceinline__ const char* baseB(int k, int im) const { return (im ? bim : bre) + kt(k) * 128; }
};
struct Src2 {
  const char* a; const char* bre; const char* bim; int rot;
  static constexpr int ldA = 0, ldB = D_ * 2, AUXA = AUXH, AUXB = 0;
  __device__ __forceinline__ int kt(int k) const { const int s = k + rot; return s >= 20 ? s - 20 : s; }
  __device__ __forceinline__ const char* baseA(int k) const { return a + kt(k) * (64 * 128); }
  __device__ __forceinline__ const char* baseB(int k, int) const { const int t = kt(k); return t < 10 ? bre + t * 128 : bim + (t - 10) * 128; }
};
struct Src3 {
  const char* a; const char* bg; const char* bu; int rot;
  static constexpr int ldA = 0, ldB = F_ * 2, AUXA = AUXH, AUXB = 0;
  __device__ __forceinline__ int kt(int k) const { const int s = k + rot; return s >= 20 ? s - 20 : s; }
  __device__ __forceinline__ const char* baseA(int k) const { return a + kt(k) * (256 * 64); }
  __device__ __forceinline__ const char* baseB(int k, int up) const { return (up ? bu : bg) + kt(k) * (32 * F_ * 2); }
};
struct Src4 {
  const char* a; const char* b;
  static constexpr int ldA = 0, ldB = 0, AUXA = AUXH, AUXB = AUXH;
  __device__ __forceinline__ const char* baseA(int k) const { return a + k * (64 * 128); }
  __device__ __forceinline__ const char* baseB(int k, int) const { return b + k * (80 * 128); }
};

struct Params {
  const u16* x; const u16* sre; const u16* sim; const u16* hre; const u16* him; const u16* wg; const u16* wu; const u16* wd;
  u16* xnI; u16* pqI; u16* hI; u16* hhI; u16* wdI; f32x2* stats; float* cs; u64* flags; u16* out;
};

template <int CTRL, int RMASK> __device__ __forceinline__ float dpp_add(float v) {
  return v + __int_as_float(__builtin_amdgcn_update_dpp(0, __float_as_int(v), CTRL, RMASK, 0xf, true));
}
__device__ __forceinline__ float wave_sum(float v) {
  v = dpp_add<0x111, 0xf>(v); v = dpp_add<0x112, 0xf>(v); v = dpp_add<0x114, 0xf>(v); v = dpp_add<0x118, 0xf>(v);
  v = dpp_add<0x142, 0xa>(v); v = dpp_add<0x143, 0xc>(v);
  return __int_as_float(__builtin_amdgcn_readlane(__float_as_int(v), 63));
}
__device__ __forceinline__ float sum_rows(float v) {
  const auto a = __builtin_amdgcn_permlane16_swap(__float_as_uint(v), __float_as_uint(v), false, false);
  v = __uint_as_float(a[0]) + __uint_as_float(a[1]);
  const auto b = __builtin_amdgcn_permlane32_swap(__float_as_uint(v), __float_as_uint(v), false, false);
  return __uint_as_float(b[0]) + __uint_as_float(b[1]);
}
__device__ __forceinline__ u32x4 col8(LAS char* tile, int cb, int lane) {
  LAS char* a = tile + ((lane >> 2) & 3) * 1296 + (cb * 64 + (lane >> 4) * 16 + (lane & 3) * 4) * 2;
  const bf16x4 lo = __builtin_amdgcn_ds_read_tr16_b64_v4i16((LAS bf16x4*)a);
  const bf16x4 hi = __builtin_amdgcn_ds_read_tr16_b64_v4i16((LAS bf16x4*)(a + 4 * 1296));
  return __builtin_bit_cast(u32x4, __builtin_shufflevector(lo, hi, 0, 1, 2, 3, 4, 5, 6, 7));
}
struct Hook3 {
  const Params& p; LAS f32x4* rs; LAS float* csl; LAS unsigned* arr; u64 tag; int mt3, j3, lw, lane;
  f32x2 (&pv)[2][8]; u64& fc; float& cv;
  static constexpr int K0 = C3::NK - C3::NSB - 1;
  static constexpr int extra(int k) { return k <= K0 + 2 ? 17 : 1; }
  template <int K> __device__ __forceinline__ void at() {
    if constexpr (K == K0) {
      fc = __hip_atomic_load(p.flags + FLC + j3 * 16, RLX_AGENT);
#pragma unroll
      for (int h = 0; h < 2; ++h)
#pragma unroll
        for (int i = 0; i < 8; ++i) pv[h][i] = __builtin_bit_cast(f32x2, ld8(p.stats + i * S_ + mt3 * 256 + lw * 128 + h * 64 + lane));
    } else if constexpr (K == K0 + 2) {
      if (!__all(fc == tag)) await<PSLEEP, 1>(p.flags + FLC + j3 * 16, true, tag, 0, arr + 11, 1u);
      __builtin_amdgcn_fence(__ATOMIC_ACQUIRE, "wavefront");
      cv = __hip_atomic_load(p.cs + j3 * 128 + lw * 64 + lane, RLX_AGENT);
#pragma unroll
      for (int h = 0; h < 2; ++h) {
        float mean = 0.f, q = 0.f, dd = 0.f;
#pragma unroll
        for (int i = 0; i < 8; ++i) { mean += pv[h][i][0]; q += pv[h][i][1]; }
        mean *= 0.125f;
#pragma unroll
        for (int i = 0; i < 8; ++i) { const float d = pv[h][i][0] - mean; dd += d * d; }
        q += 80.0f * dd;
        const float den = rbf(__builtin_amdgcn_sqrtf(rbf(rbf(q * (1.0f / D_)) + 1e-5f)));
        rs[lw * 128 + h * 64 + lane] = (f32x4){-rbf(mean), den, -1.44269504089f * __builtin_amdgcn_rcpf(den), 0.f};
      }
    } else if constexpr (K == C3::NK - 2) {
      csl[lw * 64 + lane] = cv;
      wait_lgkm0();
    }
  }
};

__global__ __launch_bounds__(NWAVES * 64) void mega(const Params p) {
  __shared__ __attribute__((aligned(1024))) char smem_[LDS_TOTAL];
  LAS char* lds = (LAS char*)smem_;
  const int wid = __builtin_amdgcn_readfirstlane(threadIdx.x >> 6), lane = threadIdx.x & 63;
  const int bid = blockIdx.x, g = bid & 7, idx = bid >> 3;
  LAS unsigned* arr = (LAS unsigned*)(lds + LDS_MISC);
  if (wid == 0) STAMP(0, 0);
  const u16* xr = p.x + (bid * 8 + wid) * D_;
  const bf16x8 v0 = *(const bf16x8*)(xr + lane * 8);
  const bf16x8 v1 = *(const bf16x8*)(xr + 512 + (lane & 15) * 8);
  const u64 ep = __hip_atomic_load(p.flags + ((wid == 7 && lane == 63) ? bid * 16 : FLE), RLX_AGENT);
  if (threadIdx.x < 16) arr[threadIdx.x] = 0u;
  const int j2 = idx & 7, mq = idx >> 3, mt2 = g * 4 + mq;
  const int mt3 = (g & 6) + (idx & 1), j3 = (g & 1) * 14 + (idx >> 1);
  const bool on3 = idx < 28 && j3 < 27;
  LAS f32x4* rs = (LAS f32x4*)(lds + LDS_X);
  LAS float* csl = (LAS float*)(lds + LDS_X + 4096);

  const Src1 s1{(const char*)(p.xnI + (size_t)j2 * 256 * 80 * 8), (const char*)(p.sre + (size_t)mt2 * 64 * S_),
                (const char*)(p.sim + ((ptrdiff_t)mt2 - 1) * 64 * S_), (g + 2 * mq) & 7, j2};
  {
    float f[16];
#pragma unroll
    for (int i = 0; i < 8; ++i) { f[i] = bf2f((u16)v0[i]); f[8 + i] = bf2f((u16)v1[i]); }
    const float piv = __int_as_float(__builtin_amdgcn_readfirstlane(__float_as_int(f[0])));
    if (wid == 0) STAMP(0, 4);
    float s0 = 0.f, s1 = 0.f, q0 = 0.f, q1 = 0.f;
#pragma unroll
    for (int i = 0; i < 8; ++i) { const float d = f[i] - piv, e = f[8 + i] - piv; s0 += d; q0 += d * d; s1 += e; q1 += e * e; }
    const float sd = wave_sum(s0 + (lane < 16 ? s1 : 0.f)) * (1.0f / D_);
    const float qd = wave_sum(q0 + (lane < 16 ? q1 : 0.f)) * (1.0f / D_);
    const float mb = rbf(piv + sd), vb = rbf(fmaxf(qd - sd * sd, 0.f));
    const float rden = __builtin_amdgcn_rcpf(rbf(__builtin_amdgcn_sqrtf(rbf(vb + 1e-5f))));
    u32x4 w0, w1;
#pragma unroll
    for (int i = 0; i < 4; ++i) {
      w0[i] = pk2(rbf(f[2 * i] - mb) * rden, rbf(f[2 * i + 1] - mb) * rden);
      w1[i] = pk2(rbf(f[8 + 2 * i] - mb) * rden, rbf(f[9 + 2 * i] - mb) * rden);
    }
    LAS char* tb = lds + LDS_X;
    *(LAS u32x4*)(tb + wid * 1296 + lane * 16) = w0;
    if (lane < 16) *(LAS u32x4*)(tb + wid * 1296 + 1024 + lane * 16) = w1;
    wait_lgkm0(); bar();
  }
  const u64 tag = (((u64)(unsigned)__builtin_amdgcn_readfirstlane((unsigned)(ep >> 32)) << 32) | (unsigned)__builtin_amdgcn_readfirstlane((unsigned)ep)) + 1ull;
  {
    LAS char* tb = lds + LDS_X;
    auto rsrc = __builtin_amdgcn_make_buffer_rsrc(p.xnI, 0, S_ * D_ * 2, 0x00020000);
#pragma unroll
    for (int h = 0; h < 2; ++h) {
      const int cb = h * 8 + wid;
      if (cb < 10) {
        const int d = cb * 64 + lane, j = d / 80, n = d % 80;
        __builtin_amdgcn_raw_buffer_store_b128(col8(tb, cb, lane), rsrc, ((j * 256 + bid) * 80 + n) * 16, 0, AUXH);
      }
    }
    if (wid == 0) STAMP(0, 7);
    wait_vm<0>();
    unsigned old = 0;
    if (lane == 0) old = __hip_atomic_fetch_add(arr + 5, 1u, RLX_WG);
    if (__builtin_amdgcn_readfirstlane(old) == 7u && lane < 8) __hip_atomic_store(p.flags + FL0 + lane * 256 + bid, tag, RLX_AGENT);
    if (wid == 0) STAMP(0, 3);
  }
  const Src2 s2{(const char*)(p.pqI + (size_t)mt2 * 160 * 64 * 8), (const char*)(p.hre + (size_t)j2 * 80 * D_), (const char*)(p.him + (size_t)j2 * 80 * D_), (mt2 * 5) >> 3};
  const Src3 s3{(const char*)(p.hI + (size_t)mt3 * 80 * 256 * 8), (const char*)(p.wg + j3 * 64), (const char*)(p.wu + j3 * 64), (mt3 * 5) >> 1};
  const Src4 s4{(const char*)(p.hhI + (size_t)mt2 * 216 * 64 * 8), (const char*)(p.wdI + (size_t)j2 * 216 * 80 * 8)};
  if (wid >= 6) {
    const int lw = wid - 6;
    {
      Loader<C1, Src1, true> L(lds, s1, lw, lane);
      lds_wait(arr + 5, 8);
      await<PSLEEP, 4>(p.flags + FL0 + g * 256 + lane, true, tag, lw, arr + 3, 1u);
      if (bid == 0 && lw == 0 && lane == 0) __hip_atomic_store(p.flags + FLE, tag, RLX_AGENT);
      if (lw == 0) STAMP(1, 0);
      wait_vm<0>();
      L.fill(0, NRAMP); L.run<NRAMP>(NoHook{});
      bar();
    }
    {
      Loader<C2, Src2, true> L(lds, s2, lw, lane);
      lds_wait(arr + 0, C1::NC);
      await<PSLEEP, 1>(p.flags + FL1 + mt2 * 8 + (lane & 7), true, tag, lw, arr + 3, 2u);
      if (lw == 0) STAMP(2, 0);
      wait_vm<0>();
      L.fill(0, NRAMP); L.run<NRAMP>(NoHook{});
      bar(); bar();
    }
    if (on3) {
      Loader<C3, Src3, true> L(lds, s3, lw, lane);
      lds_wait(arr + 1, C2::NC);
      await<PSLEEP, 1, 28>(p.flags + FL2 + (mt3 * 32 + (lane & 31)), true, tag, lw, arr + 3, 3u);
      if (lw == 0) STAMP(3, 0);
      wait_vm<0>();
      L.fill(0, NRAMP); L.run<NRAMP>(NoHook{});
      bar();
    }
    {
      Loader<C4, Src4, true> L(lds, s4, lw, lane);
      lds_wait(arr + 2, 4);
      if (on3) await<PSLEEP, 1>(p.flags + FL3 + (g * 32 + (lane & 31)), (lane & 31) < 27, tag, lw, arr + 3, 4u);
      else await<ISLEEP, 1>(p.flags + FL3 + (g * 32 + (lane & 31)), (lane & 31) < 27, tag, lw, arr + 3, 4u);
      if (lw == 0) STAMP(4, 0);
      wait_vm<0>();
      L.fill(0, NRAMP); L.run<NRAMP>(NoHook{});
      bar(); bar();
    }
    return;
  }
  if (wid >= 4) {
    const int lw = wid - 4;
    {
      Loader<C1, Src1, false> L(lds, s1, lw, lane);
      lds_wait(arr + 5, 8); L.fill(0, C1::NPRE);
      L.run<C1::NPRE>(NoHook{});
      bar();
    }
    {
      Loader<C2, Src2, false> L(lds, s2, lw, lane);
      lds_wait(arr + 0, C1::NC); L.fill(0, C2::NPRE);
      L.run<C2::NPRE>(NoHook{});
      bar(); bar();
    }
    if (on3) {
      Loader<C3, Src3, false> L(lds, s3, lw, lane);
      static_assert(Hook3::K0 + 2 < C3::NK - 2, "");
      f32x2 pv[2][8]; u64 fc; float cv;
      lds_wait(arr + 1, C2::NC); L.fill(0, C3::NPRE);
      L.run<C3::NPRE>(Hook3{p, rs, csl, arr, tag, mt3, j3, lw, lane, pv, fc, cv});
      bar();
    }
    {
      Loader<C4, Src4, false> L(lds, s4, lw, lane);
      await<ISLEEP, 1, 0>(p.flags + FLW + min(lane, 39), true, tag, lw, arr + 13, 1u);
      wait_vm<0>();
      lds_wait(arr + 2, 4); L.fill(0, C4::NPRE);
      L.run<C4::NPRE>(NoHook{});
      bar(); bar();
    }
    return;
  }

#define FRESH_LANE int lane_ = threadIdx.x & 63; asm volatile("" : "+v"(lane_)); const int lane = lane_, fr = lane & 15, fq = lane >> 4; (void)fr; (void)fq;
  {
    FRESH_LANE
    Consumer<C1> L(lds, wid, lane);
    f32x4 acc[C1::MF][C1::NF];
    L.run(acc, [] {});
    const int isQ = wid >> 1;
    const float sg = isQ ? -1.f : 1.f;
#pragma unroll
    for (int n = 0; n < 2; ++n) {
      const int r = (wid & 1) * 32 + n * 16 + fr;
#pragma unroll
      for (int m = 0; m < 5; ++m) {
        const int koct = isQ * 80 + j2 * 10 + m * 2 + (fq >> 1);
        u32x2 w; w[0] = pk2(sg * acc[m][n][0], sg * acc[m][n][1]); w[1] = pk2(sg * acc[m][n][2], sg * acc[m][n][3]);
        st8(p.pqI + (((size_t)mt2 * 160 + koct) * 64 + r) * 8 + (fq & 1) * 4, w);
      }
    }
    publish(arr + 0, C1::NC, p.flags + FL1 + mt2 * 8 + j2, tag, lane, 1);
    if (wid == 0) STAMP(1, 3);
  }
  {
    FRESH_LANE
    Consumer<C2> L(lds, wid, lane);
    f32x4 acc[C2::MF][C2::NF];
    const int t = mt2 * 64 + wid * 16 + fr;
    bf16x4 xv[C2::NF];
#pragma unroll
    for (int n = 0; n < C2::NF; ++n) xv[n] = *(const bf16x4*)(p.x + (size_t)t * D_ + j2 * 80 + n * 16 + fq * 4);
#pragma unroll
    for (int n = 0; n < C2::NF; ++n) asm volatile("" : "+v"(xv[n]));
    L.run(acc, [] {});
    f32x4 red[1][5];
    L.reduce_k(acc, red);
    float hv[C2::NF][4];
    float s = 0.f;
#pragma unroll
    for (int n = 0; n < C2::NF; ++n) {
      u32x2 w;
      w[0] = pk2(red[0][n][0] + bf2f((u16)xv[n][0]), red[0][n][1] + bf2f((u16)xv[n][1]));
      w[1] = pk2(red[0][n][2] + bf2f((u16)xv[n][2]), red[0][n][3] + bf2f((u16)xv[n][3]));
      hv[n][0] = __uint_as_float(w[0] << 16); hv[n][1] = __uint_as_float(w[0] & 0xffff0000u);
      hv[n][2] = __uint_as_float(w[1] << 16); hv[n][3] = __uint_as_float(w[1] & 0xffff0000u);
      s += (hv[n][0] + hv[n][1]) + (hv[n][2] + hv[n][3]);
      const int koct = j2 * 10 + n * 2 + (fq >> 1);
      st8(p.hI + (((size_t)(t >> 8) * 80 + koct) * 256 + (t & 255)) * 8 + (fq & 1) * 4, w);
      *(LAS u32x2*)(lds + LDS_H + ((wid * 5 + n) * 64 + lane) * 8) = w;
    }
    const float mean = sum_rows(s) * (1.0f / 80.0f);
    float q = 0.f;
#pragma unroll
    for (int n = 0; n < C2::NF; ++n)
#pragma unroll
      for (int r = 0; r < 4; ++r) { const float d = hv[n][r] - mean; q += d * d; }
    q = sum_rows(q);
    if (fq == 0) st8(p.stats + j2 * S_ + t, __builtin_bit_cast(u32x2, (f32x2){mean, q}));
    publish(arr + 1, C2::NC, p.flags + FL2 + (g * 32 + idx), tag, lane, 2);
    if (wid == 0) STAMP(2, 3);
  }
  if (on3) {
    FRESH_LANE
    Consumer<C3> L(lds, wid, lane);
    f32x4 acc[C3::MF][C3::NF];
    f32x4 st[C3::MF], cg[2], cu[2];
    L.run(acc, [&] {
#pragma unroll
      for (int m = 0; m < C3::MF; ++m) st[m] = rs[L.wm * 128 + m * 16 + fr];
#pragma unroll
      for (int i = 0; i < 2; ++i) {
        const int fl = L.wn * 32 + i * 16 + fq * 4;
        cg[i] = *(const LAS f32x4*)(csl + fl); cu[i] = *(const LAS f32x4*)(csl + 64 + fl);
      }
    });
    u16* hb = p.hhI + (((size_t)(mt3 * 4 + L.wm * 2) * 216 + j3 * 8 + L.wn * 4 + (fq >> 1)) * 64 + fr) * 8 + (fq & 1) * 4;
#pragma unroll
    for (int m = 0; m < C3::MF; ++m) {
      u32x2 w[2];
#pragma unroll
      for (int i = 0; i < 2; ++i) {
        const f32x4 s0 = {st[m][0], st[m][0], st[m][0], st[m][0]}, s1 = {st[m][1], st[m][1], st[m][1], st[m][1]};
        const f32x4 x = __builtin_elementwise_fma(cg[i], s0, acc[m][i]) * st[m][2];
        f32x4 e;
#pragma unroll
        for (int r = 0; r < 4; ++r) e[r] = __builtin_amdgcn_exp2f(x[r]);
        const f32x4 d = __builtin_elementwise_fma(e, s1, s1);
        f32x4 rc;
#pragma unroll
        for (int r = 0; r < 4; ++r) rc[r] = __builtin_amdgcn_rcpf(d[r]);
        const f32x4 o = __builtin_elementwise_fma(cu[i], s0, acc[m][i + 2]) * rc;
        w[i][0] = pk2(o[0], o[1]); w[i][1] = pk2(o[2], o[3]);
        st8(hb + (m >> 2) * (216 * 64 * 8) + i * (2 * 64 * 8) + (m & 3) * 128, w[i]);
      }
    }
    publish(arr + 2, C3::NC, p.flags + FL3 + (mt3 * 32 + j3), tag, lane, 3);
    if (wid == 0) STAMP(3, 3);
  } else {
    FRESH_LANE
    const int id = idx >= 28 ? g * 4 + idx - 28 : 32 + (g >> 1) * 2 + idx - 26;
    auto rsrc = __builtin_amdgcn_make_buffer_rsrc(p.wdI, 0, F_ * D_ * 2, 0x00020000);
    lds_wait(arr + 1, C2::NC);
    if (id < 27) {
      const u16* w = ((wid & 2) ? p.wu : p.wg) + (size_t)((wid & 1) * 320 + (lane >> 3)) * F_ + id * 64 + (lane & 7) * 8;
      f32x2 a[4] = {{0.f, 0.f}, {0.f, 0.f}, {0.f, 0.f}, {0.f, 0.f}};
#pragma unroll 1
      for (int b = 0; b < 2; ++b) {
        u32x4 v[20];
#pragma unroll
        for (int i = 0; i < 20; ++i) v[i] = *(const u32x4*)(w + (size_t)(b * 20 + i) * 8 * F_);
#pragma unroll
        for (int i = 0; i < 20; ++i)
#pragma unroll
          for (int e = 0; e < 4; ++e) a[e] += (f32x2){__uint_as_float(v[i][e] << 16), __uint_as_float(v[i][e] & 0xffff0000u)};
      }
      LAS float* sf = (LAS float*)(lds + LDS_X);
#pragma unroll
      for (int e = 0; e < 4; ++e)
#pragma unroll
        for (int c = 0; c < 2; ++c) { float t = sum_rows(a[e][c]); t += __shfl_xor(t, 8); a[e][c] = t; }
      if (lane < 8) {
        *(LAS f32x4*)(sf + wid * 64 + lane * 8) = (f32x4){a[0][0], a[0][1], a[1][0], a[1][1]};
        *(LAS f32x4*)(sf + wid * 64 + lane * 8 + 4) = (f32x4){a[2][0], a[2][1], a[3][0], a[3][1]};
      }
      wait_lgkm0();
      unsigned old = 0;
      if (lane == 0) old = __hip_atomic_fetch_add(arr + 12, 1u, RLX_WG);
      if (__builtin_amdgcn_readfirstlane(old) == 3u) {
        __hip_atomic_store(p.cs + id * 128 + lane, sf[lane] + sf[64 + lane], RLX_AGENT);
        __hip_atomic_store(p.cs + id * 128 + 64 + lane, sf[128 + lane] + sf[192 + lane], RLX_AGENT);
        wait_vm<0>();
        if (lane == 0) __hip_atomic_store(p.flags + FLC + id * 16, tag, RLX_AGENT);
        STAMP(0, 5);
      }
    }
    const int k0 = id < 27 ? id * 3 : 81 + (id - 27) * 11, nk = id < 27 ? 3 : min(11, 216 - k0);
#pragma unroll 1
    for (int i = 0; i < 2 * nk; ++i) {
      const int row = (i & 1) * 4 + wid;
      const char* wr = (const char*)(p.wd + (size_t)((k0 + (i >> 1)) * 8 + row) * D_);
      set_m0((unsigned)(size_t)lds + (i >> 1) * 10368 + row * 1296);
      dma16<0, 0>(wr, lane * 16);
      if (lane < 16) dma16<0, 1024>(wr, lane * 16);
    }
    wait_vm<0>();
    if (lane == 0) __hip_atomic_fetch_add(arr + 6, 1u, RLX_WG);
    lds_wait(arr + 6, 4);
#pragma unroll 1
    for (int i = 0; i < nk; ++i) {
#pragma unroll
      for (int h = 0; h < 3; ++h) {
        const int cb = h * 4 + wid;
        if (cb < 10) {
          const int d = cb * 64 + lane, j = d / 80, n = d % 80;
          __builtin_amdgcn_raw_buffer_store_b128(col8(lds + i * 10368, cb, lane), rsrc, ((j * 216 + k0 + i) * 80 + n) * 16, 0, AUXH);
        }
      }
    }
    publish(arr + 2, 4, p.flags + FLW + id, tag, lane, 0);
  }
  {
    FRESH_LANE
    Consumer<C4> L(lds, wid, lane);
    f32x4 acc[C4::MF][C4::NF];
    const int t = mt2 * 64 + wid * 16 + fr;
    u32x2 hv[5];
    L.run(acc, [] {});
#pragma unroll
    for (int n = 0; n < 5; ++n) hv[n] = *(const LAS u32x2*)(lds + LDS_H + ((wid * 5 + n) * 64 + lane) * 8);
    f32x4 red[1][5];
    L.reduce_k(acc, red);
#pragma unroll
    for (int n = 0; n < 5; ++n) {
      u32x2 w;
      w[0] = pk2(red[0][n][0] + __uint_as_float(hv[n][0] << 16), red[0][n][1] + __uint_as_float(hv[n][0] & 0xffff0000u));
      w[1] = pk2(red[0][n][2] + __uint_as_float(hv[n][1] << 16), red[0][n][3] + __uint_as_float(hv[n][1] & 0xffff0000u));
      *(u32x2*)(p.out + (size_t)t * D_ + j2 * 80 + n * 16 + fq * 4) = w;
    }
    if (wid == 0) { wait_vm<0>(); STAMP(4, 3); }
  }
}

extern "C" void kernel_launch(void* const* d_in, const int* in_sizes, int n_in, void* d_out, int out_size,
                              void* d_ws, size_t ws_size, hipStream_t stream) {
  char* ws = (char*)d_ws;
  Params p;
  p.x = (const u16*)d_in[0]; p.sre = (const u16*)d_in[1]; p.sim = (const u16*)d_in[2];
  p.hre = (const u16*)d_in[3]; p.him = (const u16*)d_in[4];
  p.wg = (const u16*)d_in[6]; p.wu = (const u16*)d_in[7]; p.wd = (const u16*)d_in[8];
  p.flags = (u64*)ws;
  p.cs = (float*)(ws + 262144);
  p.stats = (f32x2*)(ws + 524288);
  p.xnI = (u16*)(ws + (1u << 20));
  p.pqI = (u16*)(ws + (4u << 20));
  p.hI = (u16*)(ws + (10u << 20));
  p.hhI = (u16*)(ws + (13u << 20));
  p.wdI = (u16*)(ws + (21u << 20));
  p.out = (u16*)d_out;
  mega<<<256, NWAVES * 64, 0, stream>>>(p);
}
```

```cpp
#include <hip/hip_runtime.h>
#include <hip/hip_bf16.h>

constexpr int PSLEEP = 4;
constexpr int ISLEEP = 16;
constexpr int PDELAY = 40;
constexpr int NRAMP = 3;
constexpr int AUXH = 16;

typedef unsigned short u16;
typedef unsigned long long u64;
typedef short bf16x8 __attribute__((ext_vector_type(8)));
typedef short bf16x4 __attribute__((ext_vector_type(4)));
typedef float f32x4 __attribute__((ext_vector_type(4)));
typedef float f32x2 __attribute__((ext_vector_type(2)));
typedef unsigned u32x2 __attribute__((ext_vector_type(2)));
typedef unsigned u32x4 __attribute__((ext_vector_type(4)));
#define LAS __attribute__((address_space(3)))
#define RLX_AGENT __ATOMIC_RELAXED, __HIP_MEMORY_SCOPE_AGENT
#define RLX_WG __ATOMIC_RELAXED, __HIP_MEMORY_SCOPE_WORKGROUP

constexpr int S_ = 2048, D_ = 640, F_ = 1728;

__device__ __forceinline__ float bf2f(u16 v) { return __uint_as_float(((unsigned)v) << 16); }
typedef __bf16 bf16x2n __attribute__((ext_vector_type(2)));
__device__ __forceinline__ unsigned pk2(float lo, float hi) {
  return __builtin_bit_cast(unsigned, __builtin_convertvector((f32x2){lo, hi}, bf16x2n));
}
__device__ __forceinline__ float rbf(float v) { return __uint_as_float(pk2(v, v) << 16); }

template <int N> __device__ __forceinline__ void wait_vm() { asm volatile("s_waitcnt vmcnt(%0)" ::"n"(N) : "memory"); }
__device__ __forceinline__ void wait_lgkm0() { asm volatile("" ::: "memory"); __builtin_amdgcn_s_waitcnt(0xC07F); asm volatile("" ::: "memory"); }
__device__ __forceinline__ void bar() { asm volatile("" ::: "memory"); __builtin_amdgcn_s_barrier(); asm volatile("" ::: "memory"); }
template <int AUX, int OFF> __device__ __forceinline__ void dma16(const char* sbase, unsigned voff) {
  if constexpr (AUX == 16) asm volatile("global_load_lds_dwordx4 %0, %1 offset:%2 sc1" :: "v"(voff), "s"(sbase), "n"(OFF) : "memory");
  else asm volatile("global_load_lds_dwordx4 %0, %1 offset:%2" :: "v"(voff), "s"(sbase), "n"(OFF) : "memory");
}
__device__ __forceinline__ void set_m0(unsigned v) { asm volatile("s_mov_b32 m0, %0\n\ts_nop 0" :: "s"(v) : "memory"); }
__device__ __forceinline__ void st8(void* p, u32x2 v) { __hip_atomic_store((u64*)p, __builtin_bit_cast(u64, v), RLX_AGENT); }
__device__ __forceinline__ u32x2 ld8(const void* p) { return __builtin_bit_cast(u32x2, __hip_atomic_load((u64*)p, RLX_AGENT)); }

constexpr int NL = 2;
constexpr int NWAVES = 8;
constexpr int LDS_H = 120 * 1024;
constexpr int LDS_RING = 136 * 1024;
constexpr int LDS_X = LDS_RING;
constexpr int LDS_TOTAL = 160 * 1024;
constexpr int LDS_MISC = LDS_TOTAL - 256;

template <int KID_, int BM_, int BN_, int BK_, int WM_, int WN_, int WK_, int NSA_, int NSB_, int NK_, int AMODE_, int BMODE_, int SWAP_, int NPRE_>
struct Cfg {
  static constexpr int KID = KID_, BM = BM_, BN = BN_, BK = BK_, WM = WM_, WN = WN_, WK = WK_, NSA = NSA_, NSB = NSB_, NK = NK_;
  static constexpr int AMODE = AMODE_, BMODE = BMODE_, SWAP = SWAP_, NPRE = NPRE_;
  static constexpr int NC = WM * WN * WK;
  static constexpr int MF = BM / WM / 16, NF = BN / WN / 16, KSW = BK / 32 / WK;
  static constexpr int ABYTES = BM * BK * 2, BBYTES = BN * BK * 2;
  static constexpr int PA = ABYTES / 1024, PB = BBYTES / 1024;
  static constexpr int GA = PA / NL, GB = PB / NL, G = GA + GB;
  static constexpr int RA = NSA * ABYTES, RB = NSB * BBYTES;
  static_assert(PA % NL == 0 && PB % NL == 0, "pieces");
  static_assert(RA + RB <= (KID == 1 ? LDS_RING : LDS_H), "lds");
  static_assert(NSA <= NK && NSB <= NK, "stages");
  static_assert(KSW >= 1 && KSW <= 2, "ksw");
};

template <class C> struct Frags { bf16x8 a[C::KSW][C::MF]; bf16x8 b[C::KSW][C::NF]; };

template <class C, class Src, bool ISA>
struct Loader {
  static constexpr int G = ISA ? C::GA : C::GB, NS = ISA ? C::NSA : C::NSB, BYTES = ISA ? C::ABYTES : C::BBYTES;
  static constexpr int RING = NS * BYTES, MODE = ISA ? C::AMODE : C::BMODE, AUX = ISA ? Src::AUXA : Src::AUXB;
  const Src& src;
  int lw;
  unsigned lbase, cur;
  unsigned voff[G];
  __device__ __forceinline__ Loader(LAS char* lds, const Src& s, int lw_, int lane) : src(s), lw(lw_) {
    lbase = (unsigned)(size_t)lds + (ISA ? 0 : C::RA) + lw * (G * 1024);
#pragma unroll
    for (int i = 0; i < G; ++i) {
      const int p = (lw * G + i) * 64 + lane;
      unsigned v;
      if constexpr (MODE == 0) v = p * 16;
      else if constexpr (MODE == 1) { const int row = p >> 3, c = (p & 7) ^ ((row >> 1) & 7); v = row * (ISA ? Src::ldA : Src::ldB) + c * 16; }
      else { const int q = p % (C::BK * 8), k = q >> 3, c = (q & 7) ^ ((((k >> 3) & 1) << 2) | (((k >> 1) & 1) << 1)); v = k * Src::ldB + c * 16; }
      voff[i] = v - (i & 3) * 1024;
    }
  }
  template <int I> __device__ __forceinline__ void pieces(const char* gb, unsigned l) {
    if constexpr (I < G) {
      if constexpr ((I & 3) == 0) set_m0(l + I * 1024);
      dma16<AUX, (I & 3) * 1024>(gb, voff[I]);
      pieces<I + 1>(gb, l);
    }
  }
  __device__ __forceinline__ void issue(int k, unsigned buf) {
    pieces<0>(ISA ? src.baseA(k) : src.baseB(k, lw), lbase + buf);
  }
  __device__ __forceinline__ void fill(int s0, int s1) {
#pragma unroll 1
    for (int s = s0; s < s1; ++s) issue(s, s * BYTES);
  }
  template <int K, class Hook> __device__ __forceinline__ void tail(Hook hook) {
    if constexpr (K < C::NK - 1) {
      wait_vm<(C::NK - 2 - K) * G + Hook::extra(K)>();
      bar();
      hook.template at<K>();
      tail<K + 1>(hook);
    }
  }
  template <int N0, class Hook> __device__ __forceinline__ void run(Hook hook) {
    static_assert(N0 >= 1 && N0 <= NS && (NS - 1) * G <= 63, "ring");
    wait_vm<(N0 - 1) * G>();
    bar();
    fill(N0, NS);
    cur = 0;
#pragma unroll 1
    for (int k = 0; k < C::NK - NS; ++k) {
      wait_vm<(NS - 2) * G>();
      bar();
      issue(k + NS, cur);
      cur += BYTES; if (cur == RING) cur = 0;
    }
    hook.template at<C::NK - NS - 1>();
    tail<C::NK - NS>(hook);
  }
};
struct NoHook {
  static constexpr int extra(int) { return 0; }
  template <int K> __device__ __forceinline__ void at() {}
};

template <class C>
struct Consumer {
  LAS char* lds;
  int wid, lane, wm, wn, wk;
  unsigned rdA[2], rdB[4];

  __device__ __forceinline__ Consumer(LAS char* lds_, int wid_, int lane_) : lds(lds_), wid(wid_), lane(lane_) {
    wk = wid % C::WK; wn = (wid / C::WK) % C::WN; wm = wid / (C::WK * C::WN);
    const int fr = lane & 15, fq = lane >> 4;
    const int arow = wm * (C::BM / C::WM) + fr;
    const int ss0 = wk * C::KSW;
#pragma unroll
    for (int s = 0; s < 2; ++s) {
      const int ss = ss0 + s;
      if constexpr (C::AMODE == 0) rdA[s] = ((ss0 * 4 + fq) * C::BM + arow) * 16;
      else rdA[s] = arow * 128 + ((((ss & 1) * 4 + fq) ^ ((fr >> 1) & 7)) * 16);
    }
    if constexpr (C::BMODE <= 1) {
      const int brow = wn * (C::BN / C::WN) + fr;
#pragma unroll
      for (int s = 0; s < 2; ++s) {
        const int ss = ss0 + s;
        if constexpr (C::BMODE == 0) rdB[s] = C::RA + ((ss0 * 4 + fq) * C::BN + brow) * 16;
        else rdB[s] = C::RA + brow * 128 + ((((ss & 1) * 4 + fq) ^ ((fr >> 1) & 7)) * 16);
      }
    } else {
      const int g = lane >> 4, q = (lane >> 2) & 3, p = lane & 3;
#pragma unroll
      for (int j = 0; j < 4; ++j) {
        const int jj = 2 * wn + (j & 1), mat = j >> 1;
        rdB[j] = C::RA + mat * (C::BK * 128) + (32 * ss0 + 8 * g + q) * 128 + ((((jj ^ (((g & 1) << 1) | (q >> 1))) << 1) | (p >> 1)) * 16) + 8 * (p & 1);
      }
    }
  }

  __device__ __forceinline__ void read(Frags<C>& f, unsigned bufa, unsigned bufb) {
    LAS char* a = lds + bufa; LAS char* b = lds + bufb;
#pragma unroll
    for (int s = 0; s < C::KSW; ++s) {
      if constexpr (C::BMODE == 0) {
#pragma unroll
        for (int n = 0; n < C::NF; ++n) f.b[s][n] = *(const LAS bf16x8*)(b + rdB[0] + s * (C::BN * 64) + n * 256);
      } else if constexpr (C::BMODE == 1) {
#pragma unroll
        for (int n = 0; n < C::NF; ++n) f.b[s][n] = *(const LAS bf16x8*)(b + rdB[s] + n * 2048);
      } else {
#pragma unroll
        for (int n = 0; n < C::NF; ++n) {
          bf16x4 lo = __builtin_amdgcn_ds_read_tr16_b64_v4i16((LAS bf16x4*)(b + rdB[n] + s * 4096));
          bf16x4 hi = __builtin_amdgcn_ds_read_tr16_b64_v4i16((LAS bf16x4*)(b + rdB[n] + s * 4096 + 512));
          f.b[s][n] = __builtin_shufflevector(lo, hi, 0, 1, 2, 3, 4, 5, 6, 7);
        }
      }
      if constexpr (C::AMODE == 0) {
#pragma unroll
        for (int m = 0; m < C::MF; ++m) f.a[s][m] = *(const LAS bf16x8*)(a + rdA[0] + s * (C::BM * 64) + m * 256);
      } else {
#pragma unroll
        for (int m = 0; m < C::MF; ++m) f.a[s][m] = *(const LAS bf16x8*)(a + rdA[s] + m * 2048);
      }
    }
  }

  __device__ __forceinline__ void mma(f32x4 (&acc)[C::MF][C::NF], const Frags<C>& f) {
#pragma unroll
    for (int s = 0; s < C::KSW; ++s)
#pragma unroll
      for (int m = 0; m < C::MF; ++m)
#pragma unroll
        for (int n = 0; n < C::NF; ++n)
          acc[m][n] = C::SWAP ? __builtin_amdgcn_mfma_f32_16x16x32_bf16(f.b[s][n], f.a[s][m], acc[m][n], 0, 0, 0)
                              : __builtin_amdgcn_mfma_f32_16x16x32_bf16(f.a[s][m], f.b[s][n], acc[m][n], 0, 0, 0);
  }
  static constexpr int NRD = C::KSW * (C::MF + (C::BMODE >= 2 ? 2 : 1) * C::NF);
  static constexpr int NMM = C::KSW * C::MF * C::NF;
  __device__ __forceinline__ void interleave() {
    constexpr int RPM = (NRD + NMM - 3) / (NMM - 2);
#pragma unroll
    for (int i = 0; i < NMM; ++i) {
      __builtin_amdgcn_sched_group_barrier(0x100, RPM, 0);
      __builtin_amdgcn_sched_group_barrier(0x008, 1, 0);
    }
  }
  unsigned na, nb;
  __device__ __forceinline__ void step(f32x4 (&acc)[C::MF][C::NF], Frags<C>& cur, Frags<C>& nxt) {
    na += C::ABYTES; if (na == C::RA) na = 0;
    nb += C::BBYTES; if (nb == C::RB) nb = 0;
    wait_lgkm0(); bar();
    __builtin_amdgcn_sched_barrier(0);
    read(nxt, na, nb);
    mma(acc, cur);
    interleave();
    __builtin_amdgcn_sched_barrier(0);
  }

  template <class Hook> __device__ __forceinline__ void run(f32x4 (&acc)[C::MF][C::NF], Hook hook) {
    Frags<C> f0, f1;
#pragma unroll
    for (int m = 0; m < C::MF; ++m)
#pragma unroll
      for (int n = 0; n < C::NF; ++n) { acc[m][n] = (f32x4){0.f, 0.f, 0.f, 0.f}; asm volatile("" : "+v"(acc[m][n])); }
    bar();
    read(f0, 0, 0);
    na = 0; nb = 0;
#pragma unroll 1
    for (int k = 0; k + 2 < C::NK; k += 2) {
      step(acc, f0, f1);
      step(acc, f1, f0);
    }
    if constexpr (C::NK % 2 == 0) {
      step(acc, f0, f1);
      wait_lgkm0(); bar();
      hook();
      mma(acc, f1);
    } else {
      wait_lgkm0(); bar();
      mma(acc, f0);
    }
  }

  static constexpr bool SPLM = (C::MF % 2 == 0);
  static constexpr int RM = SPLM ? C::MF / 2 : C::MF, RN = SPLM ? C::NF : C::NF / 2;
  __device__ __forceinline__ void reduce_k(f32x4 (&acc)[C::MF][C::NF], f32x4 (&red)[RM][RN]) {
    static_assert(C::WK == 2, "");
    static_assert(LDS_X + C::NC * RM * RN * 1024 <= LDS_MISC, "");
    LAS f32x4* x = (LAS f32x4*)(lds + LDS_X);
#pragma unroll
    for (int m = 0; m < RM; ++m)
#pragma unroll
      for (int n = 0; n < RN; ++n) {
        const f32x4 lo = acc[m][n], hi = SPLM ? acc[m + RM][n] : acc[m][n + RN];
        x[(wid * RM * RN + m * RN + n) * 64 + lane] = wk ? lo : hi;
        red[m][n] = wk ? hi : lo;
      }
    wait_lgkm0(); bar();
#pragma unroll
    for (int m = 0; m < RM; ++m)
#pragma unroll
      for (int n = 0; n < RN; ++n) red[m][n] += x[((wid ^ 1) * RM * RN + m * RN + n) * 64 + lane];
  }
};

constexpr int FLE = 0;
constexpr int FL0 = 32;
constexpr int FL1 = FL0 + 2048;
constexpr int FL2 = FL1 + 16 * 16;
constexpr int FL3 = FL2 + 8 * 32;
constexpr int FLW = FL3 + 8 * 32;
constexpr int FLC = FLW + 48;

__device__ __forceinline__ void publish(LAS unsigned* arr, unsigned nwaves, u64* flag, u64 tag, int lane) {
  wait_vm<0>();
  if (lane == 0) {
    const unsigned old = __hip_atomic_fetch_add(arr, 1u, RLX_WG);
    if (old == nwaves - 1) __hip_atomic_store(flag, tag, RLX_AGENT);
  }
}
__device__ __forceinline__ void lds_wait(LAS unsigned* w, unsigned want) {
  const unsigned a = (unsigned)(size_t)w;
#pragma unroll 1
  for (unsigned i = 0; i < (1u << 24); ++i) {
    unsigned v;
    asm volatile("ds_read_b32 %0, %1\n\ts_waitcnt lgkmcnt(0)" : "=v"(v) : "v"(a) : "memory");
    if (__builtin_amdgcn_readfirstlane(v) >= want) break;
    __builtin_amdgcn_s_sleep(1);
  }
}
template <int SLEEP, int NW, int DELAY = PDELAY>
__device__ __forceinline__ void await(const u64* word, bool need, u64 tag, int lw, LAS unsigned* go, unsigned epoch) {
  if (lw == 0) {
    const u64 t0 = wall_clock64();
    if (DELAY) __builtin_amdgcn_s_sleep(DELAY);
    for (;;) {
      bool ok = true;
#pragma unroll
      for (int i = 0; i < NW; ++i) ok &= __hip_atomic_load(word + i * 64, RLX_AGENT) == tag;
      if (__all(!need || ok)) break;
      if (wall_clock64() - t0 > 2000000ull) break;
      if (SLEEP > PSLEEP && __any(need && ok)) __builtin_amdgcn_s_sleep(PSLEEP); else __builtin_amdgcn_s_sleep(SLEEP);
    }
    __hip_atomic_store(go, epoch, RLX_WG);
  } else {
    lds_wait(go, epoch);
  }
  __builtin_amdgcn_fence(__ATOMIC_ACQUIRE, "wavefront");
}
using C1 = Cfg<1, 80, 128, 64, 1, 4, 1, 4, 6, 32, 0, 1, 0, 2>;
using C2 = Cfg<2, 64, 80, 64, 2, 1, 2, 4, 6, 20, 0, 1, 1, 6>;
using C3 = Cfg<3, 256, 128, 32, 2, 2, 1, 4, 5, 20, 0, 3, 1, 3>;
using C4 = Cfg<4, 64, 80, 64, 2, 1, 2, 4, 6, 27, 0, 0, 1, 6>;
struct Src1 {
  const char* a; const char* bre; const char* bim; int o, c;
  static constexpr int ldA = 0, ldB = S_ * 2, AUXA = AUXH, AUXB = 0;
  __device__ __forceinline__ int kt(int k) const { const int s = (k + o) & 31; return (s & 24) | ((s + c) & 7); }
  __device__ __forceinline__ const char* baseA(int k) const { return a + kt(k) * (80 * 128); }
  __device__ __forceinline__ const char* baseB(int k, int im) const { return (im ? bim : bre) + kt(k) * 128; }
};
struct Src2 {
  const char* a; const char* bre; const char* bim; int rot;
  static constexpr int ldA = 0, ldB = D_ * 2, AUXA = AUXH, AUXB = 0;
  __device__ __forceinline__ int kt(int k) const { const int s = k + rot; return s >= 20 ? s - 20 : s; }
  __device__ __forceinline__ const char* baseA(int k) const { return a + kt(k) * (64 * 128); }
  __device__ __forceinline__ const char* baseB(int k, int) const { const int t = kt(k); return t < 10 ? bre + t * 128 : bim + (t - 10) * 128; }
};
struct Src3 {
  const char* a; const char* bg; const char* bu; int rot;
  static constexpr int ldA = 0, ldB = F_ * 2, AUXA = AUXH, AUXB = 0;
  __device__ __forceinline__ int kt(int k) const { const int s = k + rot; return s >= 20 ? s - 20 : s; }
  __device__ __forceinline__ const char* baseA(int k) const { return a + kt(k) * (256 * 64); }
  __device__ __forceinline__ const char* baseB(int k, int up) const { return (up ? bu : bg) + kt(k) * (32 * F_ * 2); }
};
struct Src4 {
  const char* a; const char* b;
  static constexpr int ldA = 0, ldB = 0, AUXA = AUXH, AUXB = AUXH;
  __device__ __forceinline__ const char* baseA(int k) const { return a + k * (64 * 128); }
  __device__ __forceinline__ const char* baseB(int k, int) const { return b + k * (80 * 128); }
};

struct Params {
  const u16* x; const u16* sre; const u16* sim; const u16* hre; const u16* him; const u16* wg; const u16* wu; const u16* wd;
  u16* xnI; u16* pqI; u16* hI; u16* hhI; u16* wdI; f32x2* stats; float* cs; u64* flags; u16* out;
};

template <int CTRL, int RMASK> __device__ __forceinline__ float dpp_add(float v) {
  return v + __int_as_float(__builtin_amdgcn_update_dpp(0, __float_as_int(v), CTRL, RMASK, 0xf, true));
}
__device__ __forceinline__ float wave_sum(float v) {
  v = dpp_add<0x111, 0xf>(v); v = dpp_add<0x112, 0xf>(v); v = dpp_add<0x114, 0xf>(v); v = dpp_add<0x118, 0xf>(v);
  v = dpp_add<0x142, 0xa>(v); v = dpp_add<0x143, 0xc>(v);
  return __int_as_float(__builtin_amdgcn_readlane(__float_as_int(v), 63));
}
__device__ __forceinline__ float sum_rows(float v) {
  const auto a = __builtin_amdgcn_permlane16_swap(__float_as_uint(v), __float_as_uint(v), false, false);
  v = __uint_as_float(a[0]) + __uint_as_float(a[1]);
  const auto b = __builtin_amdgcn_permlane32_swap(__float_as_uint(v), __float_as_uint(v), false, false);
  return __uint_as_float(b[0]) + __uint_as_float(b[1]);
}
__device__ __forceinline__ u32x4 col8(LAS char* tile, int cb, int lane) {
  LAS char* a = tile + ((lane >> 2) & 3) * 1296 + (cb * 64 + (lane >> 4) * 16 + (lane & 3) * 4) * 2;
  const bf16x4 lo = __builtin_amdgcn_ds_read_tr16_b64_v4i16((LAS bf16x4*)a);
  const bf16x4 hi = __builtin_amdgcn_ds_read_tr16_b64_v4i16((LAS bf16x4*)(a + 4 * 1296));
  return __builtin_bit_cast(u32x4, __builtin_shufflevector(lo, hi, 0, 1, 2, 3, 4, 5, 6, 7));
}
struct Hook3 {
  const Params& p; LAS f32x4* rs; LAS float* csl; LAS unsigned* arr; u64 tag; int mt3, j3, lw, lane;
  f32x2 (&pv)[2][8]; u64& fc; float& cv;
  static constexpr int K0 = C3::NK - C3::NSB - 1;
  static constexpr int extra(int k) { return k <= K0 + 2 ? 17 : 1; }
  template <int K> __device__ __forceinline__ void at() {
    if constexpr (K == K0) {
      fc = __hip_atomic_load(p.flags + FLC + j3 * 16, RLX_AGENT);
#pragma unroll
      for (int h = 0; h < 2; ++h)
#pragma unroll
        for (int i = 0; i < 8; ++i) pv[h][i] = __builtin_bit_cast(f32x2, ld8(p.stats + i * S_ + mt3 * 256 + lw * 128 + h * 64 + lane));
    } else if constexpr (K == K0 + 2) {
      if (!__all(fc == tag)) await<PSLEEP, 1>(p.flags + FLC + j3 * 16, true, tag, 0, arr + 11, 1u);
      __builtin_amdgcn_fence(__ATOMIC_ACQUIRE, "wavefront");
      cv = __hip_atomic_load(p.cs + j3 * 128 + lw * 64 + lane, RLX_AGENT);
#pragma unroll
      for (int h = 0; h < 2; ++h) {
        float mean = 0.f, q = 0.f, dd = 0.f;
#pragma unroll
        for (int i = 0; i < 8; ++i) { mean += pv[h][i][0]; q += pv[h][i][1]; }
        mean *= 0.125f;
#pragma unroll
        for (int i = 0; i < 8; ++i) { const float d = pv[h][i][0] - mean; dd += d * d; }
        q += 80.0f * dd;
        const float den = rbf(__builtin_amdgcn_sqrtf(rbf(rbf(q * (1.0f / D_)) + 1e-5f)));
        rs[lw * 128 + h * 64 + lane] = (f32x4){-rbf(mean), den, -1.44269504089f * __builtin_amdgcn_rcpf(den), 0.f};
      }
    } else if constexpr (K == C3::NK - 2) {
      csl[lw * 64 + lane] = cv;
      wait_lgkm0();
    }
  }
};

__global__ __launch_bounds__(NWAVES * 64) void mega(const Params p) {
  __shared__ __attribute__((aligned(1024))) char smem_[LDS_TOTAL];
  LAS char* lds = (LAS char*)smem_;
  const int wid = __builtin_amdgcn_readfirstlane(threadIdx.x >> 6), lane = threadIdx.x & 63;
  const int bid = blockIdx.x, g = bid & 7, idx = bid >> 3;
  LAS unsigned* arr = (LAS unsigned*)(lds + LDS_MISC);
  const u16* xr = p.x + (bid * 8 + wid) * D_;
  const bf16x8 v0 = *(const bf16x8*)(xr + lane * 8);
  const bf16x8 v1 = *(const bf16x8*)(xr + 512 + (lane & 15) * 8);
  const u64 ep = __hip_atomic_load(p.flags + ((wid == 7 && lane == 63) ? bid * 16 : FLE), RLX_AGENT);
  if (threadIdx.x < 16) arr[threadIdx.x] = 0u;
  const int j2 = idx & 7, mq = idx >> 3, mt2 = g * 4 + mq;
  const int mt3 = (g & 6) + (idx & 1), j3 = (g & 1) * 14 + (idx >> 1);
  const bool on3 = idx < 28 && j3 < 27;
  LAS f32x4* rs = (LAS f32x4*)(lds + LDS_X);
  LAS float* csl = (LAS float*)(lds + LDS_X + 4096);

  const Src1 s1{(const char*)(p.xnI + (size_t)j2 * 256 * 80 * 8), (const char*)(p.sre + (size_t)mt2 * 64 * S_),
                (const char*)(p.sim + ((ptrdiff_t)mt2 - 1) * 64 * S_), (g + 2 * mq) & 7, j2};
  {
    float f[16];
#pragma unroll
    for (int i = 0; i < 8; ++i) { f[i] = bf2f((u16)v0[i]); f[8 + i] = bf2f((u16)v1[i]); }
    const float piv = __int_as_float(__builtin_amdgcn_readfirstlane(__float_as_int(f[0])));
    float s0 = 0.f, s1 = 0.f, q0 = 0.f, q1 = 0.f;
#pragma unroll
    for (int i = 0; i < 8; ++i) { const float d = f[i] - piv, e = f[8 + i] - piv; s0 += d; q0 += d * d; s1 += e; q1 += e * e; }
    const float sd = wave_sum(s0 + (lane < 16 ? s1 : 0.f)) * (1.0f / D_);
    const float qd = wave_sum(q0 + (lane < 16 ? q1 : 0.f)) * (1.0f / D_);
    const float mb = rbf(piv + sd), vb = rbf(fmaxf(qd - sd * sd, 0.f));
    const float rden = __builtin_amdgcn_rcpf(rbf(__builtin_amdgcn_sqrtf(rbf(vb + 1e-5f))));
    u32x4 w0, w1;
#pragma unroll
    for (int i = 0; i < 4; ++i) {
      w0[i] = pk2(rbf(f[2 * i] - mb) * rden, rbf(f[2 * i + 1] - mb) * rden);
      w1[i] = pk2(rbf(f[8 + 2 * i] - mb) * rden, rbf(f[9 + 2 * i] - mb) * rden);
    }
    LAS char* tb = lds + LDS_X;
    *(LAS u32x4*)(tb + wid * 1296 + lane * 16) = w0;
    if (lane < 16) *(LAS u32x4*)(tb + wid * 1296 + 1024 + lane * 16) = w1;
    wait_lgkm0(); bar();
  }
  const u64 tag = (((u64)(unsigned)__builtin_amdgcn_readfirstlane((unsigned)(ep >> 32)) << 32) | (unsigned)__builtin_amdgcn_readfirstlane((unsigned)ep)) + 1ull;
  {
    LAS char* tb = lds + LDS_X;
    auto rsrc = __builtin_amdgcn_make_buffer_rsrc(p.xnI, 0, S_ * D_ * 2, 0x00020000);
#pragma unroll
    for (int h = 0; h < 2; ++h) {
      const int cb = h * 8 + wid;
      if (cb < 10) {
        const int d = cb * 64 + lane, j = d / 80, n = d % 80;
        __builtin_amdgcn_raw_buffer_store_b128(col8(tb, cb, lane), rsrc, ((j * 256 + bid) * 80 + n) * 16, 0, AUXH);
      }
    }
    wait_vm<0>();
    unsigned old = 0;
    if (lane == 0) old = __hip_atomic_fetch_add(arr + 5, 1u, RLX_WG);
    if (__builtin_amdgcn_readfirstlane(old) == 7u && lane < 8) __hip_atomic_store(p.flags + FL0 + lane * 256 + bid, tag, RLX_AGENT);
  }
  const Src2 s2{(const char*)(p.pqI + (size_t)mt2 * 160 * 64 * 8), (const char*)(p.hre + (size_t)j2 * 80 * D_), (const char*)(p.him + (size_t)j2 * 80 * D_), (mt2 * 5) >> 3};
  const Src3 s3{(const char*)(p.hI + (size_t)mt3 * 80 * 256 * 8), (const char*)(p.wg + j3 * 64), (const char*)(p.wu + j3 * 64), (mt3 * 5) >> 1};
  const Src4 s4{(const char*)(p.hhI + (size_t)mt2 * 216 * 64 * 8), (const char*)(p.wdI + (size_t)j2 * 216 * 80 * 8)};
  if (wid >= 6) {
    const int lw = wid - 6;
    {
      Loader<C1, Src1, true> L(lds, s1, lw, lane);
      lds_wait(arr + 5, 8);
      await<PSLEEP, 4>(p.flags + FL0 + g * 256 + lane, true, tag, lw, arr + 3, 1u);
      if (bid == 0 && lw == 0 && lane == 0) __hip_atomic_store(p.flags + FLE, tag, RLX_AGENT);
      wait_vm<0>();
      L.fill(0, NRAMP); L.run<NRAMP>(NoHook{});
      bar();
    }
    {
      Loader<C2, Src2, true> L(lds, s2, lw, lane);
      lds_wait(arr + 0, C1::NC);
      await<PSLEEP, 1>(p.flags + FL1 + mt2 * 8 + (lane & 7), true, tag, lw, arr + 3, 2u);
      wait_vm<0>();
      L.fill(0, NRAMP); L.run<NRAMP>(NoHook{});
      bar(); bar();
    }
    if (on3) {
      Loader<C3, Src3, true> L(lds, s3, lw, lane);
      lds_wait(arr + 1, C2::NC);
      await<PSLEEP, 1, 28>(p.flags + FL2 + (mt3 * 32 + (lane & 31)), true, tag, lw, arr + 3, 3u);
      wait_vm<0>();
      L.fill(0, NRAMP); L.run<NRAMP>(NoHook{});
      bar();
    }
    {
      Loader<C4, Src4, true> L(lds, s4, lw, lane);
      lds_wait(arr + 2, 4);
      if (on3) await<PSLEEP, 1>(p.flags + FL3 + (g * 32 + (lane & 31)), (lane & 31) < 27, tag, lw, arr + 3, 4u);
      else await<ISLEEP, 1>(p.flags + FL3 + (g * 32 + (lane & 31)), (lane & 31) < 27, tag, lw, arr + 3, 4u);
      wait_vm<0>();
      L.fill(0, NRAMP); L.run<NRAMP>(NoHook{});
      bar(); bar();
    }
    return;
  }
  if (wid >= 4) {
    const int lw = wid - 4;
    {
      Loader<C1, Src1, false> L(lds, s1, lw, lane);
      lds_wait(arr + 5, 8); L.fill(0, C1::NPRE);
      L.run<C1::NPRE>(NoHook{});
      bar();
    }
    {
      Loader<C2, Src2, false> L(lds, s2, lw, lane);
      lds_wait(arr + 0, C1::NC); L.fill(0, C2::NPRE);
      L.run<C2::NPRE>(NoHook{});
      bar(); bar();
    }
    if (on3) {
      Loader<C3, Src3, false> L(lds, s3, lw, lane);
      static_assert(Hook3::K0 + 2 < C3::NK - 2, "");
      f32x2 pv[2][8]; u64 fc; float cv;
      lds_wait(arr + 1, C2::NC); L.fill(0, C3::NPRE);
      L.run<C3::NPRE>(Hook3{p, rs, csl, arr, tag, mt3, j3, lw, lane, pv, fc, cv});
      bar();
    }
    {
      Loader<C4, Src4, false> L(lds, s4, lw, lane);
      await<ISLEEP, 1, 0>(p.flags + FLW + min(lane, 39), true, tag, lw, arr + 13, 1u);
      wait_vm<0>();
      lds_wait(arr + 2, 4); L.fill(0, C4::NPRE);
      L.run<C4::NPRE>(NoHook{});
      bar(); bar();
    }
    return;
  }

#define FRESH_LANE int lane_ = threadIdx.x & 63; asm volatile("" : "+v"(lane_)); const int lane = lane_, fr = lane & 15, fq = lane >> 4; (void)fr; (void)fq;
  {
    FRESH_LANE
    Consumer<C1> L(lds, wid, lane);
    f32x4 acc[C1::MF][C1::NF];
    L.run(acc, [] {});
    const int isQ = wid >> 1;
    const float sg = isQ ? -1.f : 1.f;
#pragma unroll
    for (int n = 0; n < 2; ++n) {
      const int r = (wid & 1) * 32 + n * 16 + fr;
#pragma unroll
      for (int m = 0; m < 5; ++m) {
        const int koct = isQ * 80 + j2 * 10 + m * 2 + (fq >> 1);
        u32x2 w; w[0] = pk2(sg * acc[m][n][0], sg * acc[m][n][1]); w[1] = pk2(sg * acc[m][n][2], sg * acc[m][n][3]);
        st8(p.pqI + (((size_t)mt2 * 160 + koct) * 64 + r) * 8 + (fq & 1) * 4, w);
      }
    }
    publish(arr + 0, C1::NC, p.flags + FL1 + mt2 * 8 + j2, tag, lane);
  }
  {
    FRESH_LANE
    Consumer<C2> L(lds, wid, lane);
    f32x4 acc[C2::MF][C2::NF];
    const int t = mt2 * 64 + wid * 16 + fr;
    bf16x4 xv[C2::NF];
#pragma unroll
    for (int n = 0; n < C2::NF; ++n) xv[n] = *(const bf16x4*)(p.x + (size_t)t * D_ + j2 * 80 + n * 16 + fq * 4);
#pragma unroll
    for (int n = 0; n < C2::NF; ++n) asm volatile("" : "+v"(xv[n]));
    L.run(acc, [] {});
    f32x4 red[1][5];
    L.reduce_k(acc, red);
    float hv[C2::NF][4];
    float s = 0.f;
#pragma unroll
    for (int n = 0; n < C2::NF; ++n) {
      u32x2 w;
      w[0] = pk2(red[0][n][0] + bf2f((u16)xv[n][0]), red[0][n][1] + bf2f((u16)xv[n][1]));
      w[1] = pk2(red[0][n][2] + bf2f((u16)xv[n][2]), red[0][n][3] + bf2f((u16)xv[n][3]));
      hv[n][0] = __uint_as_float(w[0] << 16); hv[n][1] = __uint_as_float(w[0] & 0xffff0000u);
      hv[n][2] = __uint_as_float(w[1] << 16); hv[n][3] = __uint_as_float(w[1] & 0xffff0000u);
      s += (hv[n][0] + hv[n][1]) + (hv[n][2] + hv[n][3]);
      const int koct = j2 * 10 + n * 2 + (fq >> 1);
      st8(p.hI + (((size_t)(t >> 8) * 80 + koct) * 256 + (t & 255)) * 8 + (fq & 1) * 4, w);
      *(LAS u32x2*)(lds + LDS_H + ((wid * 5 + n) * 64 + lane) * 8) = w;
    }
    const float mean = sum_rows(s) * (1.0f / 80.0f);
    float q = 0.f;
#pragma unroll
    for (int n = 0; n < C2::NF; ++n)
#pragma unroll
      for (int r = 0; r < 4; ++r) { const float d = hv[n][r] - mean; q += d * d; }
    q = sum_rows(q);
    if (fq == 0) st8(p.stats + j2 * S_ + t, __builtin_bit_cast(u32x2, (f32x2){mean, q}));
    publish(arr + 1, C2::NC, p.flags + FL2 + (g * 32 + idx), tag, lane);
  }
  if (on3) {
    FRESH_LANE
    Consumer<C3> L(lds, wid, lane);
    f32x4 acc[C3::MF][C3::NF];
    f32x4 st[C3::MF], cg[2], cu[2];
    L.run(acc, [&] {
#pragma unroll
      for (int m = 0; m < C3::MF; ++m) st[m] = rs[L.wm * 128 + m * 16 + fr];
#pragma unroll
      for (int i = 0; i < 2; ++i) {
        const int fl = L.wn * 32 + i * 16 + fq * 4;
        cg[i] = *(const LAS f32x4*)(csl + fl); cu[i] = *(const LAS f32x4*)(csl + 64 + fl);
      }
    });
    u16* hb = p.hhI + (((size_t)(mt3 * 4 + L.wm * 2) * 216 + j3 * 8 + L.wn * 4 + (fq >> 1)) * 64 + fr) * 8 + (fq & 1) * 4;
#pragma unroll
    for (int m = 0; m < C3::MF; ++m) {
      u32x2 w[2];
#pragma unroll
      for (int i = 0; i < 2; ++i) {
        const f32x4 s0 = {st[m][0], st[m][0], st[m][0], st[m][0]}, s1 = {st[m][1], st[m][1], st[m][1], st[m][1]};
        const f32x4 x = __builtin_elementwise_fma(cg[i], s0, acc[m][i]) * st[m][2];
        f32x4 e;
#pragma unroll
        for (int r = 0; r < 4; ++r) e[r] = __builtin_amdgcn_exp2f(x[r]);
        const f32x4 d = __builtin_elementwise_fma(e, s1, s1);
        f32x4 rc;
#pragma unroll
        for (int r = 0; r < 4; ++r) rc[r] = __builtin_amdgcn_rcpf(d[r]);
        const f32x4 o = __builtin_elementwise_fma(cu[i], s0, acc[m][i + 2]) * rc;
        w[i][0] = pk2(o[0], o[1]); w[i][1] = pk2(o[2], o[3]);
        st8(hb + (m >> 2) * (216 * 64 * 8) + i * (2 * 64 * 8) + (m & 3) * 128, w[i]);
      }
    }
    publish(arr + 2, C3::NC, p.flags + FL3 + (mt3 * 32 + j3), tag, lane);
  } else {
    FRESH_LANE
    const int id = idx >= 28 ? g * 4 + idx - 28 : 32 + (g >> 1) * 2 + idx - 26;
    auto rsrc = __builtin_amdgcn_make_buffer_rsrc(p.wdI, 0, F_ * D_ * 2, 0x00020000);
    lds_wait(arr + 1, C2::NC);
    if (id < 27) {
      const u16* w = ((wid & 2) ? p.wu : p.wg) + (size_t)((wid & 1) * 320 + (lane >> 3)) * F_ + id * 64 + (lane & 7) * 8;
      f32x2 a[4] = {{0.f, 0.f}, {0.f, 0.f}, {0.f, 0.f}, {0.f, 0.f}};
#pragma unroll 1
      for (int b = 0; b < 2; ++b) {
        u32x4 v[20];
#pragma unroll
        for (int i = 0; i < 20; ++i) v[i] = *(const u32x4*)(w + (size_t)(b * 20 + i) * 8 * F_);
#pragma unroll
        for (int i = 0; i < 20; ++i)
#pragma unroll
          for (int e = 0; e < 4; ++e) a[e] += (f32x2){__uint_as_float(v[i][e] << 16), __uint_as_float(v[i][e] & 0xffff0000u)};
      }
      LAS float* sf = (LAS float*)(lds + LDS_X);
#pragma unroll
      for (int e = 0; e < 4; ++e)
#pragma unroll
        for (int c = 0; c < 2; ++c) { float t = sum_rows(a[e][c]); t += __shfl_xor(t, 8); a[e][c] = t; }
      if (lane < 8) {
        *(LAS f32x4*)(sf + wid * 64 + lane * 8) = (f32x4){a[0][0], a[0][1], a[1][0], a[1][1]};
        *(LAS f32x4*)(sf + wid * 64 + lane * 8 + 4) = (f32x4){a[2][0], a[2][1], a[3][0], a[3][1]};
      }
      wait_lgkm0();
      unsigned old = 0;
      if (lane == 0) old = __hip_atomic_fetch_add(arr + 12, 1u, RLX_WG);
      if (__builtin_amdgcn_readfirstlane(old) == 3u) {
        __hip_atomic_store(p.cs + id * 128 + lane, sf[lane] + sf[64 + lane], RLX_AGENT);
        __hip_atomic_store(p.cs + id * 128 + 64 + lane, sf[128 + lane] + sf[192 + lane], RLX_AGENT);
        wait_vm<0>();
        if (lane == 0) __hip_atomic_store(p.flags + FLC + id * 16, tag, RLX_AGENT);
      }
    }
    const int k0 = id < 27 ? id * 3 : 81 + (id - 27) * 11, nk = id < 27 ? 3 : min(11, 216 - k0);
#pragma unroll 1
    for (int i = 0; i < 2 * nk; ++i) {
      const int row = (i & 1) * 4 + wid;
      const char* wr = (const char*)(p.wd + (size_t)((k0 + (i >> 1)) * 8 + row) * D_);
      set_m0((unsigned)(size_t)lds + (i >> 1) * 10368 + row * 1296);
      dma16<0, 0>(wr, lane * 16);
      if (lane < 16) dma16<0, 1024>(wr, lane * 16);
    }
    wait_vm<0>();
    if (lane == 0) __hip_atomic_fetch_add(arr + 6, 1u, RLX_WG);
    lds_wait(arr + 6, 4);
#pragma unroll 1
    for (int i = 0; i < nk; ++i) {
#pragma unroll
      for (int h = 0; h < 3; ++h) {
        const int cb = h * 4 + wid;
        if (cb < 10) {
          const int d = cb * 64 + lane, j = d / 80, n = d % 80;
          __builtin_amdgcn_raw_buffer_store_b128(col8(lds + i * 10368, cb, lane), rsrc, ((j * 216 + k0 + i) * 80 + n) * 16, 0, AUXH);
        }
      }
    }
    publish(arr + 2, 4, p.flags + FLW + id, tag, lane);
  }
  {
    FRESH_LANE
    Consumer<C4> L(lds, wid, lane);
    f32x4 acc[C4::MF][C4::NF];
    const int t = mt2 * 64 + wid * 16 + fr;
    u32x2 hv[5];
    L.run(acc, [] {});
#pragma unroll
    for (int n = 0; n < 5; ++n) hv[n] = *(const LAS u32x2*)(lds + LDS_H + ((wid * 5 + n) * 64 + lane) * 8);
    f32x4 red[1][5];
    L.reduce_k(acc, red);
#pragma unroll
    for (int n = 0; n < 5; ++n) {
      u32x2 w;
      w[0] = pk2(red[0][n][0] + __uint_as_float(hv[n][0] << 16), red[0][n][1] + __uint_as_float(hv[n][0] & 0xffff0000u));
      w[1] = pk2(red[0][n][2] + __uint_as_float(hv[n][1] << 16), red[0][n][3] + __uint_as_float(hv[n][1] & 0xffff0000u));
      *(u32x2*)(p.out + (size_t)t * D_ + j2 * 80 + n * 16 + fq * 4) = w;
    }
  }
}

extern "C" void kernel_launch(void* const* d_in, const int* in_sizes, int n_in, void* d_out, int out_size,
                              void* d_ws, size_t ws_size, hipStream_t stream) {
  char* ws = (char*)d_ws;
  Params p;
  p.x = (const u16*)d_in[0]; p.sre = (const u16*)d_in[1]; p.sim = (const u16*)d_in[2];
  p.hre = (const u16*)d_in[3]; p.him = (const u16*)d_in[4];
  p.wg = (const u16*)d_in[6]; p.wu = (const u16*)d_in[7]; p.wd = (const u16*)d_in[8];
  p.flags = (u64*)ws;
  p.cs = (float*)(ws + 262144);
  p.stats = (f32x2*)(ws + 524288);
  p.xnI = (u16*)(ws + (1u << 20));
  p.pqI = (u16*)(ws + (4u << 20));
  p.hI = (u16*)(ws + (10u << 20));
  p.hhI = (u16*)(ws + (13u << 20));
  p.wdI = (u16*)(ws + (21u << 20));
  p.out = (u16*)d_out;
  mega<<<256, NWAVES * 64, 0, stream>>>(p);
}
```
